```python
import torch

def _softmax(x, axis=-1):
    return torch.softmax(x, dim=axis)
def _gelu(x):
    return torch.nn.functional.gelu(x, approximate="tanh")
def _silu(x):
    return torch.nn.functional.silu(x)
def _sigmoid(x):
    return torch.sigmoid(x)
def _layernorm(x, axis=-1, eps=1e-5):
    m = x.mean(dim=axis, keepdim=True)
    v = x.var(dim=axis, keepdim=True, unbiased=False)
    return (x - m) / torch.sqrt(v + eps)
def _scatter_add(tgt, idx, src, axis=0):
    return tgt.clone().index_add_(axis, idx, src)
def _segment_sum(src, seg, n):
    out = torch.zeros((n,) + src.shape[1:], dtype=src.dtype)
    return out.index_add_(0, seg, src)
def _pad(x, pw):
    flat = tuple(v for pair in reversed(pw) for v in pair)
    return torch.nn.functional.pad(x, flat)

kernel_name = 'sequential_preLN__ssm_conv_s4d__glu_gated_geglu__prompt'

def setup_inputs():

    _w = (lambda a: (lambda t: t.to(torch.bfloat16) if t.is_floating_point() else t)(torch.as_tensor(a)))
    def _A_setup():
        Bsz, L, D, N = 1, 2048, 640, 192
        return {
            "u": _w((lambda *s: torch.randn(*s, dtype=torch.bfloat16) * 0.05)(Bsz, L, D)),
            "log_A_real": _w((lambda *s: torch.randn(*s, dtype=torch.bfloat16) * 6.4)(D, N) * 0.5),
            "A_imag": _w((lambda *s: torch.randn(*s, dtype=torch.bfloat16) * 0.05)(D, N) * 3.14),
            "C_real": _w((lambda *s: torch.randn(*s, dtype=torch.bfloat16) * 0.05)(D, N) / N ** 0.5),
            "C_imag": _w((lambda *s: torch.randn(*s, dtype=torch.bfloat16) * 0.05)(D, N) / N ** 0.5),
            "log_dt": _w((lambda *s: torch.randn(*s, dtype=torch.bfloat16) * 0.05)(D) * 0.5 - 4.0),
            "D_skip": _w((lambda *s: torch.randn(*s, dtype=torch.bfloat16) * 0.05)(D)),
        }
    _A_in = _A_setup()

    def _B_setup():
        B, S, Dm, Df = 1, 2048, 640, 1536
        return {
            "x": _w((lambda *s: torch.randn(*s, dtype=torch.bfloat16) * 0.05)(B, S, Dm)),
            "w_gate": _w((lambda *s: torch.randn(*s, dtype=torch.bfloat16) * 0.4)(Dm, Df) * (Dm ** -0.5)),
            "w_up": _w((lambda *s: torch.randn(*s, dtype=torch.bfloat16) * 0.4)(Dm, Df) * (Dm ** -0.5)),
            "w_down": _w((lambda *s: torch.randn(*s, dtype=torch.bfloat16) * 0.4)(Df, Dm) * (Df ** -0.5)),
        }
    _B_in = _B_setup()

    return {
        "A_u": _A_in["u"],
        "A_log_A_real": _A_in["log_A_real"],
        "A_A_imag": _A_in["A_imag"],
        "A_C_real": _A_in["C_real"],
        "A_C_imag": _A_in["C_imag"],
        "A_log_dt": _A_in["log_dt"],
        "A_D_skip": _A_in["D_skip"],
        "B_x": _B_in["x"],
        "B_w_gate": _B_in["w_gate"],
        "B_w_up": _B_in["w_up"],
        "B_w_down": _B_in["w_down"],
    }

def reference(A_u, A_log_A_real, A_A_imag, A_C_real, A_C_imag, A_log_dt, A_D_skip, B_x, B_w_gate, B_w_up, B_w_down):
    def _A_ref(u, log_A_real, A_imag, C_real, C_imag, log_dt, D_skip):
        L = u.shape[1]
        dt = torch.exp(log_dt)
        A = -torch.exp(log_A_real) + 1j * A_imag
        C = (C_real + 1j * C_imag) * dt[:, None]
        pos = (lambda x, dt: x.to(dt))(torch.arange(L), log_dt.dtype)
        P = torch.exp((A * dt[:, None])[:, :, None] * pos[None, None, :])
        K = 2.0 * torch.einsum("dn,dnl->dl", C, P).real
        L2 = 2 * L
        u_t = (lambda x, *d: x.permute(*d))(u, 0, 2, 1)
        Kf = (lambda x, *a, **k: torch.fft.rfft(x.float(), *a, **k))(K, L2)
        Uf = (lambda x, *a, **k: torch.fft.rfft(x.float(), *a, **k))(u_t, L2)
        y = (lambda x, *a, **k: torch.fft.irfft(x, *a, **k).to(torch.bfloat16))(Uf * Kf[None, :, :], L2)[:, :, :L]
        y = (lambda x, *d: x.permute(*d))(y, 0, 2, 1)
        return y + u * D_skip[None, None, :]

    def _B_ref(x, w_gate, w_up, w_down):
        g = torch.einsum("bsd,df->bsf", x, w_gate)
        u = torch.einsum("bsd,df->bsf", x, w_up)
        h = _gelu(g) * u
        return torch.einsum("bsf,fd->bsd", h, w_down)

    x = A_u
    h = x + _A_ref(_layernorm(x, axis=-1), A_log_A_real, A_A_imag, A_C_real, A_C_imag, A_log_dt, A_D_skip)
    h2 = h + _B_ref(_layernorm(h, axis=-1), B_w_gate, B_w_up, B_w_down)
    return h2
```

```cpp
#include <hip/hip_runtime.h>
#include <hip/hip_bf16.h>
#include <stdint.h>

typedef unsigned short bf16_t;
constexpr int T = 2048, D = 640, NM = 192, F = 1536;

__device__ __forceinline__ float bf2f(bf16_t v) { return __uint_as_float(((unsigned)v) << 16); }
__device__ __forceinline__ bf16_t f2bf(float f) { unsigned u = __float_as_uint(f); u += 0x7FFF + ((u >> 16) & 1); return (bf16_t)(u >> 16); }
__device__ __forceinline__ float rbf(float f) { return bf2f(f2bf(f)); }

__global__ void k_ln(const bf16_t* x, bf16_t* y) {
    __shared__ float red[256];
    int t = blockIdx.x, tid = threadIdx.x;
    float s = 0.f;
    for (int i = tid; i < D; i += 256) s += bf2f(x[t * D + i]);
    red[tid] = s; __syncthreads();
    for (int o = 128; o > 0; o >>= 1) { if (tid < o) red[tid] += red[tid + o]; __syncthreads(); }
    float mean = red[0] / D; __syncthreads();
    s = 0.f;
    for (int i = tid; i < D; i += 256) { float d = bf2f(x[t * D + i]) - mean; s += d * d; }
    red[tid] = s; __syncthreads();
    for (int o = 128; o > 0; o >>= 1) { if (tid < o) red[tid] += red[tid + o]; __syncthreads(); }
    float var = red[0] / D;
    float m = rbf(mean), v = rbf(var);
    float sd = rbf(sqrtf(rbf(v + 1e-5f)));
    for (int i = tid; i < D; i += 256) { float d = rbf(bf2f(x[t * D + i]) - m); y[t * D + i] = f2bf(d / sd); }
}

__global__ void k_kgen(const bf16_t* logA, const bf16_t* Aim, const bf16_t* Cre, const bf16_t* Cim, const bf16_t* logdt, float* K) {
    int d = blockIdx.y, l = blockIdx.x * 256 + threadIdx.x;
    float dt = rbf(expf(bf2f(logdt[d])));
    float pos = rbf((float)l);
    float acc = 0.f;
    for (int n = 0; n < NM; ++n) {
        float are = -rbf(expf(bf2f(logA[d * NM + n]))) * dt, aim = bf2f(Aim[d * NM + n]) * dt;
        float cre = bf2f(Cre[d * NM + n]) * dt, cim = bf2f(Cim[d * NM + n]) * dt;
        float e = expf(are * pos), sn, cs; sincosf(aim * pos, &sn, &cs);
        acc += cre * e * cs - cim * e * sn;
    }
    K[d * T + l] = 2.f * acc;
}

__global__ void k_conv(const bf16_t* x, const bf16_t* u, const float* K, const bf16_t* Dskip, bf16_t* h) {
    int t = blockIdx.y, d = blockIdx.x * 128 + threadIdx.x;
    float acc = 0.f;
    for (int s = 0; s <= t; ++s) acc += K[d * T + s] * bf2f(u[(t - s) * D + d]);
    float y = rbf(acc);
    float sk = rbf(bf2f(u[t * D + d]) * bf2f(Dskip[d]));
    float z = rbf(y + sk);
    h[t * D + d] = f2bf(bf2f(x[t * D + d]) + z);
}

__global__ void k_gemm1(const bf16_t* a, const bf16_t* wg, const bf16_t* wu, bf16_t* m) {
    int t = blockIdx.y, f = blockIdx.x * 256 + threadIdx.x;
    float g = 0.f, u = 0.f;
    for (int k = 0; k < D; ++k) { float av = bf2f(a[t * D + k]); g += av * bf2f(wg[k * F + f]); u += av * bf2f(wu[k * F + f]); }
    g = rbf(g); u = rbf(u);
    float inner = 0.7978845608028654f * (g + 0.044715f * g * g * g);
    float ge = rbf(0.5f * g * (1.f + tanhf(inner)));
    m[t * F + f] = f2bf(ge * u);
}

__global__ void k_gemm2(const bf16_t* m, const bf16_t* wd, const bf16_t* h, bf16_t* out) {
    int t = blockIdx.y, d = blockIdx.x * 128 + threadIdx.x;
    float o = 0.f;
    for (int k = 0; k < F; ++k) o += bf2f(m[t * F + k]) * bf2f(wd[k * D + d]);
    out[t * D + d] = f2bf(bf2f(h[t * D + d]) + rbf(o));
}

extern "C" void kernel_launch(void* const* d_in, const int* in_sizes, int n_in,
                              void* d_out, int out_size, void* d_ws, size_t ws_size,
                              hipStream_t stream) {
    const bf16_t* x = (const bf16_t*)d_in[0];
    const bf16_t* logA = (const bf16_t*)d_in[1];
    const bf16_t* Aim = (const bf16_t*)d_in[2];
    const bf16_t* Cre = (const bf16_t*)d_in[3];
    const bf16_t* Cim = (const bf16_t*)d_in[4];
    const bf16_t* logdt = (const bf16_t*)d_in[5];
    const bf16_t* Dskip = (const bf16_t*)d_in[6];
    const bf16_t* wg = (const bf16_t*)d_in[8];
    const bf16_t* wu = (const bf16_t*)d_in[9];
    const bf16_t* wd = (const bf16_t*)d_in[10];
    bf16_t* out = (bf16_t*)d_out;
    char* ws = (char*)d_ws;
    bf16_t* u = (bf16_t*)(ws);
    bf16_t* h = (bf16_t*)(ws + (4 << 20));
    bf16_t* a = (bf16_t*)(ws + (8 << 20));
    bf16_t* m = (bf16_t*)(ws + (12 << 20));
    float* K = (float*)(ws + (24 << 20));
    k_ln<<<T, 256, 0, stream>>>(x, u);
    k_kgen<<<dim3(T / 256, D), 256, 0, stream>>>(logA, Aim, Cre, Cim, logdt, K);
    k_conv<<<dim3(D / 128, T), 128, 0, stream>>>(x, u, K, Dskip, h);
    k_ln<<<T, 256, 0, stream>>>(h, a);
    k_gemm1<<<dim3(F / 256, T), 256, 0, stream>>>(a, wg, wu, m);
    k_gemm2<<<dim3(D / 128, T), 128, 0, stream>>>(m, wd, h, out);
}
```

```cpp
#include <hip/hip_runtime.h>
#include <stdint.h>

typedef unsigned short bf16_t;
typedef short s16x4 __attribute__((ext_vector_type(4)));
typedef __bf16 bf16x8 __attribute__((ext_vector_type(8)));
typedef __bf16 bf16x2 __attribute__((ext_vector_type(2)));
typedef _Float16 h16x8 __attribute__((ext_vector_type(8)));
typedef _Float16 h16x2 __attribute__((ext_vector_type(2)));
typedef float f32x2 __attribute__((ext_vector_type(2)));
typedef float f32x4 __attribute__((ext_vector_type(4)));
typedef float f32x16 __attribute__((ext_vector_type(16)));
typedef unsigned u32x2 __attribute__((ext_vector_type(2)));
typedef unsigned u32x4 __attribute__((ext_vector_type(4)));
#define LAS __attribute__((address_space(3)))
typedef LAS char* lptr;

constexpr int T = 2048, D = 640, NM = 192, F = 1536;
constexpr size_t OFF_UT = 0;
constexpr size_t OFF_XT = OFF_UT + (size_t)T * D * 2;
constexpr size_t OFF_KX = OFF_XT + (size_t)T * D * 2;
constexpr size_t OFF_HT = OFF_KX + (size_t)D * 2 * 2048 * 2;
constexpr size_t OFF_CS = OFF_HT + (size_t)T * D * 2;
constexpr size_t OFF_MF = OFF_CS + 16384;
constexpr size_t OFF_WD = OFF_MF + (size_t)T * F * 2;
constexpr size_t OFF_PROF = OFF_WD + (size_t)F * D * 2;

#define STAMP(kid, i)
#define PROF_ARG
#define PROF_PASS

__device__ __forceinline__ float bf2f(unsigned v) { return __uint_as_float(v << 16); }
__device__ __forceinline__ float bflo(unsigned v) { return __uint_as_float(v << 16); }
__device__ __forceinline__ float bfhi(unsigned v) { return __uint_as_float(v & 0xffff0000u); }
__device__ __forceinline__ unsigned pk_bf16(float lo, float hi) { f32x2 v = {lo, hi}; return __builtin_bit_cast(unsigned, __builtin_convertvector(v, bf16x2)); }
__device__ __forceinline__ float rbf(float f) { return bflo(pk_bf16(f, f)); }
__device__ __forceinline__ unsigned pk_f16(float lo, float hi) { f32x2 v = {lo, hi}; return __builtin_bit_cast(unsigned, __builtin_convertvector(v, h16x2)); }
template <int CTRL> __device__ __forceinline__ float dppf(float x) { return __int_as_float(__builtin_amdgcn_update_dpp(0, __float_as_int(x), CTRL, 0xF, 0xF, true)); }
__device__ __forceinline__ float row16_sum(float x) { x += dppf<0xB1>(x); x += dppf<0x4E>(x); x += dppf<0x141>(x); x += dppf<0x140>(x); return x; }
__device__ __forceinline__ float row16_max(float x) { x = fmaxf(x, dppf<0xB1>(x)); x = fmaxf(x, dppf<0x4E>(x)); x = fmaxf(x, dppf<0x141>(x)); x = fmaxf(x, dppf<0x140>(x)); return x; }
__device__ __forceinline__ u32x2 tr_read(lptr p) { return __builtin_bit_cast(u32x2, __builtin_amdgcn_ds_read_tr16_b64_v4i16((LAS s16x4*)p)); }
__device__ __forceinline__ int uni(int v) { return __builtin_amdgcn_readfirstlane(v); }

__device__ __forceinline__ void dma16(const void* sbase, unsigned voff, unsigned ldsaddr) {
    asm volatile("s_mov_b32 m0, %2\n\ts_nop 0\n\tglobal_load_lds_dwordx4 %0, %1" :: "v"(voff), "s"(sbase), "s"(ldsaddr) : "memory");
}
__device__ __forceinline__ void dma4(const void* sbase, unsigned voff, unsigned ldsaddr) {
    asm volatile("s_mov_b32 m0, %2\n\ts_nop 0\n\tglobal_load_lds_dword %0, %1" :: "v"(voff), "s"(sbase), "s"(ldsaddr) : "memory");
}
#define WAIT_VM(n) asm volatile("s_waitcnt vmcnt(" #n ")" ::: "memory")
template <int N> __device__ __forceinline__ void wait_vm() { asm volatile("s_waitcnt vmcnt(%0)" :: "n"(N) : "memory"); }
__device__ __forceinline__ void bar() { asm volatile("s_waitcnt lgkmcnt(0)\n\ts_barrier" ::: "memory"); }

struct KTab { unsigned short v[2][2048]; };
constexpr int ci_of(int l) {
    if (l < 0) return 641;
    if (l < 256) return l;
    const int s = l < 512 ? 1 : (l < 1024 ? 2 : 3);
    return ((l + (1 << (s - 1)) - 1 + ((l >> s) & 1)) >> s) + 128 * s;
}
constexpr KTab make_tab() { KTab t{}; for (int c = 0; c < 2; ++c) for (int y = 0; y < 2048; ++y) t.v[c][y] = (unsigned short)(2 * ci_of(2047 - c - y)); return t; }
__device__ const KTab g_ktab = make_tab();

constexpr int LN_ROW = 1344;
constexpr int KG_ROW = 272;
constexpr int KG_WAVE = 57 * KG_ROW;
constexpr int KG_KC = 3 * KG_WAVE + 7 * KG_ROW;
constexpr int K1_LDS = KG_KC + 1536;

__device__ __forceinline__ void ln1_block(int b, const bf16_t* __restrict__ x, bf16_t* __restrict__ uT, bf16_t* __restrict__ xT, lptr sm) {
    const int tid = threadIdx.x, r = tid >> 4, l16 = tid & 15, lane = tid & 63, w = uni(tid >> 6);
    const int t = 16 * b + r;
    u32x4 v[5];
#pragma unroll
    for (int j = 0; j < 5; ++j) v[j] = *(const u32x4*)(x + (size_t)t * D + (l16 + 16 * j) * 8);
    float f[40];
    float s = 0.f;
#pragma unroll
    for (int j = 0; j < 5; ++j)
#pragma unroll
        for (int i = 0; i < 4; ++i) { f[8 * j + 2 * i] = bflo(v[j][i]); f[8 * j + 2 * i + 1] = bfhi(v[j][i]); s += f[8 * j + 2 * i] + f[8 * j + 2 * i + 1]; }
    s = row16_sum(s);
    const float mean = s / (float)D;
    float q = 0.f;
#pragma unroll
    for (int i = 0; i < 40; ++i) { const float dv = f[i] - mean; q += dv * dv; }
    q = row16_sum(q);
    const float m = rbf(mean), vv = rbf(q / (float)D);
    const float sd = rbf(sqrtf(rbf(vv + 1e-5f)));
    const float rs = 1.0f / sd;
#pragma unroll
    for (int j = 0; j < 5; ++j) {
        u32x4 o;
#pragma unroll
        for (int i = 0; i < 4; ++i) {
            const unsigned dd = pk_bf16(f[8 * j + 2 * i] - m, f[8 * j + 2 * i + 1] - m);
            o[i] = pk_bf16(bflo(dd) * rs, bfhi(dd) * rs);
        }
        *(LAS u32x4*)(sm + r * LN_ROW + (l16 + 16 * j) * 16) = v[j];
        *(LAS u32x4*)(sm + (16 + r) * LN_ROW + (l16 + 16 * j) * 16) = o;
    }
    __syncthreads();
    const int g = lane >> 4, q4 = (lane & 15) >> 2, p = lane & 3;
#pragma unroll
    for (int it = 0; it < 10; ++it) {
        const int item = w + 4 * it;
        const int arr = item / 20, o = (item / 10) & 1, db = item % 10;
        lptr a = sm + (arr * 16 + 8 * o + q4) * LN_ROW + (64 * db + 16 * g + 4 * p) * 2;
        const u32x2 r1 = tr_read(a), r2 = tr_read(a + 4 * LN_ROW);
        const u32x4 ov = {r1[0], r1[1], r2[0], r2[1]};
        bf16_t* dst = (arr ? uT : xT) + ((size_t)(2 * b + o) * D + 64 * db + lane) * 8;
        *(u32x4*)dst = ov;
    }
}

struct cf { float x, y; };
__device__ __forceinline__ cf cmul(cf a, cf b) { return {a.x * b.x - a.y * b.y, a.x * b.y + a.y * b.x}; }
__device__ __forceinline__ cf cexp_p(float arl, float air, float p) {
    const float mag = __builtin_amdgcn_exp2f(arl * p);
    float rev = air * p; rev -= rintf(rev);
    return {mag * __builtin_amdgcn_cosf(rev), mag * __builtin_amdgcn_sinf(rev)};
}

__device__ __forceinline__ void kgen_block(int d, const bf16_t* __restrict__ logA, const bf16_t* __restrict__ Aim, const bf16_t* __restrict__ Cre,
                                           const bf16_t* __restrict__ Cim, const bf16_t* __restrict__ logdt, bf16_t* __restrict__ Kx, lptr sm) {
    const int tid = threadIdx.x, lane = tid & 63, w = uni(tid >> 6);
    float unscale = 1.f;
    const u32x4 tb0 = *(const u32x4*)(&g_ktab.v[0][8 * tid]), tb1 = *(const u32x4*)(&g_ktab.v[1][8 * tid]);
    {
        const float dt = rbf(expf(bf2f(logdt[d])));
        float cr[3], ci[3];
#pragma unroll
        for (int i = 0; i < 3; ++i) { cr[i] = bf2f(Cre[d * NM + 64 * i + lane]); ci[i] = bf2f(Cim[d * NM + 64 * i + lane]); }
        float mx = 0.f;
#pragma unroll
        for (int i = 0; i < 3; ++i) mx = fmaxf(mx, fmaxf(fabsf(cr[i]), fabsf(ci[i])));
        mx = row16_max(mx); mx = fmaxf(mx, __shfl_xor(mx, 16)); mx = fmaxf(mx, __shfl_xor(mx, 32));
        mx *= 2.f * dt;
        int e = (int)((__float_as_uint(mx) >> 23) & 0xff) - 126;
        e = e < -100 ? -100 : (e > 100 ? 100 : e);
        const float scale = __uint_as_float((unsigned)(127 - e) << 23);
        unscale = __uint_as_float((unsigned)(127 + e) << 23);
        if (w < 3) {
            const int n = 64 * w + lane;
            const float are = -rbf(expf(bf2f(logA[d * NM + n]))) * dt, aim = bf2f(Aim[d * NM + n]) * dt;
            const float crw = (w == 0 ? cr[0] : (w == 1 ? cr[1] : cr[2])) * dt, ciw = (w == 0 ? ci[0] : (w == 1 ? ci[1] : ci[2])) * dt;
            const float arl = are * 1.4426950408889634f, air = aim * 0.15915494309189535f;
            lptr base = sm + w * KG_WAVE + 4 * lane;
#define PUT(row, z) *(LAS unsigned*)(base + (row) * KG_ROW) = pk_f16((z).x, (z).y)
            const cf w1 = cexp_p(arl, air, 1.f), w8 = cexp_p(arl, air, 8.f), H1 = cexp_p(arl, air, 128.f);
            cf qv = {2.f * scale * crw, 2.f * scale * ciw};
#pragma unroll
            for (int c = 0; c < 16; ++c) { *(LAS unsigned*)(base + c * KG_ROW) = pk_f16(qv.x, -qv.y); if (c < 15) qv = cmul(qv, w8); }
            cf E[8]; E[0] = {1.f, 0.f}; E[1] = w1;
#pragma unroll
            for (int i = 2; i < 8; ++i) E[i] = cmul(E[i - 1], w1);
#pragma unroll
            for (int i = 0; i < 8; ++i) PUT(16 + i, E[i]);
            PUT(24, H1);
#pragma unroll
            for (int i = 1; i < 8; ++i) { const cf z = cmul(H1, E[i]); PUT(24 + i, z); }
            cf H = H1;
#pragma unroll
            for (int i = 0; i < 2; ++i) {
                H = cmul(H, H1); PUT(32 + 4 * i, H);
#pragma unroll
                for (int e2 = 1; e2 < 4; ++e2) { const cf z = cmul(H, E[2 * e2]); PUT(32 + 4 * i + e2, z); }
            }
#pragma unroll
            for (int i = 0; i < 4; ++i) { H = cmul(H, H1); PUT(40 + 2 * i, H); const cf z = cmul(H, E[4]); PUT(41 + 2 * i, z); }
#pragma unroll
            for (int i = 0; i < 9; ++i) { H = cmul(H, H1); PUT(48 + i, H); }
#undef PUT
            asm volatile("" ::: "memory");
            lptr rb = sm + w * KG_WAVE + (lane & 15) * KG_ROW + (lane >> 4) * 16;
            f32x4 acc[3] = {};
#pragma unroll
            for (int ks = 0; ks < 4; ++ks) {
                const h16x8 bq = *(LAS h16x8*)(rb + ks * 64);
#pragma unroll
                for (int mt = 0; mt < 3; ++mt) {
                    const h16x8 ar = *(LAS h16x8*)(rb + (16 + 16 * mt) * KG_ROW + ks * 64);
                    acc[mt] = __builtin_amdgcn_mfma_f32_16x16x32_f16(ar, bq, acc[mt], 0, 0, 0);
                }
            }
            asm volatile("" ::: "memory");
#pragma unroll
            for (int mt = 0; mt < 3; ++mt) *(LAS f32x4*)(sm + w * KG_WAVE + (mt * 64 + lane) * 16) = acc[mt];
        }
    }
    __syncthreads();
    {
        const int reg = w, row = 4 * (lane >> 4) + reg, c = lane & 15;
#pragma unroll
        for (int mt = 0; mt < 3; ++mt) {
            float s = 0.f;
#pragma unroll
            for (int ww = 0; ww < 3; ++ww) s += *(LAS float*)(sm + ww * KG_WAVE + (mt * 64 + lane) * 16 + reg * 4);
            int ci; bool ok = true;
            if (mt == 0) ci = 128 * (row >> 3) + (row & 7) + 8 * c;
            else if (mt == 1) ci = row < 8 ? 256 + 64 * (row >> 2) + (row & 3) + 4 * c : 384 + 32 * ((row - 8) >> 1) + (row & 1) + 2 * c;
            else { ci = 512 + 16 * row + c; ok = row < 8 || (row == 8 && c == 0); }
            if (ok) *(LAS unsigned short*)(sm + KG_KC + 2 * ci) = (unsigned short)pk_bf16(s * unscale, 0.f);
        }
        if (tid == 0) *(LAS unsigned short*)(sm + KG_KC + 2 * 641) = 0;
    }
    __syncthreads();
#pragma unroll
    for (int c = 0; c < 2; ++c) {
        const u32x4 tb = c ? tb1 : tb0;
        u32x4 o;
#pragma unroll
        for (int i = 0; i < 4; ++i) {
            const unsigned lo = *(LAS unsigned short*)(sm + KG_KC + (tb[i] & 0xffffu)), hi = *(LAS unsigned short*)(sm + KG_KC + (tb[i] >> 16));
            o[i] = lo | (hi << 16);
        }
        *(u32x4*)(Kx + ((size_t)d * 2 + c) * 2048 + 8 * tid) = o;
    }
}

__global__ __launch_bounds__(256, 3) void k1_ln_kgen(const bf16_t* __restrict__ x, const bf16_t* __restrict__ logA, const bf16_t* __restrict__ Aim,
                                                    const bf16_t* __restrict__ Cre, const bf16_t* __restrict__ Cim, const bf16_t* __restrict__ logdt,
                                                    char* __restrict__ ws PROF_ARG) {
    extern __shared__ __attribute__((aligned(16))) char smem[];
    lptr sm = (lptr)smem;
    STAMP(0, 0);
    if (blockIdx.x < 128) ln1_block(blockIdx.x, x, (bf16_t*)(ws + OFF_UT), (bf16_t*)(ws + OFF_XT), sm);
    else kgen_block(blockIdx.x - 128, logA, Aim, Cre, Cim, logdt, (bf16_t*)(ws + OFF_KX), sm);
    STAMP(0, 7);
}

constexpr int CV_KCH = 8320;
constexpr int CV_U = 4 * CV_KCH;
constexpr int CV_X = CV_U + 288 * 64;
constexpr int K2_LDS = 84 * 1024;

__device__ __forceinline__ void conv_block(int b, const char* __restrict__ ws, const bf16_t* __restrict__ Dskip, bf16_t* __restrict__ hT, lptr sm PROF_ARG) {
    const int tid = threadIdx.x, lane = tid & 63, w = uni(tid >> 6);
    const int d0 = 4 * b, d = d0 + w;
    const unsigned sm0 = (unsigned)(size_t)sm;
    {
        const unsigned vo = (unsigned)((lane >> 2) * (D * 16) + (d0 + ((lane & 3) ^ (lane >> 4))) * 16);
#pragma unroll
        for (int j = 0; j < 4; ++j) dma16(ws + OFF_UT + (size_t)(4 * w + j) * (16 * D * 16), vo, sm0 + CV_U + 2048 + (4 * w + j) * 1024);
#pragma unroll
        for (int c = 0; c < 2; ++c)
#pragma unroll
            for (int j = 0; j < 4; ++j) dma16(ws + OFF_KX + ((size_t)d * 2 + c) * 4096 + j * 1024, lane * 16, sm0 + w * CV_KCH + c * 4160 + j * 1024);
#pragma unroll
        for (int j = 0; j < 4; ++j) dma16(ws + OFF_XT + (size_t)(4 * w + j) * (16 * D * 16), vo, sm0 + CV_X + (4 * w + j) * 1024);
    }
    const u32x4 z4 = {0u, 0u, 0u, 0u};
    if (tid < 128) *(LAS u32x4*)(sm + CV_U + tid * 16) = z4;
    if (lane < 8) *(LAS u32x4*)(sm + w * CV_KCH + (lane >> 2) * 4160 + 4096 + (lane & 3) * 16) = z4;
    const float dsk = bf2f(Dskip[d]);
    STAMP(1, 1);
    wait_vm<4>();
    bar();
    STAMP(1, 2);
    const int n = lane & 15, h = lane >> 4;
    lptr fb = sm + w * CV_KCH + ((n & 1) ? 0 : 4160) + 4 * (((2031 - n) >> 1) + 4 * h) - 512 * 7;
    f32x4 acc[8] = {};
    bf16x8 Fa[2][8], Ga[2][8];
    auto load_frags = [&](int rr, bf16x8 (&Fv)[8], bf16x8 (&Gv)[8]) {
        const int g0 = 2 * n + h + 30 - 4 * rr;
        lptr gp = sm + CV_U + 64 * g0 + 16 * (w ^ ((g0 >> 2) & 3));
        lptr fp = fb - 64 * rr;
#pragma unroll
        for (int c = 0; c < 8; ++c) {
            Gv[c] = *(LAS bf16x8*)(gp + 2048 * c);
            u32x4 fv;
#pragma unroll
            for (int i = 0; i < 4; ++i) fv[i] = *(LAS unsigned*)(fp + 512 * (7 - c) + 4 * i);
            Fv[c] = __builtin_bit_cast(bf16x8, fv);
        }
    };
    load_frags(0, Fa[0], Ga[0]);
#pragma unroll
    for (int rr = 0; rr < 8; ++rr) {
        if (rr < 7) load_frags(rr + 1, Fa[(rr + 1) & 1], Ga[(rr + 1) & 1]);
#pragma unroll
        for (int a = 0; a < 8; ++a)
#pragma unroll
            for (int c = a; c < 8; ++c) acc[c] = __builtin_amdgcn_mfma_f32_16x16x32_bf16(Fa[rr & 1][a], Ga[rr & 1][c - a], acc[c], 0, 0, 0);
    }
    STAMP(1, 3);
    asm volatile("" ::: "memory");
    lptr yb = sm + w * CV_KCH;
#pragma unroll
    for (int c = 0; c < 8; ++c) {
        const u32x2 yv = {pk_bf16(acc[c][0], acc[c][1]), pk_bf16(acc[c][2], acc[c][3])};
        *(LAS u32x2*)(yb + (256 * c + 16 * n + 4 * h) * 2) = yv;
    }
    wait_vm<0>();
    bar();
#pragma unroll
    for (int j = 0; j < 4; ++j) {
        const int gm = 64 * j + lane;
        const int sl = 4 * gm + (w ^ ((gm >> 2) & 3));
        const u32x4 yv = *(LAS u32x4*)(yb + gm * 16), uv = *(LAS u32x4*)(sm + CV_U + 2048 + sl * 16), xv = *(LAS u32x4*)(sm + CV_X + sl * 16);
        u32x4 o;
#pragma unroll
        for (int i = 0; i < 4; ++i) {
            const unsigned sk = pk_bf16(bflo(uv[i]) * dsk, bfhi(uv[i]) * dsk);
            const unsigned zz = pk_bf16(bflo(yv[i]) + bflo(sk), bfhi(yv[i]) + bfhi(sk));
            o[i] = pk_bf16(bflo(xv[i]) + bflo(zz), bfhi(xv[i]) + bfhi(zz));
        }
        *(u32x4*)(hT + (size_t)d * T + gm * 8) = o;
    }
}

__device__ __forceinline__ void wdtile_block(int fb, const bf16_t* __restrict__ wd, bf16_t* __restrict__ wdf, lptr sm) {
    const int tid = threadIdx.x, lane = tid & 63, w = uni(tid >> 6);
#pragma unroll
    for (int i = 0; i < 10; ++i) {
        const int ch = tid + 256 * i, row = ch / 80, c = ch % 80;
        *(LAS u32x4*)(sm + row * 1344 + c * 16) = *(const u32x4*)(wd + (size_t)(32 * fb + row) * D + c * 8);
    }
    __syncthreads();
    const int g = lane >> 4, q = (lane & 15) >> 2, p = lane & 3;
#pragma unroll
    for (int i = 0; i < 10; ++i) {
        const int dt = w + 4 * i;
        lptr a = sm + (16 * (g >> 1) + 4 * (g & 1) + q) * 1344 + (16 * dt + 4 * p) * 2;
        const u32x2 r1 = tr_read(a), r2 = tr_read(a + 8 * 1344);
        const u32x4 ov = {r1[0], r1[1], r2[0], r2[1]};
        *(u32x4*)(wdf + (((size_t)fb * 40 + dt) * 64 + lane) * 8) = ov;
    }
}

__device__ __forceinline__ void colsum_block(int cb, const bf16_t* __restrict__ wg, const bf16_t* __restrict__ wu, float* __restrict__ cs, lptr sm) {
    const int tid = threadIdx.x, l8 = tid & 7, rg = tid >> 3;
    const int c0 = 64 * cb;
    const bf16_t* wsrc = (c0 < F ? wg + c0 : wu + (c0 - F)) + l8 * 8;
    float a[8] = {};
    u32x4 v[20];
#pragma unroll
    for (int i = 0; i < 20; ++i) v[i] = *(const u32x4*)(wsrc + (size_t)(rg + 32 * i) * F);
#pragma unroll
    for (int i = 0; i < 20; ++i)
#pragma unroll
        for (int j = 0; j < 4; ++j) { a[2 * j] += bflo(v[i][j]); a[2 * j + 1] += bfhi(v[i][j]); }
#pragma unroll
    for (int j = 0; j < 8; ++j) *(LAS float*)(sm + (rg * 65 + l8 * 8 + j) * 4) = a[j];
    __syncthreads();
    if (tid < 64) {
        float s = 0.f;
#pragma unroll
        for (int i = 0; i < 32; ++i) s += *(LAS float*)(sm + (i * 65 + tid) * 4);
        cs[c0 + tid] = s;
    }
}

__global__ __launch_bounds__(256, 1) void k2_conv(char* __restrict__ ws, const bf16_t* __restrict__ Dskip, const bf16_t* __restrict__ wg,
                                                 const bf16_t* __restrict__ wu, const bf16_t* __restrict__ wd PROF_ARG) {
    extern __shared__ __attribute__((aligned(16))) char smem[];
    lptr sm = (lptr)smem;
    STAMP(1, 0);
    const int b = blockIdx.x;
    if (b < 160) conv_block(b, ws, Dskip, (bf16_t*)(ws + OFF_HT), sm
    );
    else if (b < 208) wdtile_block(b - 160, wd, (bf16_t*)(ws + OFF_WD), sm);
    else colsum_block(b - 208, wg, wu, (float*)(ws + OFF_CS), sm);
    STAMP(1, 7);
}

constexpr int G1_NS = 7, G1_SS = 20736, G1_G = 8192, G1_U = 8192 + 6144 + 32;
constexpr int G1_CS = G1_NS * G1_SS;
constexpr int G1_ST = G1_CS + 1024;
constexpr int K3_LDS = G1_ST + 1024;

__global__ __launch_bounds__(256, 1) void k3_gemm1(char* __restrict__ ws, const bf16_t* __restrict__ wg, const bf16_t* __restrict__ wu PROF_ARG) {
    extern __shared__ __attribute__((aligned(16))) char smem[];
    lptr sm = (lptr)smem;
    STAMP(2, 0);
    const int tid = threadIdx.x, lane = tid & 63, w = uni(tid >> 6);
    const unsigned sm0 = (unsigned)(size_t)sm;
    const int grp = blockIdx.x & 7, idx = blockIdx.x >> 3;
    const int rb = (grp & 1) * 8 + (idx & 7), cb = (grp >> 1) * 4 + (idx >> 3);
    const int t0 = 128 * rb, f0 = 96 * cb;
    const char* hT = ws + OFF_HT;
    unsigned vh[2], vw[3];
#pragma unroll
    for (int j = 0; j < 2; ++j) {
        const int o = (2 * w + j) * 1024 + lane * 16, k = o >> 8, c = ((o >> 4) & 15) ^ ((k & 3) << 2);
        vh[j] = (unsigned)(k * (T * 2) + t0 * 2 + c * 16);
    }
#pragma unroll
    for (int j = 0; j < 3; ++j) {
        const int jj = (w + 4 * j) % 6, o = jj * 1024 + lane * 16, k = o / 192, c = (o % 192) >> 4;
        vw[j] = (unsigned)(k * (F * 2) + f0 * 2 + c * 16);
    }
    auto stage = [&](int s) {
        const unsigned lb = sm0 + (s % G1_NS) * G1_SS;
        const char* hs = hT + (size_t)s * 32 * (T * 2);
#pragma unroll
        for (int j = 0; j < 2; ++j) dma16(hs, vh[j], lb + (2 * w + j) * 1024);
#pragma unroll
        for (int j = 0; j < 3; ++j) {
            const int i = w + 4 * j;
            const bool up = i >= 6;
            const char* src = (const char*)(up ? wu : wg) + (size_t)s * 32 * (F * 2);
            dma16(src, vw[j], lb + (up ? G1_U + (i - 6) * 1024 : G1_G + i * 1024));
        }
    };
    if (w < 3) dma4(ws + OFF_CS, (unsigned)(((w == 0 ? f0 + lane : (w == 1 ? (lane < 32 ? f0 + 64 + lane : F + f0 + lane - 32) : F + f0 + 32 + lane))) * 4), sm0 + G1_CS + w * 256);
#pragma unroll
    for (int s = 0; s < G1_NS - 1; ++s) stage(s);
    STAMP(2, 1);

    const int wr = w >> 1, wc = w & 1;
    const int g = lane >> 4, q = (lane & 15) >> 2, p = lane & 3, hh = g >> 1;
    const int krow = 4 * hh + q;
    int a_off[3], b_off[2];
#pragma unroll
    for (int ft = 0; ft < 3; ++ft) a_off[ft] = ((g & 1) ? G1_U : G1_G) + krow * 192 + (3 * wc + ft) * 32 + p * 8;
#pragma unroll
    for (int tt = 0; tt < 2; ++tt) b_off[tt] = krow * 256 + (((2 * wr + tt) ^ q) * 64) + (g & 1) * 32 + p * 8;
    f32x16 acc[3][2] = {};
    bf16x8 Af[2][3], Bf[2][2];
    float ssum = 0.f, ssq = 0.f;
    auto load_sub = [&](int sub, bf16x8 (&A)[3], bf16x8 (&B)[2]) {
        lptr sb = sm + ((sub >> 1) % G1_NS) * G1_SS;
        const int kk = sub & 1;
#pragma unroll
        for (int tt = 0; tt < 2; ++tt) {
            const u32x2 r1 = tr_read(sb + b_off[tt] + kk * 16 * 256), r2 = tr_read(sb + b_off[tt] + kk * 16 * 256 + 8 * 256);
            const u32x4 v = {r1[0], r1[1], r2[0], r2[1]}; B[tt] = __builtin_bit_cast(bf16x8, v);
        }
#pragma unroll
        for (int ft = 0; ft < 3; ++ft) {
            const u32x2 r1 = tr_read(sb + a_off[ft] + kk * 16 * 192), r2 = tr_read(sb + a_off[ft] + kk * 16 * 192 + 8 * 192);
            const u32x4 v = {r1[0], r1[1], r2[0], r2[1]}; A[ft] = __builtin_bit_cast(bf16x8, v);
        }
    };
    auto mma_sub = [&](const bf16x8 (&A)[3], const bf16x8 (&B)[2]) {
#pragma unroll
        for (int ft = 0; ft < 3; ++ft)
#pragma unroll
            for (int tt = 0; tt < 2; ++tt) acc[ft][tt] = __builtin_amdgcn_mfma_f32_32x32x16_bf16(A[ft], B[tt], acc[ft][tt], 0, 0, 0);
        const u32x4 sv = __builtin_bit_cast(u32x4, wc ? B[1] : B[0]);
        const bf16x2 one = __builtin_bit_cast(bf16x2, 0x3F803F80u);
#pragma unroll
        for (int i = 0; i < 4; ++i) {
            const unsigned e32 = sv[i];
            const bf16x2 e = __builtin_bit_cast(bf16x2, e32);
            ssum = __builtin_amdgcn_fdot2_f32_bf16(e, one, ssum, false);
            ssq = __builtin_amdgcn_fdot2_f32_bf16(e, e, ssq, false);
        }
    };
    wait_vm<25>();
    bar();
    STAMP(2, 2);
    load_sub(0, Af[0], Bf[0]);
#pragma unroll
    for (int s = 0; s < 20; ++s) {
        load_sub(2 * s + 1, Af[1], Bf[1]);
        mma_sub(Af[0], Bf[0]);
        if (s < 19) {
            if (s <= 14) wait_vm<20>(); else if (s == 15) wait_vm<15>(); else if (s == 16) wait_vm<10>(); else if (s == 17) wait_vm<5>(); else wait_vm<0>();
            asm volatile("s_barrier" ::: "memory");
            if (s + G1_NS - 1 < 20) stage(s + G1_NS - 1);
            load_sub(2 * s + 2, Af[0], Bf[0]);
        }
        mma_sub(Af[1], Bf[1]);
    }
    STAMP(2, 3);
    ssum += __shfl_xor(ssum, 32); ssq += __shfl_xor(ssq, 32);
    {
        const float mean = ssum / (float)D;
        const float var = fmaxf(ssq / (float)D - mean * mean, 0.f);
        const float m = rbf(mean), sd = rbf(sqrtf(rbf(rbf(var) + 1e-5f)));
        const float rs = 1.0f / sd;
        if (lane < 32) *(LAS f32x2*)(sm + G1_ST + (64 * wr + 32 * wc + lane) * 8) = (f32x2){rs, m * rs};
    }
    bar();
    bf16_t* mF = (bf16_t*)(ws + OFF_MF);
#pragma unroll
    for (int tt = 0; tt < 2; ++tt) {
        const f32x2 st = *(LAS f32x2*)(sm + G1_ST + (64 * wr + 32 * tt + (lane & 31)) * 8);
        const int tg = t0 + 64 * wr + 32 * tt + (lane & 31);
#pragma unroll
        for (int ft = 0; ft < 3; ++ft) {
            const int fl = 16 * (3 * wc + ft) + 4 * hh;
            const f32x4 cg0 = *(LAS f32x4*)(sm + G1_CS + fl * 4), cg1 = *(LAS f32x4*)(sm + G1_CS + (fl + 8) * 4);
            const f32x4 cu0 = *(LAS f32x4*)(sm + G1_CS + (96 + fl) * 4), cu1 = *(LAS f32x4*)(sm + G1_CS + (96 + fl + 8) * 4);
            float mv[8];
#pragma unroll
            for (int r = 0; r < 8; ++r) {
                const float cg = r < 4 ? cg0[r & 3] : cg1[r & 3], cu = r < 4 ? cu0[r & 3] : cu1[r & 3];
                const float gv = acc[ft][tt][r] * st[0] - st[1] * cg, uv = acc[ft][tt][8 + r] * st[0] - st[1] * cu;
                const float e = __builtin_amdgcn_exp2f(gv * (-2.302208198f + -0.1029432397f * gv * gv));
                mv[r] = gv * uv * __builtin_amdgcn_rcpf(1.0f + e);
            }
            const int ftg = 6 * cb + 3 * wc + ft;
            const int lp = (tg & 15) + 16 * (2 * (ftg & 1) + hh);
            const u32x4 o = {pk_bf16(mv[0], mv[1]), pk_bf16(mv[2], mv[3]), pk_bf16(mv[4], mv[5]), pk_bf16(mv[6], mv[7])};
            *(u32x4*)(mF + (((size_t)(tg >> 4) * 48 + (ftg >> 1)) * 64 + lp) * 8) = o;
        }
    }
    STAMP(2, 7);
}

constexpr int G2_NS = 4, G2_SS = 36 * 1024, G2_W = 16 * 1024;
constexpr int G2_H = G2_NS * G2_SS;
constexpr int K4_LDS = G2_H + 10240;

template <int ND>
__device__ __forceinline__ void g2_body(lptr sm, unsigned sm0, int w, int lane, int th, int dl0, const char* ms, const char* wsrc, unsigned vm, unsigned vwv,
                                        f32x4 (&acc)[3][2]) {
    auto stage = [&](int s) {
        const unsigned lb = sm0 + (s % G2_NS) * G2_SS;
#pragma unroll
        for (int j = 0; j < 4; ++j) dma16(ms + (size_t)(4 * s + j) * 1024, vm, lb + (4 * w + j) * 1024);
#pragma unroll
        for (int j = 0; j < 5; ++j) dma16(wsrc + (size_t)(4 * s) * (40 * 1024) + j * 1024, vwv, lb + G2_W + (5 * w + j) * 1024);
    };
#pragma unroll
    for (int s = 0; s < G2_NS - 1; ++s) stage(s);
#pragma unroll
    for (int s = 0; s < 12; ++s) {
        if (s <= 9) wait_vm<18>(); else if (s == 10) wait_vm<9>(); else wait_vm<0>();
        bar();
        if (s + G2_NS - 1 < 12) stage(s + G2_NS - 1);
        lptr sb = sm + (s % G2_NS) * G2_SS + lane * 16;
#pragma unroll
        for (int fl = 0; fl < 4; ++fl) {
            bf16x8 B[2], A[ND];
#pragma unroll
            for (int tt = 0; tt < 2; ++tt) B[tt] = *(LAS bf16x8*)(sb + ((2 * th + tt) * 4 + fl) * 1024);
#pragma unroll
            for (int dt = 0; dt < ND; ++dt) A[dt] = *(LAS bf16x8*)(sb + G2_W + (fl * 5 + dl0 + dt) * 1024);
#pragma unroll
            for (int dt = 0; dt < ND; ++dt)
#pragma unroll
                for (int tt = 0; tt < 2; ++tt) acc[dt][tt] = __builtin_amdgcn_mfma_f32_16x16x32_bf16(A[dt], B[tt], acc[dt][tt], 0, 0, 0);
        }
    }
}

__global__ __launch_bounds__(256, 1) void k4_gemm2(char* __restrict__ ws, bf16_t* __restrict__ out PROF_ARG) {
    extern __shared__ __attribute__((aligned(16))) char smem[];
    lptr sm = (lptr)smem;
    STAMP(3, 0);
    const int tid = threadIdx.x, lane = tid & 63, w = uni(tid >> 6);
    const unsigned sm0 = (unsigned)(size_t)sm;
    const int grp = blockIdx.x & 7, idx = blockIdx.x >> 3;
    const int tb = (grp >> 1) * 8 + (idx & 7), db = (grp & 1) * 4 + (idx >> 3);
    const int t0 = 64 * tb, d0 = 80 * db;
#pragma unroll
    for (int j = 0; j < 3; ++j) {
        const int i = w + 4 * j;
        if (i < 10) {
            const int o = i * 1024 + lane * 16, row = o >> 7, c = ((o >> 4) & 7) ^ (((row >> 1) & 3) << 1);
            dma16(ws + OFF_HT, (unsigned)((d0 + row) * (T * 2) + t0 * 2 + c * 16), sm0 + G2_H + i * 1024);
        }
    }
    const char* ms = ws + OFF_MF + (size_t)(4 * tb + w) * 48 * 1024;
    const char* wsrc = ws + OFF_WD + (size_t)w * (40 * 1024) + (size_t)(5 * db) * 1024;
    const int th = w & 1, dh = w >> 1;
    f32x4 acc[3][2] = {};
    if (dh == 0) g2_body<3>(sm, sm0, w, lane, th, 0, ms, wsrc, lane * 16, lane * 16, acc);
    else g2_body<2>(sm, sm0, w, lane, th, 3, ms, wsrc, lane * 16, lane * 16, acc);
    STAMP(3, 3);
    bar();
    const int g = lane >> 4, q = (lane & 15) >> 2, p = lane & 3;
    const int nd = dh == 0 ? 3 : 2, dl0 = dh == 0 ? 0 : 3;
#pragma unroll
    for (int dt = 0; dt < 3; ++dt) {
        if (dt < nd) {
#pragma unroll
            for (int tt = 0; tt < 2; ++tt) {
                const int row = 16 * (dl0 + dt) + 4 * g + q, seg = (2 * th + tt) ^ ((row >> 1) & 3);
                const u32x2 hv = tr_read(sm + G2_H + row * 128 + seg * 32 + p * 8);
                const unsigned o01 = pk_bf16(acc[dt][tt][0], acc[dt][tt][1]), o23 = pk_bf16(acc[dt][tt][2], acc[dt][tt][3]);
                const u32x2 r = {pk_bf16(bflo(hv[0]) + bflo(o01), bfhi(hv[0]) + bfhi(o01)), pk_bf16(bflo(hv[1]) + bflo(o23), bfhi(hv[1]) + bfhi(o23))};
                *(LAS u32x2*)(sm + (16 * (2 * th + tt) + (lane & 15)) * 176 + (16 * (dl0 + dt) + 4 * g) * 2) = r;
            }
        }
    }
    bar();
#pragma unroll
    for (int j = 0; j < 3; ++j) {
        const int ch = tid + 256 * j;
        if (ch < 640) {
            const int row = ch / 10, c = ch % 10;
            *(u32x4*)(out + (size_t)(t0 + row) * D + d0 + c * 8) = *(LAS u32x4*)(sm + row * 176 + c * 16);
        }
    }
    STAMP(3, 7);
}

extern "C" void kernel_launch(void* const* d_in, const int* in_sizes, int n_in, void* d_out, int out_size, void* d_ws, size_t ws_size, hipStream_t stream) {
    const bf16_t* x = (const bf16_t*)d_in[0];
    const bf16_t* logA = (const bf16_t*)d_in[1];
    const bf16_t* Aim = (const bf16_t*)d_in[2];
    const bf16_t* Cre = (const bf16_t*)d_in[3];
    const bf16_t* Cim = (const bf16_t*)d_in[4];
    const bf16_t* logdt = (const bf16_t*)d_in[5];
    const bf16_t* Dskip = (const bf16_t*)d_in[6];
    const bf16_t* wg = (const bf16_t*)d_in[8];
    const bf16_t* wu = (const bf16_t*)d_in[9];
    const bf16_t* wd = (const bf16_t*)d_in[10];
    char* ws = (char*)d_ws;
    (void)hipFuncSetAttribute((const void*)k1_ln_kgen, hipFuncAttributeMaxDynamicSharedMemorySize, K1_LDS);
    (void)hipFuncSetAttribute((const void*)k2_conv, hipFuncAttributeMaxDynamicSharedMemorySize, K2_LDS);
    (void)hipFuncSetAttribute((const void*)k3_gemm1, hipFuncAttributeMaxDynamicSharedMemorySize, K3_LDS);
    (void)hipFuncSetAttribute((const void*)k4_gemm2, hipFuncAttributeMaxDynamicSharedMemorySize, K4_LDS);
    k1_ln_kgen<<<128 + D, 256, K1_LDS, stream>>>(x, logA, Aim, Cre, Cim, logdt, ws PROF_PASS);
    k2_conv<<<256, 256, K2_LDS, stream>>>(ws, Dskip, wg, wu, wd PROF_PASS);
    k3_gemm1<<<256, 256, K3_LDS, stream>>>(ws, wg, wu PROF_PASS);
    k4_gemm2<<<256, 256, K4_LDS, stream>>>(ws, (bf16_t*)d_out PROF_PASS);
}
```

```cpp
#include <hip/hip_runtime.h>
#include <stdint.h>

typedef unsigned short bf16_t;
typedef short s16x4 __attribute__((ext_vector_type(4)));
typedef __bf16 bf16x8 __attribute__((ext_vector_type(8)));
typedef __bf16 bf16x2 __attribute__((ext_vector_type(2)));
typedef _Float16 h16x8 __attribute__((ext_vector_type(8)));
typedef _Float16 h16x2 __attribute__((ext_vector_type(2)));
typedef float f32x2 __attribute__((ext_vector_type(2)));
typedef float f32x4 __attribute__((ext_vector_type(4)));
typedef float f32x16 __attribute__((ext_vector_type(16)));
typedef unsigned u32x2 __attribute__((ext_vector_type(2)));
typedef unsigned u32x4 __attribute__((ext_vector_type(4)));
#define LAS __attribute__((address_space(3)))
typedef LAS char* lptr;

constexpr int T = 2048, D = 640, NM = 192, F = 1536;
constexpr int KC_PITCH = 1296;
constexpr size_t OFF_U = 0;
constexpr size_t OFF_X = OFF_U + (size_t)T * D * 2;
constexpr size_t OFF_KC = OFF_X + (size_t)T * D * 2;
constexpr size_t OFF_HT = OFF_KC + (size_t)D * KC_PITCH + 4096;
constexpr size_t OFF_CS = OFF_HT + (size_t)T * D * 2;
constexpr size_t OFF_MF = OFF_CS + 32768;
constexpr size_t OFF_WD = OFF_MF + (size_t)T * F * 2;
constexpr size_t OFF_WG = OFF_WD + (size_t)F * D * 2;
constexpr size_t OFF_PROF = OFF_WG + (size_t)2 * F * D * 2;

#define STAMP(kid, i)
#define CSTAMP(kid, i)
#define PROF_ARG
#define PROF_PASS
#define PROF_FWD

__device__ __forceinline__ float bf2f(unsigned v) { return __uint_as_float(v << 16); }
__device__ __forceinline__ float bflo(unsigned v) { return __uint_as_float(v << 16); }
__device__ __forceinline__ float bfhi(unsigned v) { return __uint_as_float(v & 0xffff0000u); }
__device__ __forceinline__ unsigned pk_bf16(float lo, float hi) { f32x2 v = {lo, hi}; return __builtin_bit_cast(unsigned, __builtin_convertvector(v, bf16x2)); }
__device__ __forceinline__ float rbf(float f) { return bflo(pk_bf16(f, f)); }
__device__ __forceinline__ unsigned pk_f16(f32x2 v) { return __builtin_bit_cast(unsigned, __builtin_convertvector(v, h16x2)); }
template <int CTRL> __device__ __forceinline__ float dppf(float x) { return __int_as_float(__builtin_amdgcn_update_dpp(0, __float_as_int(x), CTRL, 0xF, 0xF, true)); }
__device__ __forceinline__ float row16_sum(float x) { x += dppf<0xB1>(x); x += dppf<0x4E>(x); x += dppf<0x141>(x); x += dppf<0x140>(x); return x; }
__device__ __forceinline__ float row16_max(float x) { x = fmaxf(x, dppf<0xB1>(x)); x = fmaxf(x, dppf<0x4E>(x)); x = fmaxf(x, dppf<0x141>(x)); x = fmaxf(x, dppf<0x140>(x)); return x; }
__device__ __forceinline__ float xsum16_32(float x) {
    const unsigned u = __float_as_uint(x);
    const auto r = __builtin_amdgcn_permlane16_swap(u, u, false, false);
    const unsigned v = __float_as_uint(__uint_as_float(r[0]) + __uint_as_float(r[1]));
    const auto q = __builtin_amdgcn_permlane32_swap(v, v, false, false);
    return __uint_as_float(q[0]) + __uint_as_float(q[1]);
}
__device__ __forceinline__ u32x2 tr_read(lptr p) { return __builtin_bit_cast(u32x2, __builtin_amdgcn_ds_read_tr16_b64_v4i16((LAS s16x4*)p)); }
__device__ __forceinline__ u32x4 tr_read2(lptr p, int off2) { const u32x2 r1 = tr_read(p), r2 = tr_read(p + off2); return (u32x4){r1[0], r1[1], r2[0], r2[1]}; }
__device__ __forceinline__ int uni(int v) { return __builtin_amdgcn_readfirstlane(v); }

__device__ __forceinline__ void dma16(const void* sbase, unsigned voff, unsigned ldsaddr) {
    asm volatile("s_mov_b32 m0, %2\n\ts_nop 0\n\tglobal_load_lds_dwordx4 %0, %1" :: "v"(voff), "s"(sbase), "s"(ldsaddr) : "memory");
}
__device__ __forceinline__ void dma4(const void* sbase, unsigned voff, unsigned ldsaddr) {
    asm volatile("s_mov_b32 m0, %2\n\ts_nop 0\n\tglobal_load_lds_dword %0, %1" :: "v"(voff), "s"(sbase), "s"(ldsaddr) : "memory");
}
template <int N> __device__ __forceinline__ void wait_vm() { asm volatile("s_waitcnt vmcnt(%0)" :: "n"(N) : "memory"); }
template <int N> __device__ __forceinline__ void wait_vm_rt(int n) {
    if (n >= N) wait_vm<N>(); else if constexpr (N > 0) wait_vm_rt<N - 1>(n);
}
__device__ __forceinline__ void bar() { asm volatile("s_waitcnt lgkmcnt(0)\n\ts_barrier" ::: "memory"); }
__device__ __forceinline__ void sbar() { asm volatile("s_barrier" ::: "memory"); }
#define SB() __builtin_amdgcn_sched_barrier(0)
#define PIN(x) asm volatile("" : "+v"(x))

constexpr int LN_ROW = 1344;
constexpr int KG_ROW = 272;
constexpr int KG_WAVE = 57 * KG_ROW;
constexpr int KG_KC = 3 * KG_WAVE + 7 * KG_ROW;
constexpr int K1_LDS = KG_KC + 1536;
constexpr int K1_NLN = 128, K1_NKG = D;

__device__ __forceinline__ void ln1_block(int b, const bf16_t* __restrict__ x, char* __restrict__ ws, lptr sm) {
    const int tid = threadIdx.x, r = tid >> 4, l16 = tid & 15, lane = tid & 63, w = uni(tid >> 6);
    const int t = 16 * b + r;
    u32x4 v[5];
#pragma unroll
    for (int j = 0; j < 5; ++j) v[j] = *(const u32x4*)(x + (size_t)t * D + (l16 + 16 * j) * 8);
    f32x2 f[20];
    f32x2 s2 = {0.f, 0.f};
#pragma unroll
    for (int j = 0; j < 5; ++j)
#pragma unroll
        for (int i = 0; i < 4; ++i) { f[4 * j + i] = (f32x2){bflo(v[j][i]), bfhi(v[j][i])}; s2 += f[4 * j + i]; }
    const float mean = row16_sum(s2[0] + s2[1]) / (float)D;
    f32x2 q2 = {0.f, 0.f};
#pragma unroll
    for (int i = 0; i < 20; ++i) { const f32x2 dv = f[i] - mean; q2 += dv * dv; }
    const float var = row16_sum(q2[0] + q2[1]) / (float)D;
    const float m = rbf(mean);
    const float rs = 1.0f / rbf(sqrtf(rbf(rbf(var) + 1e-5f)));
#pragma unroll
    for (int j = 0; j < 5; ++j) {
        u32x4 o;
#pragma unroll
        for (int i = 0; i < 4; ++i) {
            const f32x2 df = f[4 * j + i] - m;
            const unsigned dd = pk_bf16(df[0], df[1]);
            const f32x2 uu = (f32x2){bflo(dd), bfhi(dd)} * rs;
            o[i] = pk_bf16(uu[0], uu[1]);
        }
        *(LAS u32x4*)(sm + r * LN_ROW + (l16 + 16 * j) * 16) = o;
        *(LAS u32x4*)(sm + (16 + r) * LN_ROW + (l16 + 16 * j) * 16) = v[j];
    }
    __syncthreads();
    const int g = lane >> 4, q4 = (lane & 15) >> 2, p = lane & 3;
#pragma unroll
    for (int it = 0; it < 5; ++it) {
        const int item = w + 4 * it;
        const int arr = item / 10, db = item % 10;
        lptr a = sm + (16 * arr + q4) * LN_ROW + (64 * db + 16 * g + 4 * p) * 2;
        const u32x4 v0 = tr_read2(a, 4 * LN_ROW), v1 = tr_read2(a + 8 * LN_ROW, 4 * LN_ROW);
        char* dst = ws + (arr ? OFF_X : OFF_U) + ((size_t)b * D + 64 * db + lane) * 32;
        *(u32x4*)dst = v0;
        *(u32x4*)(dst + 16) = v1;
    }
}

__device__ __forceinline__ f32x2 cmul(f32x2 a, f32x2 b) {
    const f32x2 t = (f32x2){a[0], a[0]} * b;
    const f32x2 bs = {-b[1], b[0]};
    return __builtin_elementwise_fma((f32x2){a[1], a[1]}, bs, t);
}
__device__ __forceinline__ f32x2 cexp_p(float arl, float air, float p) {
    const float mag = __builtin_amdgcn_exp2f(arl * p);
    float rev = air * p; rev -= rintf(rev);
    return (f32x2){mag * __builtin_amdgcn_cosf(rev), mag * __builtin_amdgcn_sinf(rev)};
}

__device__ __forceinline__ void kgen_block(int d, const bf16_t* __restrict__ logA, const bf16_t* __restrict__ Aim, const bf16_t* __restrict__ Cre,
                                           const bf16_t* __restrict__ Cim, const bf16_t* __restrict__ logdt, char* __restrict__ Kc, lptr sm) {
    const int tid = threadIdx.x, lane = tid & 63, w = uni(tid >> 6);
    const int wn = w < 3 ? w : 0;
    unsigned ldt = logdt[d], la = logA[d * NM + 64 * wn + lane], ai = Aim[d * NM + 64 * wn + lane];
    unsigned cr[3], ci[3];
#pragma unroll
    for (int i = 0; i < 3; ++i) { cr[i] = Cre[d * NM + 64 * i + lane]; ci[i] = Cim[d * NM + 64 * i + lane]; }
    asm volatile("" : "+v"(ldt), "+v"(la), "+v"(ai), "+v"(cr[0]), "+v"(cr[1]), "+v"(cr[2]), "+v"(ci[0]), "+v"(ci[1]), "+v"(ci[2]));
    const float dt = rbf(expf(bf2f(ldt)));
    float mx = 0.f;
#pragma unroll
    for (int i = 0; i < 3; ++i) mx = fmaxf(mx, fmaxf(fabsf(bf2f(cr[i])), fabsf(bf2f(ci[i]))));
    mx = row16_max(mx);
    {
        const auto r1 = __builtin_amdgcn_permlane16_swap(__float_as_uint(mx), __float_as_uint(mx), false, false);
        mx = fmaxf(__uint_as_float(r1[0]), __uint_as_float(r1[1]));
        const auto r2 = __builtin_amdgcn_permlane32_swap(__float_as_uint(mx), __float_as_uint(mx), false, false);
        mx = fmaxf(__uint_as_float(r2[0]), __uint_as_float(r2[1]));
    }
    mx *= 2.f * dt;
    int e = (int)((__float_as_uint(mx) >> 23) & 0xff) - 126;
    e = e < -100 ? -100 : (e > 100 ? 100 : e);
    const float scale = __uint_as_float((unsigned)(127 - e) << 23), unscale = __uint_as_float((unsigned)(127 + e) << 23);
    if (w < 3) {
        const float are = -rbf(expf(bf2f(la))) * dt, aim = bf2f(ai) * dt;
        const float crw = bf2f(w == 0 ? cr[0] : (w == 1 ? cr[1] : cr[2])) * dt, ciw = bf2f(w == 0 ? ci[0] : (w == 1 ? ci[1] : ci[2])) * dt;
        const float arl = are * 1.4426950408889634f, air = aim * 0.15915494309189535f;
        lptr base = sm + w * KG_WAVE + 4 * lane;
#define PUT(row, z) *(LAS unsigned*)(base + (row) * KG_ROW) = pk_f16(z)
        const f32x2 w1 = cexp_p(arl, air, 1.f), w8 = cexp_p(arl, air, 8.f), H1 = cexp_p(arl, air, 128.f);
        f32x2 qv = {2.f * scale * crw, -2.f * scale * ciw};
        const f32x2 w8c = {w8[0], -w8[1]};
        f32x2 E[8], H[17];
        E[0] = (f32x2){1.f, 0.f}; E[1] = w1; H[1] = H1;
        PUT(0, qv); PUT(16, E[0]); PUT(17, E[1]); PUT(24, H1);
#pragma unroll
        for (int i = 1; i < 16; ++i) {
            qv = cmul(qv, w8c);
            H[i + 1] = cmul(H[i], H1);
            if (i < 7) E[i + 1] = cmul(E[i], w1);
            PUT(i, qv);
            if (i < 7) PUT(17 + i, E[i + 1]);
            const int hi = i + 1;
            if (hi < 4) PUT(32 + 4 * (hi - 2), H[hi]); else if (hi < 8) PUT(40 + 2 * (hi - 4), H[hi]); else PUT(48 + (hi - 8), H[hi]);
            if (i >= 7 && i < 14) PUT(25 + (i - 7), cmul(E[i - 6], H1));
            if (i >= 7 && i < 10) PUT(33 + (i - 7), cmul(E[2 * (i - 6)], H[2]));
            if (i >= 10 && i < 13) PUT(37 + (i - 10), cmul(E[2 * (i - 9)], H[3]));
            if (i >= 11 && i < 15) PUT(41 + 2 * (i - 11), cmul(H[4 + (i - 11)], E[4]));
        }
#undef PUT
        asm volatile("" ::: "memory");
        lptr rb = sm + w * KG_WAVE + (lane & 15) * KG_ROW + (lane >> 4) * 16;
        f32x4 acc[3] = {};
#pragma unroll
        for (int ks = 0; ks < 4; ++ks) {
            const h16x8 bq = *(LAS h16x8*)(rb + ks * 64);
#pragma unroll
            for (int mt = 0; mt < 3; ++mt) {
                const h16x8 ar = *(LAS h16x8*)(rb + (16 + 16 * mt) * KG_ROW + ks * 64);
                acc[mt] = __builtin_amdgcn_mfma_f32_16x16x32_f16(ar, bq, acc[mt], 0, 0, 0);
            }
        }
        asm volatile("" ::: "memory");
#pragma unroll
        for (int mt = 0; mt < 3; ++mt) *(LAS f32x4*)(sm + w * KG_WAVE + (mt * 64 + lane) * 16) = acc[mt];
    } else if (lane < 7) *(LAS unsigned short*)(sm + KG_KC + 2 * (641 + lane)) = 0;
    __syncthreads();
    {
        const int reg = w, row = 4 * (lane >> 4) + reg, c = lane & 15;
#pragma unroll
        for (int mt = 0; mt < 3; ++mt) {
            float s = 0.f;
#pragma unroll
            for (int ww = 0; ww < 3; ++ww) s += *(LAS float*)(sm + ww * KG_WAVE + (mt * 64 + lane) * 16 + reg * 4);
            int ci2; bool ok = true;
            if (mt == 0) ci2 = 128 * (row >> 3) + (row & 7) + 8 * c;
            else if (mt == 1) ci2 = row < 8 ? 256 + 64 * (row >> 2) + (row & 3) + 4 * c : 384 + 32 * ((row - 8) >> 1) + (row & 1) + 2 * c;
            else { ci2 = 512 + 16 * row + c; ok = row < 8 || (row == 8 && c == 0); }
            if (ok) *(LAS unsigned short*)(sm + KG_KC + 2 * ci2) = (unsigned short)pk_bf16(s * unscale, 0.f);
        }
    }
    __syncthreads();
    if (tid < 81) *(u32x4*)(Kc + (size_t)d * KC_PITCH + 16 * tid) = *(LAS u32x4*)(sm + KG_KC + 16 * tid);
}

__global__ __launch_bounds__(256, 3) void k1_prep(const bf16_t* __restrict__ x, const bf16_t* __restrict__ logA, const bf16_t* __restrict__ Aim,
                                                 const bf16_t* __restrict__ Cre, const bf16_t* __restrict__ Cim, const bf16_t* __restrict__ logdt,
                                                 char* __restrict__ ws PROF_ARG) {
    extern __shared__ __attribute__((aligned(16))) char smem[];
    lptr sm = (lptr)smem;
    STAMP(0, 0);
    const int b = blockIdx.x;
    if (b < K1_NLN) ln1_block(b, x, ws, sm);
    else kgen_block(b - K1_NLN, logA, Aim, Cre, Cim, logdt, ws + OFF_KC, sm);
    STAMP(0, 15);
}

struct KTab { unsigned short v[2][2048]; };
constexpr int ci_of(int l) {
    if (l < 0) return 641;
    if (l < 256) return l;
    const int s = l < 512 ? 1 : (l < 1024 ? 2 : 3);
    return ((l + (1 << (s - 1)) - 1 + ((l >> s) & 1)) >> s) + 128 * s;
}
constexpr KTab make_tab() { KTab t{}; for (int c = 0; c < 2; ++c) for (int y = 0; y < 2048; ++y) t.v[c][y] = (unsigned short)(2 * ci_of(2047 - c - y)); return t; }
__device__ const __attribute__((aligned(16))) KTab g_ktab = make_tab();

constexpr int CV_KCH = 8320;
constexpr int CV_U = 4 * CV_KCH;
constexpr int CV_X = CV_U + 144 * 128;
constexpr int CV_KC = CV_X + 128 * 128;
constexpr int K2_LDS = 84 * 1024;

__device__ __forceinline__ void conv_block(int b, const char* __restrict__ ws, const bf16_t* __restrict__ Dskip, bf16_t* __restrict__ hT, lptr sm PROF_ARG) {
    const int tid = threadIdx.x, lane = tid & 63, w = uni(tid >> 6);
    const int d0 = 4 * b, d = d0 + w;
    const unsigned sm0 = (unsigned)(size_t)sm;
    const unsigned vo = (unsigned)((lane >> 3) * (D * 32) + d0 * 32 + (((lane & 7) ^ ((lane >> 4) << 1)) * 16));
    u32x4 tb[2][4];
#pragma unroll
    for (int c = 0; c < 2; ++c)
#pragma unroll
        for (int i = 0; i < 4; ++i) asm volatile("global_load_dwordx4 %0, %1, %2" : "=v"(tb[c][i]) : "v"(lane * 16), "s"((const char*)&g_ktab + c * 4096 + i * 1024) : "memory");
    const unsigned dsk_raw = ((const unsigned*)Dskip)[d >> 1];
#pragma unroll
    for (int j = 0; j < 2; ++j) dma16(ws + OFF_KC + (size_t)d * KC_PITCH + j * 1024, lane * 16, sm0 + CV_KC + w * 2048 + j * 1024);
#pragma unroll
    for (int j = 0; j < 4; ++j) dma16(ws + OFF_U + (size_t)(4 * w + j) * (8 * D * 32), vo, sm0 + CV_U + 2048 + (4 * w + j) * 1024);
    const u32x4 z4 = {0u, 0u, 0u, 0u};
    if (tid < 128) *(LAS u32x4*)(sm + CV_U + tid * 16) = z4;
    if (lane < 8) *(LAS u32x4*)(sm + w * CV_KCH + (lane >> 2) * 4160 + 4096 + (lane & 3) * 16) = z4;
    STAMP(1, 1);
    asm volatile("s_waitcnt vmcnt(4)" : "+v"(tb[0][0]), "+v"(tb[0][1]), "+v"(tb[0][2]), "+v"(tb[0][3]), "+v"(tb[1][0]), "+v"(tb[1][1]), "+v"(tb[1][2]), "+v"(tb[1][3]) :: "memory");
    {
        lptr kc = sm + CV_KC + w * 2048;
        u32x4 o[2][4];
#pragma unroll
        for (int c = 0; c < 2; ++c)
#pragma unroll
            for (int i = 0; i < 4; ++i)
#pragma unroll
                for (int k = 0; k < 4; ++k) {
                    const unsigned tk = tb[c][i][k];
                    const unsigned lo = *(LAS unsigned short*)(kc + (tk & 0xffffu)), hi = *(LAS unsigned short*)(kc + (tk >> 16));
                    o[c][i][k] = lo | (hi << 16);
                }
#pragma unroll
        for (int c = 0; c < 2; ++c)
#pragma unroll
            for (int i = 0; i < 4; ++i) *(LAS u32x4*)(sm + w * CV_KCH + c * 4160 + (lane + 64 * i) * 16) = o[c][i];
    }
    STAMP(1, 6);
    wait_vm<0>();
    bar();
    STAMP(1, 2); CSTAMP(1, 4);
    const int n = lane & 15, h = lane >> 4;
    const lptr fb = sm + w * CV_KCH + ((n & 1) ? 0 : 4160) + 4 * (((2031 - n) >> 1) + 4 * h) - 512 * 7;
    f32x4 acc[8] = {};
    bf16x8 Fa[2][8], Ga[2][8];
    lptr gp, fp[4];
    auto addr = [&](int rr) {
        const int ln = n + (h >> 1) + 15 - 2 * rr;
        gp = sm + CV_U + 128 * ln + 16 * ((2 * w + (h & 1)) ^ (((ln >> 1) & 3) << 1));
#pragma unroll
        for (int k = 0; k < 4; ++k) { fp[k] = fb - 64 * rr + 1024 * k; asm volatile("" : "+v"(fp[k])); }
    };
    auto ldG = [&](int P, int c) { Ga[P][c] = *(LAS bf16x8*)(gp + 2048 * c); };
    u32x4 ftmp[2];
    auto ldF = [&](int P, int a, int half) {
        lptr q = fp[(7 - a) >> 1] + 512 * ((7 - a) & 1) + 8 * half;
        ftmp[a & 1][2 * half] = *(LAS unsigned*)q; ftmp[a & 1][2 * half + 1] = *(LAS unsigned*)(q + 4);
        if (half) Fa[P][a] = __builtin_bit_cast(bf16x8, ftmp[a & 1]);
    };
    addr(0);
#pragma unroll
    for (int c = 0; c < 8; ++c) { ldF(0, c, 0); ldF(0, c, 1); ldG(0, c); }
    SB();
#pragma unroll
    for (int rr = 0; rr < 8; ++rr) {
        const int P = rr & 1;
        int i = 0;
#pragma unroll
        for (int a = 0; a < 8; ++a)
#pragma unroll
            for (int c = a; c < 8; ++c) {
                acc[c] = __builtin_amdgcn_mfma_f32_16x16x32_bf16(Fa[P][a], Ga[P][c - a], acc[c], 0, 0, 0); PIN(acc[c]); SB();
                if (rr < 7) {
                    if (i == 0) { addr(rr + 1); SB(); }
                    else if (i <= 24) { const int k = (i - 1) / 3, m = (i - 1) % 3; if (m == 0) ldG(P ^ 1, k); else ldF(P ^ 1, k, m - 1); SB(); }
                }
                if (rr == 0 && i >= 26 && i < 34 && !(i & 1))
                    dma16(ws + OFF_X + (size_t)(4 * w + ((i - 26) >> 1)) * (8 * D * 32), vo, sm0 + CV_X + (4 * w + ((i - 26) >> 1)) * 1024);
                ++i;
            }
    }
    STAMP(1, 3); CSTAMP(1, 5);
    asm volatile("" ::: "memory");
    lptr yb = sm + w * CV_KCH;
#pragma unroll
    for (int c = 0; c < 8; ++c) {
        const u32x2 yv = {pk_bf16(acc[c][0], acc[c][1]), pk_bf16(acc[c][2], acc[c][3])};
        *(LAS u32x2*)(yb + (256 * c + 16 * n + 4 * h) * 2) = yv;
    }
    wait_vm<0>();
    bar();
    const float dsk = (d & 1) ? bfhi(dsk_raw) : bflo(dsk_raw);
    const f32x2 dk2 = {dsk, dsk};
#pragma unroll
    for (int j = 0; j < 4; ++j) {
        const int gm = 64 * j + lane, ln = gm >> 1;
        const int so = ln * 128 + 16 * ((2 * w + (gm & 1)) ^ (((ln >> 1) & 3) << 1));
        const u32x4 yv = *(LAS u32x4*)(yb + gm * 16), uv = *(LAS u32x4*)(sm + CV_U + 2048 + so), xv = *(LAS u32x4*)(sm + CV_X + so);
        u32x4 o;
#pragma unroll
        for (int i = 0; i < 4; ++i) {
            const f32x2 sf = (f32x2){bflo(uv[i]), bfhi(uv[i])} * dk2;
            const unsigned sk = pk_bf16(sf[0], sf[1]);
            const f32x2 zf = (f32x2){bflo(yv[i]), bfhi(yv[i])} + (f32x2){bflo(sk), bfhi(sk)};
            const unsigned zz = pk_bf16(zf[0], zf[1]);
            const f32x2 hf = (f32x2){bflo(xv[i]), bfhi(xv[i])} + (f32x2){bflo(zz), bfhi(zz)};
            o[i] = pk_bf16(hf[0], hf[1]);
        }
        *(u32x4*)(hT + (size_t)d * T + gm * 8) = o;
    }
}

__device__ __forceinline__ void spare_delay() {
#define SPARE_DELAY 30
    const unsigned long long t0 = wall_clock64();
    while (wall_clock64() - t0 < SPARE_DELAY) __builtin_amdgcn_s_sleep(4);
}

__device__ __forceinline__ void wgtile_block(int b, const bf16_t* __restrict__ wg, const bf16_t* __restrict__ wu, char* __restrict__ wgf, float* __restrict__ cs, lptr sm) {
    const int tid = threadIdx.x, lane = tid & 63, w = uni(tid >> 6);
    const int j = b >> 1, kh = b & 1;
    const int c16 = tid & 15, r0 = tid >> 4;
    spare_delay();
    u32x4 v[20];
    const bf16_t* src = (c16 < 8 ? wg : wu) + (size_t)(320 * kh + r0) * F + 64 * j + (c16 & 7) * 8;
#pragma unroll
    for (int i = 0; i < 20; ++i) v[i] = *(const u32x4*)(src + (size_t)(16 * i) * F);
    f32x2 a[4] = {};
#pragma unroll
    for (int i = 0; i < 20; ++i) {
        const int k = 16 * i + r0;
        *(LAS u32x4*)(sm + k * 256 + (((c16 >> 1) ^ (k & 3)) * 32) + (c16 & 1) * 16) = v[i];
#pragma unroll
        for (int e = 0; e < 4; ++e) a[e] += (f32x2){bflo(v[i][e]), bfhi(v[i][e])};
    }
#pragma unroll
    for (int e = 0; e < 4; ++e)
#pragma unroll
        for (int hf = 0; hf < 2; ++hf) {
            const float s = xsum16_32(a[e][hf]);
            if (lane < 16) *(LAS float*)(sm + 81920 + (w * 128 + c16 * 8 + 2 * e + hf) * 4) = s;
        }
    __syncthreads();
    if (tid < 128) {
        const float s = (*(LAS float*)(sm + 81920 + tid * 4) + *(LAS float*)(sm + 81920 + (128 + tid) * 4)) + (*(LAS float*)(sm + 81920 + (256 + tid) * 4) + *(LAS float*)(sm + 81920 + (384 + tid) * 4));
        cs[kh * 2 * F + (tid >> 6) * F + 64 * j + (tid & 63)] = s;
    }
    const int g = lane >> 4, q = (lane & 15) >> 2, p = lane & 3;
#pragma unroll
    for (int i = 0; i < 20; ++i) {
        const int ftl = w, sub = i;
        const int k = 16 * sub + 4 * (g >> 1) + q;
        lptr a0 = sm + k * 256 + ((((g & 1) * 4 + ftl) ^ q) * 32) + p * 8;
        const int ftg = 4 * j + ftl, cb = ftg / 6, ft = ftg % 6;
        *(u32x4*)(wgf + ((((size_t)cb * 40 + 20 * kh + sub) * 6 + ft) * 64 + lane) * 16) = tr_read2(a0, 8 * 256);
    }
}

__device__ __forceinline__ void wdtile_block(int fb, const bf16_t* __restrict__ wd, char* __restrict__ wdf, lptr sm) {
    const int tid = threadIdx.x, lane = tid & 63, w = uni(tid >> 6);
    spare_delay();
    u32x4 v[10];
#pragma unroll
    for (int i = 0; i < 10; ++i) { const int ch = tid + 256 * i, row = ch / 80, c = ch % 80; v[i] = *(const u32x4*)(wd + (size_t)(32 * fb + row) * D + c * 8); }
#pragma unroll
    for (int i = 0; i < 10; ++i) { const int ch = tid + 256 * i, row = ch / 80, c = ch % 80; *(LAS u32x4*)(sm + row * LN_ROW + c * 16) = v[i]; }
    __syncthreads();
    const int g = lane >> 4, q = (lane & 15) >> 2, p = lane & 3;
#pragma unroll
    for (int i = 0; i < 10; ++i) {
        const int dt = w + 4 * i;
        lptr a = sm + (16 * (g >> 1) + 4 * (g & 1) + q) * LN_ROW + (16 * dt + 4 * p) * 2;
        *(u32x4*)(wdf + (((size_t)fb * 40 + dt) * 64 + lane) * 16) = tr_read2(a, 8 * LN_ROW);
    }
}

__global__ __launch_bounds__(256, 2) void k2_conv(char* __restrict__ ws, const bf16_t* __restrict__ Dskip, const bf16_t* __restrict__ wg,
                                                 const bf16_t* __restrict__ wu, const bf16_t* __restrict__ wd PROF_ARG) {
    extern __shared__ __attribute__((aligned(16))) char smem[];
    lptr sm = (lptr)smem;
    STAMP(1, 0);
    const int b = blockIdx.x;
    if (b < 160) conv_block(b, ws, Dskip, (bf16_t*)(ws + OFF_HT), sm PROF_FWD);
    else if (b < 208) wgtile_block(b - 160, wg, wu, ws + OFF_WG, (float*)(ws + OFF_CS), sm);
    else wdtile_block(b - 208, wd, ws + OFF_WD, sm);
    STAMP(1, 15);
}

#define G1_PRE 4
constexpr int G1_NS = 7, G1_SS = 20480, G1_W = 8192;
constexpr int G1_CS = G1_NS * G1_SS;
constexpr int G1_ST = G1_CS + 2048;
constexpr int K3_LDS = G1_ST + 4096;

template <int WC>
struct G1Compute {
    f32x16 acc[3][2];
    bf16x8 Af[2][3], Bf[2][2];
    float s0, s1, q0, q1;
    template <int P> __device__ __forceinline__ void st(int i) {
        const u32x4 sv = __builtin_bit_cast(u32x4, Bf[P][WC]);
        const unsigned e = sv[i];
        const float lo = bflo(e), hi = bfhi(e);
        asm volatile("v_add_f32 %0, %0, %4\n\tv_add_f32 %1, %1, %5\n\tv_fmac_f32 %2, %4, %4\n\tv_fmac_f32 %3, %5, %5"
                     : "+v"(s0), "+v"(s1), "+v"(q0), "+v"(q1) : "v"(lo), "v"(hi));
        SB();
    }
    template <int P> __device__ __forceinline__ void mm(int ft, int tt) {
        acc[ft][tt] = __builtin_amdgcn_mfma_f32_32x32x16_bf16(Af[P][ft], Bf[P][tt], acc[ft][tt], 0, 0, 0); PIN(acc[ft][tt]); SB();
    }
    __device__ __forceinline__ static bf16x8 rdB(lptr p) { return __builtin_bit_cast(bf16x8, tr_read2(p, 8 * 256)); }
    template <int P, int KK, bool MORE, bool BAR>
    __device__ __forceinline__ void substep(lptr pa, const lptr (&pb)[2]) {
        mm<P>(0, 0);
        if (BAR) { sbar(); SB(); }
        if (MORE) { Bf[P ^ 1][0] = rdB(pb[0] + KK * 16 * 256); Af[P ^ 1][0] = *(LAS bf16x8*)(pa + KK * 6144); SB(); }
        mm<P>(0, 1);
        if (MORE) { Bf[P ^ 1][1] = rdB(pb[1] + KK * 16 * 256); Af[P ^ 1][1] = *(LAS bf16x8*)(pa + KK * 6144 + 1024); SB(); }
        mm<P>(1, 0);
        if (MORE) { Af[P ^ 1][2] = *(LAS bf16x8*)(pa + KK * 6144 + 2048); SB(); }
        st<P>(0);
        mm<P>(1, 1);
        st<P>(1);
        mm<P>(2, 0);
        st<P>(2);
        mm<P>(2, 1);
        st<P>(3);
    }
};

template <int WC>
__device__ __forceinline__ void g1_compute(lptr sm, char* __restrict__ ws, int wr, int lane, int t0, int cb PROF_ARG) {
    const int g = lane >> 4, q = (lane & 15) >> 2, p = lane & 3, hh = g >> 1, krow = 4 * hh + q;
    G1Compute<WC> C;
#pragma unroll
    for (int ft = 0; ft < 3; ++ft)
#pragma unroll
        for (int tt = 0; tt < 2; ++tt) C.acc[ft][tt] = (f32x16){};
    C.s0 = C.s1 = C.q0 = C.q1 = 0.f;
    const lptr a0 = sm + G1_W + 3 * WC * 1024 + lane * 16;
    lptr b0[2];
#pragma unroll
    for (int tt = 0; tt < 2; ++tt) b0[tt] = sm + krow * 256 + (((2 * wr + tt) ^ q) * 64) + (g & 1) * 32 + p * 8;
    sbar();
    STAMP(2, 2); CSTAMP(2, 4);
#pragma unroll
    for (int tt = 0; tt < 2; ++tt) C.Bf[0][tt] = C.rdB(b0[tt]);
#pragma unroll
    for (int ft = 0; ft < 3; ++ft) C.Af[0][ft] = *(LAS bf16x8*)(a0 + ft * 1024);
    SB();
    lptr pa = a0, pb[2] = {b0[0], b0[1]};
    int nb = 0;
#pragma unroll 1
    for (int s = 0; s < 19; ++s) {
        C.template substep<0, 1, true, false>(pa, pb);
        nb = nb + G1_SS == G1_NS * G1_SS ? 0 : nb + G1_SS;
        pa = a0 + nb; pb[0] = b0[0] + nb; pb[1] = b0[1] + nb;
        SB();
        C.template substep<1, 0, true, true>(pa, pb);
    }
    C.template substep<0, 1, true, false>(pa, pb);
    C.template substep<1, 0, false, false>(pa, pb);
    STAMP(2, 3); CSTAMP(2, 5);
    {
        const float a = C.s0 + C.s1, c = C.q0 + C.q1;
        const auto ra = __builtin_amdgcn_permlane32_swap(__float_as_uint(a), __float_as_uint(a), false, false);
        const auto rc = __builtin_amdgcn_permlane32_swap(__float_as_uint(c), __float_as_uint(c), false, false);
        if (lane < 32) *(LAS f32x2*)(sm + G1_ST + (64 * wr + 32 * WC + lane) * 8) = (f32x2){__uint_as_float(ra[0]) + __uint_as_float(ra[1]), __uint_as_float(rc[0]) + __uint_as_float(rc[1])};
    }
    bar();
    char* mF = ws + OFF_MF;
#pragma unroll
    for (int tt = 0; tt < 2; ++tt) {
        const int tl = 32 * (2 * wr + tt) + (lane & 31);
        const f32x2 sq = *(LAS f32x2*)(sm + G1_ST + tl * 8);
        const float mean = sq[0] * (1.0f / (float)D);
        const float var = fmaxf(sq[1] * (1.0f / (float)D) - mean * mean, 0.f);
        const float sd = rbf(sqrtf(rbf(rbf(var) + 1e-5f)));
        const float rs = __builtin_amdgcn_rcpf(sd), mrs = rbf(mean) * rs;
        const f32x2 rs2 = {rs, rs}, nm2 = {-mrs, -mrs};
        const int tg = t0 + tl;
#pragma unroll
        for (int ft = 0; ft < 3; ++ft) {
            const int fl = 16 * (3 * WC + ft) + 4 * hh;
            lptr cp = sm + G1_CS + fl * 4;
            const f32x4 cg0 = *(LAS f32x4*)cp + *(LAS f32x4*)(cp + 1024), cg1 = *(LAS f32x4*)(cp + 32) + *(LAS f32x4*)(cp + 1024 + 32);
            const f32x4 cu0 = *(LAS f32x4*)(cp + 384) + *(LAS f32x4*)(cp + 1024 + 384), cu1 = *(LAS f32x4*)(cp + 416) + *(LAS f32x4*)(cp + 1024 + 416);
            u32x4 o;
#pragma unroll
            for (int r = 0; r < 4; ++r) {
                const f32x2 cg = r < 2 ? (f32x2){cg0[2 * r], cg0[2 * r + 1]} : (f32x2){cg1[2 * r - 4], cg1[2 * r - 3]};
                const f32x2 cu = r < 2 ? (f32x2){cu0[2 * r], cu0[2 * r + 1]} : (f32x2){cu1[2 * r - 4], cu1[2 * r - 3]};
                const f32x2 ag = {C.acc[ft][tt][2 * r], C.acc[ft][tt][2 * r + 1]}, au = {C.acc[ft][tt][8 + 2 * r], C.acc[ft][tt][9 + 2 * r]};
                const f32x2 gv = ag * rs2 + nm2 * cg, uv = au * rs2 + nm2 * cu;
                const f32x2 ea = gv * (gv * gv * -0.1029432397f + -2.302208198f);
                const f32x2 den = (f32x2){__builtin_amdgcn_exp2f(ea[0]), __builtin_amdgcn_exp2f(ea[1])} + 1.0f;
                const f32x2 mv = gv * uv * (f32x2){__builtin_amdgcn_rcpf(den[0]), __builtin_amdgcn_rcpf(den[1])};
                o[r] = pk_bf16(mv[0], mv[1]);
            }
            const int ftg = 6 * cb + 3 * WC + ft;
            const int lp = (tg & 15) + 16 * (2 * (ftg & 1) + hh);
            *(u32x4*)(mF + (((size_t)(tg >> 4) * 48 + (ftg >> 1)) * 64 + lp) * 16) = o;
        }
    }
    STAMP(2, 15);
}

__global__ __launch_bounds__(512, 2) void k3_gemm1(char* __restrict__ ws PROF_ARG) {
    extern __shared__ __attribute__((aligned(16))) char smem[];
    lptr sm = (lptr)smem;
    STAMP(2, 0);
    const int tid = threadIdx.x, lane = tid & 63, w = uni(tid >> 6);
    const unsigned sm0 = (unsigned)(size_t)sm;
    const int grp = blockIdx.x & 7, idx = blockIdx.x >> 3;
    const int rb = (grp & 1) * 8 + (idx & 7), cb = (grp >> 1) * 4 + (idx >> 3);
    const int t0 = 128 * rb, f0 = 96 * cb;
    if (w >= 4) {
        const int lw = w & 3;
        const char* hT = ws + OFF_HT;
        const char* wsrc = ws + OFF_WG + (size_t)cb * 40 * 6144 + 3 * lw * 1024;
        unsigned vh[2];
#pragma unroll
        for (int j = 0; j < 2; ++j) {
            const int o = (2 * lw + j) * 1024 + lane * 16, k = o >> 8, c = ((o >> 4) & 15) ^ ((k & 3) << 2);
            vh[j] = (unsigned)(k * (T * 2) + t0 * 2 + c * 16);
        }
        const unsigned vl = lane * 16;
        auto stage = [&](int s) {
            const unsigned lb = sm0 + (s % G1_NS) * G1_SS;
            const char* hs = hT + (size_t)s * 32 * (T * 2);
            const char* wsx = wsrc + (size_t)s * 12288;
            dma16(hs, vh[0], lb + (2 * lw) * 1024);
            dma16(wsx, vl, lb + G1_W + (3 * lw) * 1024);
            dma16(hs, vh[1], lb + (2 * lw + 1) * 1024);
            dma16(wsx + 1024, vl, lb + G1_W + (3 * lw + 1) * 1024);
            dma16(wsx + 2048, vl, lb + G1_W + (3 * lw + 2) * 1024);
        };
        if (lw < 3) {
            const unsigned co = (unsigned)(((lw == 0 ? f0 + lane : (lw == 1 ? (lane < 32 ? f0 + 64 + lane : F + f0 + lane - 32) : F + f0 + 32 + lane))) * 4);
            dma4(ws + OFF_CS, co, sm0 + G1_CS + lw * 256);
            dma4(ws + OFF_CS + 2 * F * 4, co, sm0 + G1_CS + 1024 + lw * 256);
        }
#pragma unroll
        for (int s = 0; s < G1_PRE; ++s) stage(s);
        wait_vm<5 * (G1_PRE - 1)>();
        sbar();
#pragma unroll
        for (int s = 0; s < 19; ++s) {
            const int last = (s + G1_PRE - 1) < 19 ? (s + G1_PRE - 1) : 19;
            wait_vm_rt<30>(5 * (last - (s + 1)));
            sbar();
            if (s + G1_PRE < 20) stage(s + G1_PRE);
        }
        sbar();
        return;
    }
    if (w & 1) g1_compute<1>(sm, ws, w >> 1, lane, t0, cb PROF_FWD); else g1_compute<0>(sm, ws, w >> 1, lane, t0, cb PROF_FWD);
}

constexpr int G2_NS = 4, G2_SS = 36 * 1024, G2_W = 16 * 1024;
constexpr int G2_H = G2_NS * G2_SS;
constexpr int K4_LDS = G2_H + 10240;

template <int ND>
__device__ __forceinline__ void g2_compute(lptr sm, int lane, int th, int dl0, int t0, int d0, bf16_t* __restrict__ out PROF_ARG) {
    f32x4 acc[ND][2] = {};
    bf16x8 A[2][ND], B[2][2];
    const lptr pb0 = sm + lane * 16 + 2 * th * 4096, pa0 = sm + lane * 16 + G2_W + dl0 * 1024;
    auto ld = [&](int P, lptr pa, lptr pb, int fl) {
#pragma unroll
        for (int tt = 0; tt < 2; ++tt) B[P][tt] = *(LAS bf16x8*)(pb + (tt * 4 + fl) * 1024);
#pragma unroll
        for (int dt = 0; dt < ND; ++dt) A[P][dt] = *(LAS bf16x8*)(pa + (fl * 5 + dt) * 1024);
        SB();
    };
    auto mm = [&](int P) {
#pragma unroll
        for (int dt = 0; dt < ND; ++dt)
#pragma unroll
            for (int tt = 0; tt < 2; ++tt) { acc[dt][tt] = __builtin_amdgcn_mfma_f32_16x16x32_bf16(A[P][dt], B[P][tt], acc[dt][tt], 0, 0, 0); PIN(acc[dt][tt]); SB(); }
    };
    sbar();
    STAMP(3, 2); CSTAMP(3, 4);
    ld(0, pa0, pb0, 0);
    lptr pa = pa0, pb = pb0;
    int nb = 0;
#pragma unroll 1
    for (int s = 0; s < 12; ++s) {
        ld(1, pa, pb, 1); mm(0);
        ld(0, pa, pb, 2); mm(1);
        ld(1, pa, pb, 3); mm(0);
        if (s < 11) {
            nb = nb + G2_SS == G2_NS * G2_SS ? 0 : nb + G2_SS;
            pa = pa0 + nb; pb = pb0 + nb;
            bar();
            ld(0, pa, pb, 0);
        }
        mm(1);
    }
    STAMP(3, 3); CSTAMP(3, 5);
    bar();
    const int g = lane >> 4, q = (lane & 15) >> 2, p = lane & 3;
#pragma unroll
    for (int dt = 0; dt < ND; ++dt)
#pragma unroll
        for (int tt = 0; tt < 2; ++tt) {
            const int row = 16 * (dl0 + dt) + 4 * g + q, seg = (2 * th + tt) ^ ((row >> 1) & 3);
            const u32x2 hv = tr_read(sm + G2_H + row * 128 + seg * 32 + p * 8);
            const unsigned o01 = pk_bf16(acc[dt][tt][0], acc[dt][tt][1]), o23 = pk_bf16(acc[dt][tt][2], acc[dt][tt][3]);
            const u32x2 r = {pk_bf16(bflo(hv[0]) + bflo(o01), bfhi(hv[0]) + bfhi(o01)), pk_bf16(bflo(hv[1]) + bflo(o23), bfhi(hv[1]) + bfhi(o23))};
            *(LAS u32x2*)(sm + (16 * (2 * th + tt) + (lane & 15)) * 176 + (16 * (dl0 + dt) + 4 * g) * 2) = r;
        }
}

__global__ __launch_bounds__(512, 2) void k4_gemm2(char* __restrict__ ws, bf16_t* __restrict__ out PROF_ARG) {
    extern __shared__ __attribute__((aligned(16))) char smem[];
    lptr sm = (lptr)smem;
    STAMP(3, 0);
    const int tid = threadIdx.x, lane = tid & 63, w = uni(tid >> 6);
    const unsigned sm0 = (unsigned)(size_t)sm;
    const int grp = blockIdx.x & 7, idx = blockIdx.x >> 3;
    const int tb = (grp >> 1) * 8 + (idx & 7), db = (grp & 1) * 4 + (idx >> 3);
    const int t0 = 64 * tb, d0 = 80 * db;
    if (w >= 4) {
        const int lw = w & 3;
        const char* ms = ws + OFF_MF + (size_t)(4 * tb + lw) * 48 * 1024;
        const char* wsrc = ws + OFF_WD + (size_t)lw * (40 * 1024) + (size_t)(5 * db) * 1024;
        const unsigned vl = lane * 16;
        auto stage = [&](int s) {
            const unsigned lb = sm0 + (s % G2_NS) * G2_SS;
#pragma unroll
            for (int j = 0; j < 4; ++j) {
                dma16(ms + (size_t)(4 * s + j) * 1024, vl, lb + (4 * lw + j) * 1024);
                dma16(wsrc + (size_t)(4 * s) * (40 * 1024) + j * 1024, vl, lb + G2_W + (5 * lw + j) * 1024);
            }
            dma16(wsrc + (size_t)(4 * s) * (40 * 1024) + 4 * 1024, vl, lb + G2_W + (5 * lw + 4) * 1024);
        };
        stage(0);
#pragma unroll
        for (int j = 0; j < 3; ++j) {
            const int i = lw + 4 * j < 10 ? lw + 4 * j : lw;
            const int o = i * 1024 + lane * 16, row = o >> 7, c = ((o >> 4) & 7) ^ (((row >> 1) & 3) << 1);
            dma16(ws + OFF_HT, (unsigned)((d0 + row) * (T * 2) + t0 * 2 + c * 16), sm0 + G2_H + i * 1024);
        }
        stage(1); stage(2);
        wait_vm<21>();
        sbar();
#pragma unroll
        for (int s = 0; s < 11; ++s) {
            if (s <= 9) wait_vm<9>(); else wait_vm<0>();
            sbar();
            if (s + 3 < 12) stage(s + 3);
        }
        sbar();
    } else {
        const int th = w & 1;
        if (w >> 1) g2_compute<2>(sm, lane, th, 3, t0, d0, out PROF_FWD); else g2_compute<3>(sm, lane, th, 0, t0, d0, out PROF_FWD);
    }
    bar();
    for (int ch = tid; ch < 640; ch += 512) {
        const int row = ch / 10, c = ch % 10;
        *(u32x4*)(out + (size_t)(t0 + row) * D + d0 + c * 8) = *(LAS u32x4*)(sm + row * 176 + c * 16);
    }
    STAMP(3, 15);
}

extern "C" void kernel_launch(void* const* d_in, const int* in_sizes, int n_in, void* d_out, int out_size, void* d_ws, size_t ws_size, hipStream_t stream) {
    const bf16_t* x = (const bf16_t*)d_in[0];
    const bf16_t* logA = (const bf16_t*)d_in[1];
    const bf16_t* Aim = (const bf16_t*)d_in[2];
    const bf16_t* Cre = (const bf16_t*)d_in[3];
    const bf16_t* Cim = (const bf16_t*)d_in[4];
    const bf16_t* logdt = (const bf16_t*)d_in[5];
    const bf16_t* Dskip = (const bf16_t*)d_in[6];
    const bf16_t* wg = (const bf16_t*)d_in[8];
    const bf16_t* wu = (const bf16_t*)d_in[9];
    const bf16_t* wd = (const bf16_t*)d_in[10];
    char* ws = (char*)d_ws;
    (void)hipFuncSetAttribute((const void*)k1_prep, hipFuncAttributeMaxDynamicSharedMemorySize, K1_LDS);
    (void)hipFuncSetAttribute((const void*)k2_conv, hipFuncAttributeMaxDynamicSharedMemorySize, K2_LDS);
    (void)hipFuncSetAttribute((const void*)k3_gemm1, hipFuncAttributeMaxDynamicSharedMemorySize, K3_LDS);
    (void)hipFuncSetAttribute((const void*)k4_gemm2, hipFuncAttributeMaxDynamicSharedMemorySize, K4_LDS);
    k1_prep<<<K1_NLN + K1_NKG, 256, K1_LDS, stream>>>(x, logA, Aim, Cre, Cim, logdt, ws PROF_PASS);
    k2_conv<<<256, 256, K2_LDS, stream>>>(ws, Dskip, wg, wu, wd PROF_PASS);
    k3_gemm1<<<256, 512, K3_LDS, stream>>>(ws PROF_PASS);
    k4_gemm2<<<256, 512, K4_LDS, stream>>>(ws, (bf16_t*)d_out PROF_PASS);
}
```

```cpp
#include <hip/hip_runtime.h>
#include <stdint.h>

typedef unsigned short bf16_t;
typedef short s16x4 __attribute__((ext_vector_type(4)));
typedef __bf16 bf16x8 __attribute__((ext_vector_type(8)));
typedef __bf16 bf16x2 __attribute__((ext_vector_type(2)));
typedef _Float16 h16x8 __attribute__((ext_vector_type(8)));
typedef _Float16 h16x2 __attribute__((ext_vector_type(2)));
typedef float f32x2 __attribute__((ext_vector_type(2)));
typedef float f32x4 __attribute__((ext_vector_type(4)));
typedef float f32x16 __attribute__((ext_vector_type(16)));
typedef unsigned u32x2 __attribute__((ext_vector_type(2)));
typedef unsigned u32x4 __attribute__((ext_vector_type(4)));
#define LAS __attribute__((address_space(3)))
typedef LAS char* lptr;

constexpr int T = 2048, D = 640, NM = 192, F = 1536;
constexpr int KC_PITCH = 1296;
constexpr size_t OFF_U = 0;
constexpr size_t OFF_X = OFF_U + (size_t)T * D * 2;
constexpr size_t OFF_KC = OFF_X + (size_t)T * D * 2;
constexpr size_t OFF_HT = OFF_KC + (size_t)D * KC_PITCH + 4096;
constexpr size_t OFF_CS = OFF_HT + (size_t)T * D * 2;
constexpr size_t OFF_MF = OFF_CS + 32768;
constexpr size_t OFF_WD = OFF_MF + (size_t)T * F * 2;
constexpr size_t OFF_WG = OFF_WD + (size_t)F * D * 2;
constexpr size_t OFF_ST = OFF_WG + (size_t)2 * F * D * 2;
constexpr size_t OFF_PROF = OFF_ST + (size_t)T * 8;

#define STAMP(kid, i)
#define CSTAMP(kid, i)
#define PROF_ARG
#define PROF_PASS
#define PROF_FWD

__device__ __forceinline__ float bf2f(unsigned v) { return __uint_as_float(v << 16); }
__device__ __forceinline__ float bflo(unsigned v) { return __uint_as_float(v << 16); }
__device__ __forceinline__ float bfhi(unsigned v) { return __uint_as_float(v & 0xffff0000u); }
__device__ __forceinline__ unsigned pk_bf16(float lo, float hi) { f32x2 v = {lo, hi}; return __builtin_bit_cast(unsigned, __builtin_convertvector(v, bf16x2)); }
__device__ __forceinline__ float rbf(float f) { return bflo(pk_bf16(f, f)); }
__device__ __forceinline__ unsigned pk_f16(f32x2 v) { return __builtin_bit_cast(unsigned, __builtin_convertvector(v, h16x2)); }
template <int CTRL> __device__ __forceinline__ float dppf(float x) { return __int_as_float(__builtin_amdgcn_update_dpp(0, __float_as_int(x), CTRL, 0xF, 0xF, true)); }
__device__ __forceinline__ float row16_sum(float x) { x += dppf<0xB1>(x); x += dppf<0x4E>(x); x += dppf<0x141>(x); x += dppf<0x140>(x); return x; }
__device__ __forceinline__ float row16_max(float x) { x = fmaxf(x, dppf<0xB1>(x)); x = fmaxf(x, dppf<0x4E>(x)); x = fmaxf(x, dppf<0x141>(x)); x = fmaxf(x, dppf<0x140>(x)); return x; }
__device__ __forceinline__ float xsum16_32(float x) {
    const unsigned u = __float_as_uint(x);
    const auto r = __builtin_amdgcn_permlane16_swap(u, u, false, false);
    const unsigned v = __float_as_uint(__uint_as_float(r[0]) + __uint_as_float(r[1]));
    const auto q = __builtin_amdgcn_permlane32_swap(v, v, false, false);
    return __uint_as_float(q[0]) + __uint_as_float(q[1]);
}
__device__ __forceinline__ u32x2 tr_read(lptr p) { return __builtin_bit_cast(u32x2, __builtin_amdgcn_ds_read_tr16_b64_v4i16((LAS s16x4*)p)); }
__device__ __forceinline__ u32x4 tr_read2(lptr p, int off2) { const u32x2 r1 = tr_read(p), r2 = tr_read(p + off2); return (u32x4){r1[0], r1[1], r2[0], r2[1]}; }
__device__ __forceinline__ int uni(int v) { return __builtin_amdgcn_readfirstlane(v); }

__device__ __forceinline__ void dma16(const void* sbase, unsigned voff, unsigned ldsaddr) {
    asm volatile("s_mov_b32 m0, %2\n\ts_nop 0\n\tglobal_load_lds_dwordx4 %0, %1" :: "v"(voff), "s"(sbase), "s"(ldsaddr) : "memory");
}
__device__ __forceinline__ void dma4(const void* sbase, unsigned voff, unsigned ldsaddr) {
    asm volatile("s_mov_b32 m0, %2\n\ts_nop 0\n\tglobal_load_lds_dword %0, %1" :: "v"(voff), "s"(sbase), "s"(ldsaddr) : "memory");
}
template <int N> __device__ __forceinline__ void wait_vm() { asm volatile("s_waitcnt vmcnt(%0)" :: "n"(N) : "memory"); }
template <int N> __device__ __forceinline__ void wait_vm_rt(int n) {
    if (n >= N) wait_vm<N>(); else if constexpr (N > 0) wait_vm_rt<N - 1>(n);
}
__device__ __forceinline__ void bar() { asm volatile("s_waitcnt lgkmcnt(0)\n\ts_barrier" ::: "memory"); }
__device__ __forceinline__ void sbar() { asm volatile("s_barrier" ::: "memory"); }
#define SB() __builtin_amdgcn_sched_barrier(0)
#define PIN(x) asm volatile("" : "+v"(x))
#define ST_MODE 0
__device__ __forceinline__ void st_out(u32x4* p, u32x4 v) {
    *p = v;
}
#define ST_OUT(p, v) st_out(p, v)

constexpr int LN_ROW = 1344;
constexpr int KG_ROW = 272;
constexpr int KG_WAVE = 57 * KG_ROW;
constexpr int KG_KC = 3 * KG_WAVE + 7 * KG_ROW;
constexpr int K1_LDS = KG_KC + 1536;
constexpr int K1_NLN = 128, K1_NKG = D;

__device__ __forceinline__ void ln1_block(int b, const bf16_t* __restrict__ x, char* __restrict__ ws, lptr sm) {
    const int tid = threadIdx.x, r = tid >> 4, l16 = tid & 15, lane = tid & 63, w = uni(tid >> 6);
    const int t = 16 * b + r;
    u32x4 v[5];
#pragma unroll
    for (int j = 0; j < 5; ++j) v[j] = *(const u32x4*)(x + (size_t)t * D + (l16 + 16 * j) * 8);
    if (tid < 8) *(u32x4*)(ws + OFF_ST + (size_t)16 * b * 8 + tid * 16) = (u32x4){0u, 0u, 0u, 0u};
    f32x2 f[20];
    f32x2 s2 = {0.f, 0.f};
#pragma unroll
    for (int j = 0; j < 5; ++j)
#pragma unroll
        for (int i = 0; i < 4; ++i) { f[4 * j + i] = (f32x2){bflo(v[j][i]), bfhi(v[j][i])}; s2 += f[4 * j + i]; }
    const float mean = row16_sum(s2[0] + s2[1]) / (float)D;
    f32x2 q2 = {0.f, 0.f};
#pragma unroll
    for (int i = 0; i < 20; ++i) { const f32x2 dv = f[i] - mean; q2 += dv * dv; }
    const float var = row16_sum(q2[0] + q2[1]) / (float)D;
    const float m = rbf(mean);
    const float rs = 1.0f / rbf(sqrtf(rbf(rbf(var) + 1e-5f)));
#pragma unroll
    for (int j = 0; j < 5; ++j) {
        u32x4 o;
#pragma unroll
        for (int i = 0; i < 4; ++i) {
            const f32x2 df = f[4 * j + i] - m;
            const unsigned dd = pk_bf16(df[0], df[1]);
            const f32x2 uu = (f32x2){bflo(dd), bfhi(dd)} * rs;
            o[i] = pk_bf16(uu[0], uu[1]);
        }
        *(LAS u32x4*)(sm + r * LN_ROW + (l16 + 16 * j) * 16) = o;
        *(LAS u32x4*)(sm + (16 + r) * LN_ROW + (l16 + 16 * j) * 16) = v[j];
    }
    __syncthreads();
    const int g = lane >> 4, q4 = (lane & 15) >> 2, p = lane & 3;
#pragma unroll
    for (int it = 0; it < 5; ++it) {
        const int item = w + 4 * it;
        const int arr = item / 10, db = item % 10;
        lptr a = sm + (16 * arr + q4) * LN_ROW + (64 * db + 16 * g + 4 * p) * 2;
        const u32x4 v0 = tr_read2(a, 4 * LN_ROW), v1 = tr_read2(a + 8 * LN_ROW, 4 * LN_ROW);
        char* dst = ws + (arr ? OFF_X : OFF_U) + ((size_t)b * D + 64 * db + lane) * 32;
        ST_OUT((u32x4*)dst, v0);
        ST_OUT((u32x4*)(dst + 16), v1);
    }
}

__device__ __forceinline__ f32x2 cmul(f32x2 a, f32x2 b) {
    const f32x2 t = (f32x2){a[0], a[0]} * b;
    const f32x2 bs = {-b[1], b[0]};
    return __builtin_elementwise_fma((f32x2){a[1], a[1]}, bs, t);
}
__device__ __forceinline__ f32x2 cexp_p(float arl, float air, float p) {
    const float mag = __builtin_amdgcn_exp2f(arl * p);
    float rev = air * p; rev -= rintf(rev);
    return (f32x2){mag * __builtin_amdgcn_cosf(rev), mag * __builtin_amdgcn_sinf(rev)};
}

__device__ __forceinline__ void kgen_block(int d, const bf16_t* __restrict__ logA, const bf16_t* __restrict__ Aim, const bf16_t* __restrict__ Cre,
                                           const bf16_t* __restrict__ Cim, const bf16_t* __restrict__ logdt, char* __restrict__ Kc, lptr sm) {
    const int tid = threadIdx.x, lane = tid & 63, w = uni(tid >> 6);
    const int wn = w < 3 ? w : 0;
    unsigned ldt = logdt[d], la = logA[d * NM + 64 * wn + lane], ai = Aim[d * NM + 64 * wn + lane];
    unsigned cr[3], ci[3];
#pragma unroll
    for (int i = 0; i < 3; ++i) { cr[i] = Cre[d * NM + 64 * i + lane]; ci[i] = Cim[d * NM + 64 * i + lane]; }
    asm volatile("" : "+v"(ldt), "+v"(la), "+v"(ai), "+v"(cr[0]), "+v"(cr[1]), "+v"(cr[2]), "+v"(ci[0]), "+v"(ci[1]), "+v"(ci[2]));
    const float dt = rbf(expf(bf2f(ldt)));
    float mx = 0.f;
#pragma unroll
    for (int i = 0; i < 3; ++i) mx = fmaxf(mx, fmaxf(fabsf(bf2f(cr[i])), fabsf(bf2f(ci[i]))));
    mx = row16_max(mx);
    {
        const auto r1 = __builtin_amdgcn_permlane16_swap(__float_as_uint(mx), __float_as_uint(mx), false, false);
        mx = fmaxf(__uint_as_float(r1[0]), __uint_as_float(r1[1]));
        const auto r2 = __builtin_amdgcn_permlane32_swap(__float_as_uint(mx), __float_as_uint(mx), false, false);
        mx = fmaxf(__uint_as_float(r2[0]), __uint_as_float(r2[1]));
    }
    mx *= 2.f * dt;
    int e = (int)((__float_as_uint(mx) >> 23) & 0xff) - 126;
    e = e < -100 ? -100 : (e > 100 ? 100 : e);
    const float scale = __uint_as_float((unsigned)(127 - e) << 23), unscale = __uint_as_float((unsigned)(127 + e) << 23);
    if (w < 3) {
        const float are = -rbf(expf(bf2f(la))) * dt, aim = bf2f(ai) * dt;
        const float crw = bf2f(w == 0 ? cr[0] : (w == 1 ? cr[1] : cr[2])) * dt, ciw = bf2f(w == 0 ? ci[0] : (w == 1 ? ci[1] : ci[2])) * dt;
        const float arl = are * 1.4426950408889634f, air = aim * 0.15915494309189535f;
        lptr base = sm + w * KG_WAVE + 4 * lane;
#define PUT(row, z) *(LAS unsigned*)(base + (row) * KG_ROW) = pk_f16(z)
        const f32x2 w1 = cexp_p(arl, air, 1.f), w8 = cexp_p(arl, air, 8.f), H1 = cexp_p(arl, air, 128.f);
        f32x2 qv = {2.f * scale * crw, -2.f * scale * ciw};
        const f32x2 w8c = {w8[0], -w8[1]};
        f32x2 E[8], H[17];
        E[0] = (f32x2){1.f, 0.f}; E[1] = w1; H[1] = H1;
        PUT(0, qv); PUT(16, E[0]); PUT(17, E[1]); PUT(24, H1);
#pragma unroll
        for (int i = 1; i < 16; ++i) {
            qv = cmul(qv, w8c);
            H[i + 1] = cmul(H[i], H1);
            if (i < 7) E[i + 1] = cmul(E[i], w1);
            PUT(i, qv);
            if (i < 7) PUT(17 + i, E[i + 1]);
            const int hi = i + 1;
            if (hi < 4) PUT(32 + 4 * (hi - 2), H[hi]); else if (hi < 8) PUT(40 + 2 * (hi - 4), H[hi]); else PUT(48 + (hi - 8), H[hi]);
            if (i >= 7 && i < 14) PUT(25 + (i - 7), cmul(E[i - 6], H1));
            if (i >= 7 && i < 10) PUT(33 + (i - 7), cmul(E[2 * (i - 6)], H[2]));
            if (i >= 10 && i < 13) PUT(37 + (i - 10), cmul(E[2 * (i - 9)], H[3]));
            if (i >= 11 && i < 15) PUT(41 + 2 * (i - 11), cmul(H[4 + (i - 11)], E[4]));
        }
#undef PUT
        asm volatile("" ::: "memory");
        lptr rb = sm + w * KG_WAVE + (lane & 15) * KG_ROW + (lane >> 4) * 16;
        f32x4 acc[3] = {};
#pragma unroll
        for (int ks = 0; ks < 4; ++ks) {
            const h16x8 bq = *(LAS h16x8*)(rb + ks * 64);
#pragma unroll
            for (int mt = 0; mt < 3; ++mt) {
                const h16x8 ar = *(LAS h16x8*)(rb + (16 + 16 * mt) * KG_ROW + ks * 64);
                acc[mt] = __builtin_amdgcn_mfma_f32_16x16x32_f16(ar, bq, acc[mt], 0, 0, 0);
            }
        }
        asm volatile("" ::: "memory");
#pragma unroll
        for (int mt = 0; mt < 3; ++mt) *(LAS f32x4*)(sm + w * KG_WAVE + (mt * 64 + lane) * 16) = acc[mt];
    } else if (lane < 7) *(LAS unsigned short*)(sm + KG_KC + 2 * (641 + lane)) = 0;
    __syncthreads();
    {
        const int reg = w, row = 4 * (lane >> 4) + reg, c = lane & 15;
#pragma unroll
        for (int mt = 0; mt < 3; ++mt) {
            float s = 0.f;
#pragma unroll
            for (int ww = 0; ww < 3; ++ww) s += *(LAS float*)(sm + ww * KG_WAVE + (mt * 64 + lane) * 16 + reg * 4);
            int ci2; bool ok = true;
            if (mt == 0) ci2 = 128 * (row >> 3) + (row & 7) + 8 * c;
            else if (mt == 1) ci2 = row < 8 ? 256 + 64 * (row >> 2) + (row & 3) + 4 * c : 384 + 32 * ((row - 8) >> 1) + (row & 1) + 2 * c;
            else { ci2 = 512 + 16 * row + c; ok = row < 8 || (row == 8 && c == 0); }
            if (ok) *(LAS unsigned short*)(sm + KG_KC + 2 * ci2) = (unsigned short)pk_bf16(s * unscale, 0.f);
        }
    }
    __syncthreads();
    if (tid < 81) *(u32x4*)(Kc + (size_t)d * KC_PITCH + 16 * tid) = *(LAS u32x4*)(sm + KG_KC + 16 * tid);
}

__global__ __launch_bounds__(256, 3) void k1_prep(const bf16_t* __restrict__ x, const bf16_t* __restrict__ logA, const bf16_t* __restrict__ Aim,
                                                 const bf16_t* __restrict__ Cre, const bf16_t* __restrict__ Cim, const bf16_t* __restrict__ logdt,
                                                 char* __restrict__ ws PROF_ARG) {
    extern __shared__ __attribute__((aligned(16))) char smem[];
    lptr sm = (lptr)smem;
    STAMP(0, 0);
    const int b = blockIdx.x;
    if (b < K1_NLN) ln1_block(b, x, ws, sm);
    else kgen_block(b - K1_NLN, logA, Aim, Cre, Cim, logdt, ws + OFF_KC, sm);
    STAMP(0, 15);
}

struct KTab { unsigned short v[2][2048]; };
constexpr int ci_of(int l) {
    if (l < 0) return 641;
    if (l < 256) return l;
    const int s = l < 512 ? 1 : (l < 1024 ? 2 : 3);
    return ((l + (1 << (s - 1)) - 1 + ((l >> s) & 1)) >> s) + 128 * s;
}
constexpr KTab make_tab() { KTab t{}; for (int c = 0; c < 2; ++c) for (int y = 0; y < 2048; ++y) t.v[c][y] = (unsigned short)(2 * ci_of(2047 - c - y)); return t; }
__device__ const __attribute__((aligned(16))) KTab g_ktab = make_tab();

constexpr int CV_KCH = 8320;
constexpr int CV_U = 4 * CV_KCH;
constexpr int CV_X = CV_U + 144 * 128;
constexpr int CV_KC = CV_X + 128 * 128;
constexpr int K2_LDS = 84 * 1024;

__device__ __forceinline__ void conv_block(int b, const char* __restrict__ ws, const bf16_t* __restrict__ Dskip, bf16_t* __restrict__ hT, lptr sm PROF_ARG) {
    const int tid = threadIdx.x, lane = tid & 63, w = uni(tid >> 6);
    const int d0 = 4 * b, d = d0 + w;
    const unsigned sm0 = (unsigned)(size_t)sm;
    const unsigned vo = (unsigned)((lane >> 3) * (D * 32) + d0 * 32 + (((lane & 7) ^ ((lane >> 4) << 1)) * 16));
    u32x4 tb[2][4];
#pragma unroll
    for (int c = 0; c < 2; ++c)
#pragma unroll
        for (int i = 0; i < 4; ++i) asm volatile("global_load_dwordx4 %0, %1, %2" : "=v"(tb[c][i]) : "v"(lane * 16), "s"((const char*)&g_ktab + c * 4096 + i * 1024) : "memory");
    const unsigned dsk_raw = ((const unsigned*)Dskip)[d >> 1];
#pragma unroll
    for (int j = 0; j < 2; ++j) dma16(ws + OFF_KC + (size_t)d * KC_PITCH + j * 1024, lane * 16, sm0 + CV_KC + w * 2048 + j * 1024);
#pragma unroll
    for (int j = 0; j < 4; ++j) dma16(ws + OFF_U + (size_t)(4 * w + j) * (8 * D * 32), vo, sm0 + CV_U + 2048 + (4 * w + j) * 1024);
    const u32x4 z4 = {0u, 0u, 0u, 0u};
    if (tid < 128) *(LAS u32x4*)(sm + CV_U + tid * 16) = z4;
    if (lane < 8) *(LAS u32x4*)(sm + w * CV_KCH + (lane >> 2) * 4160 + 4096 + (lane & 3) * 16) = z4;
    STAMP(1, 1);
    asm volatile("s_waitcnt vmcnt(4)" : "+v"(tb[0][0]), "+v"(tb[0][1]), "+v"(tb[0][2]), "+v"(tb[0][3]), "+v"(tb[1][0]), "+v"(tb[1][1]), "+v"(tb[1][2]), "+v"(tb[1][3]) :: "memory");
    {
        lptr kc = sm + CV_KC + w * 2048;
        u32x4 o[2][4];
#pragma unroll
        for (int c = 0; c < 2; ++c)
#pragma unroll
            for (int i = 0; i < 4; ++i)
#pragma unroll
                for (int k = 0; k < 4; ++k) {
                    const unsigned tk = tb[c][i][k];
                    const unsigned lo = *(LAS unsigned short*)(kc + (tk & 0xffffu)), hi = *(LAS unsigned short*)(kc + (tk >> 16));
                    o[c][i][k] = lo | (hi << 16);
                }
#pragma unroll
        for (int c = 0; c < 2; ++c)
#pragma unroll
            for (int i = 0; i < 4; ++i) *(LAS u32x4*)(sm + w * CV_KCH + c * 4160 + (lane + 64 * i) * 16) = o[c][i];
    }
    STAMP(1, 6);
    wait_vm<0>();
    bar();
    STAMP(1, 2); CSTAMP(1, 4);
    const int n = lane & 15, h = lane >> 4;
    const lptr fb = sm + w * CV_KCH + ((n & 1) ? 0 : 4160) + 4 * (((2031 - n) >> 1) + 4 * h) - 512 * 7;
    f32x4 acc[8] = {};
    bf16x8 Fa[2][8], Ga[2][8];
    lptr gp, fp[4];
    auto addr = [&](int rr) {
        const int ln = n + (h >> 1) + 15 - 2 * rr;
        gp = sm + CV_U + 128 * ln + 16 * ((2 * w + (h & 1)) ^ (((ln >> 1) & 3) << 1));
#pragma unroll
        for (int k = 0; k < 4; ++k) { fp[k] = fb - 64 * rr + 1024 * k; asm volatile("" : "+v"(fp[k])); }
    };
    auto ldG = [&](int P, int c) { Ga[P][c] = *(LAS bf16x8*)(gp + 2048 * c); };
    u32x4 ftmp[2];
    auto ldF = [&](int P, int a, int half) {
        lptr q = fp[(7 - a) >> 1] + 512 * ((7 - a) & 1) + 8 * half;
        ftmp[a & 1][2 * half] = *(LAS unsigned*)q; ftmp[a & 1][2 * half + 1] = *(LAS unsigned*)(q + 4);
        if (half) Fa[P][a] = __builtin_bit_cast(bf16x8, ftmp[a & 1]);
    };
    addr(0);
#pragma unroll
    for (int c = 0; c < 8; ++c) { ldF(0, c, 0); ldF(0, c, 1); ldG(0, c); }
    SB();
#pragma unroll
    for (int rr = 0; rr < 8; ++rr) {
        const int P = rr & 1;
        int i = 0;
#pragma unroll
        for (int a = 0; a < 8; ++a)
#pragma unroll
            for (int c = a; c < 8; ++c) {
                acc[c] = __builtin_amdgcn_mfma_f32_16x16x32_bf16(Fa[P][a], Ga[P][c - a], acc[c], 0, 0, 0); PIN(acc[c]); SB();
                if (rr < 7) {
                    if (i == 0) { addr(rr + 1); SB(); }
                    else if (i <= 24) { const int k = (i - 1) / 3, m = (i - 1) % 3; if (m == 0) ldG(P ^ 1, k); else ldF(P ^ 1, k, m - 1); SB(); }
                }
                if (rr == 0 && i >= 26 && i < 34 && !(i & 1))
                    dma16(ws + OFF_X + (size_t)(4 * w + ((i - 26) >> 1)) * (8 * D * 32), vo, sm0 + CV_X + (4 * w + ((i - 26) >> 1)) * 1024);
                ++i;
            }
    }
    STAMP(1, 3); CSTAMP(1, 5);
    wait_vm<0>();
    bar();
    lptr yb = sm + w * CV_KCH;
    const float dsk = (d & 1) ? bfhi(dsk_raw) : bflo(dsk_raw);
    const f32x2 dk2 = {dsk, dsk};
    {
        const int ln0 = n + 0, oc = h >> 1;
        const int so = ln0 * 128 + 16 * ((2 * w + oc) ^ (((ln0 >> 1) & 3) << 1)) + 8 * (h & 1);
        u32x2 uv[8], xv[8];
#pragma unroll
        for (int c = 0; c < 8; ++c) { uv[c] = *(LAS u32x2*)(sm + CV_U + 2048 + so + 2048 * c); xv[c] = *(LAS u32x2*)(sm + CV_X + so + 2048 * c); }
#pragma unroll
        for (int c = 0; c < 8; ++c) {
            u32x2 o;
#pragma unroll
            for (int i = 0; i < 2; ++i) {
                const f32x2 uf = {bflo(uv[c][i]), bfhi(uv[c][i])}, xf = {bflo(xv[c][i]), bfhi(xv[c][i])};
                const f32x2 hf = (uf * dk2 + (f32x2){acc[c][2 * i], acc[c][2 * i + 1]}) + xf;
                o[i] = pk_bf16(hf[0], hf[1]);
            }
            *(LAS u32x2*)(yb + (256 * c + 16 * n + 4 * h) * 2) = o;
        }
    }
    asm volatile("" ::: "memory");
#pragma unroll
    for (int j = 0; j < 4; ++j) ST_OUT((u32x4*)((char*)hT + (size_t)d * (T * 2) + (64 * j + lane) * 16), *(LAS u32x4*)(yb + (64 * j + lane) * 16));
}

__device__ __forceinline__ void spare_delay() {
#define SPARE_DELAY 120
    const unsigned long long t0 = wall_clock64();
    while (wall_clock64() - t0 < SPARE_DELAY) __builtin_amdgcn_s_sleep(4);
}

__device__ __forceinline__ void wgtile_block(int b, const bf16_t* __restrict__ wg, const bf16_t* __restrict__ wu, char* __restrict__ wgf, float* __restrict__ cs, lptr sm) {
    const int tid = threadIdx.x, lane = tid & 63, w = uni(tid >> 6);
    const int j = b >> 1, kh = b & 1;
    const int c16 = tid & 15, r0 = tid >> 4;
    spare_delay();
    u32x4 v[20];
    const bf16_t* src = (c16 < 8 ? wg : wu) + (size_t)(320 * kh + r0) * F + 64 * j + (c16 & 7) * 8;
#pragma unroll
    for (int i = 0; i < 20; ++i) v[i] = *(const u32x4*)(src + (size_t)(16 * i) * F);
    f32x2 a[4] = {};
#pragma unroll
    for (int i = 0; i < 20; ++i) {
        const int k = 16 * i + r0;
        *(LAS u32x4*)(sm + k * 256 + (((c16 >> 1) ^ (k & 3)) * 32) + (c16 & 1) * 16) = v[i];
#pragma unroll
        for (int e = 0; e < 4; ++e) a[e] += (f32x2){bflo(v[i][e]), bfhi(v[i][e])};
    }
#pragma unroll
    for (int e = 0; e < 4; ++e)
#pragma unroll
        for (int hf = 0; hf < 2; ++hf) {
            const float s = xsum16_32(a[e][hf]);
            if (lane < 16) *(LAS float*)(sm + 81920 + (w * 128 + c16 * 8 + 2 * e + hf) * 4) = s;
        }
    __syncthreads();
    if (tid < 128) {
        const float s = (*(LAS float*)(sm + 81920 + tid * 4) + *(LAS float*)(sm + 81920 + (128 + tid) * 4)) + (*(LAS float*)(sm + 81920 + (256 + tid) * 4) + *(LAS float*)(sm + 81920 + (384 + tid) * 4));
        cs[kh * 2 * F + (tid >> 6) * F + 64 * j + (tid & 63)] = s;
    }
    const int g = lane >> 4, q = (lane & 15) >> 2, p = lane & 3;
#pragma unroll
    for (int i = 0; i < 20; ++i) {
        const int ftl = w, sub = i;
        const int k = 16 * sub + 4 * (g >> 1) + q;
        lptr a0 = sm + k * 256 + ((((g & 1) * 4 + ftl) ^ q) * 32) + p * 8;
        const int ftg = 4 * j + ftl, cb = ftg / 6, ft = ftg % 6;
        *(u32x4*)(wgf + ((((size_t)cb * 40 + 20 * kh + sub) * 6 + ft) * 64 + lane) * 16) = tr_read2(a0, 8 * 256);
    }
}

__device__ __forceinline__ void wdtile_block(int fb, const bf16_t* __restrict__ wd, char* __restrict__ wdf, lptr sm) {
    const int tid = threadIdx.x, lane = tid & 63, w = uni(tid >> 6);
    spare_delay();
    u32x4 v[10];
#pragma unroll
    for (int i = 0; i < 10; ++i) { const int ch = tid + 256 * i, row = ch / 80, c = ch % 80; v[i] = *(const u32x4*)(wd + (size_t)(32 * fb + row) * D + c * 8); }
#pragma unroll
    for (int i = 0; i < 10; ++i) { const int ch = tid + 256 * i, row = ch / 80, c = ch % 80; *(LAS u32x4*)(sm + row * LN_ROW + c * 16) = v[i]; }
    __syncthreads();
    const int g = lane >> 4, q = (lane & 15) >> 2, p = lane & 3;
#pragma unroll
    for (int i = 0; i < 10; ++i) {
        const int dt = w + 4 * i;
        lptr a = sm + (16 * (g >> 1) + 4 * (g & 1) + q) * LN_ROW + (16 * dt + 4 * p) * 2;
        *(u32x4*)(wdf + (((size_t)fb * 40 + dt) * 64 + lane) * 16) = tr_read2(a, 8 * LN_ROW);
    }
}

__global__ __launch_bounds__(256, 2) void k2_conv(char* __restrict__ ws, const bf16_t* __restrict__ Dskip, const bf16_t* __restrict__ wg,
                                                 const bf16_t* __restrict__ wu, const bf16_t* __restrict__ wd PROF_ARG) {
    extern __shared__ __attribute__((aligned(16))) char smem[];
    lptr sm = (lptr)smem;
    STAMP(1, 0);
    const int b = blockIdx.x;
    if (b < 160) conv_block(b, ws, Dskip, (bf16_t*)(ws + OFF_HT), sm PROF_FWD);
    else if (b < 208) wgtile_block(b - 160, wg, wu, ws + OFF_WG, (float*)(ws + OFF_CS), sm);
    else wdtile_block(b - 208, wd, ws + OFF_WD, sm);
    STAMP(1, 15);
}

#define G1_PRE 4
constexpr int G1_NS = 7, G1_SS = 20480, G1_W = 8192;
constexpr int G1_CS = G1_NS * G1_SS;
constexpr int G1_ST = G1_CS + 2048;
constexpr int G1_SP = G1_ST + 1024;
constexpr int K3_LDS = G1_SP + 512;

template <int WC>
struct G1Compute {
    f32x16 acc[3][2];
    bf16x8 Af[2][3], Bf[2][2];
    template <int P> __device__ __forceinline__ void mm(int ft, int tt) {
        acc[ft][tt] = __builtin_amdgcn_mfma_f32_32x32x16_bf16(Af[P][ft], Bf[P][tt], acc[ft][tt], 0, 0, 0); PIN(acc[ft][tt]); SB();
    }
    __device__ __forceinline__ static bf16x8 rdB(lptr p) { return __builtin_bit_cast(bf16x8, tr_read2(p, 8 * 256)); }
    template <int P, int KK, bool MORE, bool BAR>
    __device__ __forceinline__ void substep(lptr pa, const lptr (&pb)[2]) {
        mm<P>(0, 0);
        if (BAR) { sbar(); SB(); }
        if (MORE) { Bf[P ^ 1][0] = rdB(pb[0] + KK * 16 * 256); Af[P ^ 1][0] = *(LAS bf16x8*)(pa + KK * 6144); SB(); }
        mm<P>(0, 1);
        if (MORE) { Bf[P ^ 1][1] = rdB(pb[1] + KK * 16 * 256); Af[P ^ 1][1] = *(LAS bf16x8*)(pa + KK * 6144 + 1024); SB(); }
        mm<P>(1, 0);
        if (MORE) { Af[P ^ 1][2] = *(LAS bf16x8*)(pa + KK * 6144 + 2048); SB(); }
        mm<P>(1, 1);
        mm<P>(2, 0);
        mm<P>(2, 1);
    }
};

template <int WC>
__device__ __forceinline__ void g1_compute(lptr sm, char* __restrict__ ws, int wr, int lane, int t0, int cb PROF_ARG) {
    const int g = lane >> 4, q = (lane & 15) >> 2, p = lane & 3, hh = g >> 1, krow = 4 * hh + q;
    G1Compute<WC> C;
#pragma unroll
    for (int ft = 0; ft < 3; ++ft)
#pragma unroll
        for (int tt = 0; tt < 2; ++tt) C.acc[ft][tt] = (f32x16){};
    const lptr a0 = sm + G1_W + 3 * WC * 1024 + lane * 16;
    lptr b0[2];
#pragma unroll
    for (int tt = 0; tt < 2; ++tt) b0[tt] = sm + krow * 256 + (((2 * wr + tt) ^ q) * 64) + (g & 1) * 32 + p * 8;
    sbar();
    STAMP(2, 2); CSTAMP(2, 4);
#pragma unroll
    for (int tt = 0; tt < 2; ++tt) C.Bf[0][tt] = C.rdB(b0[tt]);
#pragma unroll
    for (int ft = 0; ft < 3; ++ft) C.Af[0][ft] = *(LAS bf16x8*)(a0 + ft * 1024);
    SB();
    lptr pa = a0, pb[2] = {b0[0], b0[1]};
    int nb = 0;
#pragma unroll 1
    for (int s = 0; s < 19; ++s) {
        C.template substep<0, 1, true, false>(pa, pb);
        nb = nb + G1_SS == G1_NS * G1_SS ? 0 : nb + G1_SS;
        pa = a0 + nb; pb[0] = b0[0] + nb; pb[1] = b0[1] + nb;
        SB();
        C.template substep<1, 0, true, true>(pa, pb);
    }
    C.template substep<0, 1, true, false>(pa, pb);
    C.template substep<1, 0, false, false>(pa, pb);
    STAMP(2, 3); CSTAMP(2, 5);
    bar();
    char* mF = ws + OFF_MF;
#pragma unroll
    for (int tt = 0; tt < 2; ++tt) {
        const int tl = 32 * (2 * wr + tt) + (lane & 31);
        const f32x2 sq = *(LAS f32x2*)(sm + G1_ST + tl * 8);
        const f32x2 rs2 = {sq[0], sq[0]}, nm2 = {sq[1], sq[1]};
        const int tg = t0 + tl;
#pragma unroll
        for (int ft = 0; ft < 3; ++ft) {
            const int fl = 16 * (3 * WC + ft) + 4 * hh;
            lptr cp = sm + G1_CS + fl * 4;
            const f32x4 cg0 = *(LAS f32x4*)cp + *(LAS f32x4*)(cp + 1024), cg1 = *(LAS f32x4*)(cp + 32) + *(LAS f32x4*)(cp + 1024 + 32);
            const f32x4 cu0 = *(LAS f32x4*)(cp + 384) + *(LAS f32x4*)(cp + 1024 + 384), cu1 = *(LAS f32x4*)(cp + 416) + *(LAS f32x4*)(cp + 1024 + 416);
            u32x4 o;
#pragma unroll
            for (int r = 0; r < 4; ++r) {
                const f32x2 cg = r < 2 ? (f32x2){cg0[2 * r], cg0[2 * r + 1]} : (f32x2){cg1[2 * r - 4], cg1[2 * r - 3]};
                const f32x2 cu = r < 2 ? (f32x2){cu0[2 * r], cu0[2 * r + 1]} : (f32x2){cu1[2 * r - 4], cu1[2 * r - 3]};
                const f32x2 ag = {C.acc[ft][tt][2 * r], C.acc[ft][tt][2 * r + 1]}, au = {C.acc[ft][tt][8 + 2 * r], C.acc[ft][tt][9 + 2 * r]};
                const f32x2 gv = ag * rs2 + nm2 * cg, uv = au * rs2 + nm2 * cu;
                const f32x2 ea = gv * (gv * gv * -0.1029432397f + -2.302208198f);
                const f32x2 den = (f32x2){__builtin_amdgcn_exp2f(ea[0]), __builtin_amdgcn_exp2f(ea[1])} + 1.0f;
                const f32x2 mv = gv * uv * (f32x2){__builtin_amdgcn_rcpf(den[0]), __builtin_amdgcn_rcpf(den[1])};
                o[r] = pk_bf16(mv[0], mv[1]);
            }
            const int ftg = 6 * cb + 3 * WC + ft;
            const int lp = (tg & 15) + 16 * (2 * (ftg & 1) + hh);
            ST_OUT((u32x4*)(mF + (((size_t)(tg >> 4) * 48 + (ftg >> 1)) * 64 + lp) * 16), o);
        }
    }
    STAMP(2, 15);
}

__global__ __launch_bounds__(512, 2) void k3_gemm1(char* __restrict__ ws PROF_ARG) {
    extern __shared__ __attribute__((aligned(16))) char smem[];
    lptr sm = (lptr)smem;
    STAMP(2, 0);
    const int tid = threadIdx.x, lane = tid & 63, w = uni(tid >> 6);
    const unsigned sm0 = (unsigned)(size_t)sm;
    const int grp = blockIdx.x & 7, idx = blockIdx.x >> 3;
    const int rb = (grp & 1) * 8 + (idx & 7), cb = (grp >> 1) * 4 + (idx >> 3);
    const int t0 = 128 * rb, f0 = 96 * cb;
    if (w >= 4) {
        const int lw = w & 3;
        const char* hT = ws + OFF_HT;
        const char* wsrc = ws + OFF_WG + (size_t)cb * 40 * 6144 + 3 * lw * 1024;
        unsigned vh[2];
#pragma unroll
        for (int j = 0; j < 2; ++j) {
            const int o = (2 * lw + j) * 1024 + lane * 16, k = o >> 8, c = ((o >> 4) & 15) ^ ((k & 3) << 2);
            vh[j] = (unsigned)(k * (T * 2) + t0 * 2 + c * 16);
        }
        const unsigned vl = lane * 16;
        unsigned long long* st = (unsigned long long*)(ws + OFF_ST);
        auto stage = [&](int s) {
            const unsigned lb = sm0 + (s % G1_NS) * G1_SS;
            const char* hs = hT + (size_t)s * 32 * (T * 2);
            const char* wsx = wsrc + (size_t)s * 12288;
            dma16(hs, vh[0], lb + (2 * lw) * 1024);
            dma16(wsx, vl, lb + G1_W + (3 * lw) * 1024);
            dma16(hs, vh[1], lb + (2 * lw + 1) * 1024);
            dma16(wsx + 1024, vl, lb + G1_W + (3 * lw + 1) * 1024);
            dma16(wsx + 2048, vl, lb + G1_W + (3 * lw + 2) * 1024);
        };
        if (lw < 3) {
            const unsigned co = (unsigned)(((lw == 0 ? f0 + lane : (lw == 1 ? (lane < 32 ? f0 + 64 + lane : F + f0 + lane - 32) : F + f0 + 32 + lane))) * 4);
            dma4(ws + OFF_CS, co, sm0 + G1_CS + lw * 256);
            dma4(ws + OFF_CS + 2 * F * 4, co, sm0 + G1_CS + 1024 + lw * 256);
        }
        u32x4 gv[3];
        unsigned long long g0, g1;
        float cs[8], sv[16];
        static_assert(G1_PRE == 4, "the counted waits below assume four stages in the prologue");
#pragma unroll
        for (int s = 0; s < G1_PRE; ++s) stage(s);
        {
            const char* hp = hT + (size_t)(160 * lw) * (T * 2) + (t0 + 8 * cb) * 2;
            const unsigned go = (unsigned)lane * (T * 2);
            asm volatile("global_load_dwordx4 %0, %1, %2" : "=v"(gv[0]) : "v"(go), "s"(hp) : "memory");
            asm volatile("global_load_dwordx4 %0, %1, %2" : "=v"(gv[1]) : "v"(go), "s"(hp + (size_t)64 * (T * 2)) : "memory");
            asm volatile("global_load_dwordx4 %0, %1, %2" : "=v"(gv[2]) : "v"(lane < 32 ? go : 0u), "s"(hp + (size_t)128 * (T * 2)) : "memory");
        }
        wait_vm<5 * (G1_PRE - 1) + 3>();
        sbar();
#pragma unroll
        for (int s = 0; s < 19; ++s) {
            const int last = (s + G1_PRE - 1) < 19 ? (s + G1_PRE - 1) : 19;
            if (s == 3) asm volatile("s_waitcnt vmcnt(10)" : "+v"(gv[0]), "+v"(gv[1]), "+v"(gv[2]) :: "memory");
            else if (s == 17) asm volatile("s_waitcnt vmcnt(5)" : "+v"(g0), "+v"(g1) :: "memory");
            else wait_vm_rt<30>(5 * (last - (s + 1)) + (s < 3 ? 3 : (s == 15 || s == 16 ? 2 : 0)));
            sbar();
            if (s + G1_PRE < 20) stage(s + G1_PRE);
            if (s == 14) {
                asm volatile("global_load_dwordx2 %0, %1, %2 sc1" : "=v"(g0) : "v"(lane * 8), "s"(st + t0) : "memory");
                asm volatile("global_load_dwordx2 %0, %1, %2 sc1" : "=v"(g1) : "v"(lane * 8), "s"(st + t0 + 64) : "memory");
            }
            if (s >= 3 && s <= 6) {
#pragma unroll
                for (int e = 2 * (s - 3); e < 2 * (s - 3) + 2; ++e) {
                    const unsigned c32 = (unsigned)__builtin_amdgcn_readlane((int)gv[0][e >> 1], 0);
                    cs[e] = (e & 1) ? bfhi(c32) : bflo(c32);
                    const float d0 = ((e & 1) ? bfhi(gv[0][e >> 1]) : bflo(gv[0][e >> 1])) - cs[e], d1 = ((e & 1) ? bfhi(gv[1][e >> 1]) : bflo(gv[1][e >> 1])) - cs[e];
                    const float d2 = lane < 32 ? ((e & 1) ? bfhi(gv[2][e >> 1]) : bflo(gv[2][e >> 1])) - cs[e] : 0.f;
                    sv[e] = d0 + d1 + d2; sv[8 + e] = d0 * d0 + d1 * d1 + d2 * d2;
                    PIN(sv[e]); PIN(sv[8 + e]);
                }
            }
            if (s == 7) {
#pragma unroll
                for (int j = 0; j < 8; ++j) {
                    const auto r = __builtin_amdgcn_permlane32_swap(__float_as_uint(sv[j]), __float_as_uint(sv[j + 8]), false, false);
                    sv[j] = __uint_as_float(r[0]) + __uint_as_float(r[1]); PIN(sv[j]);
                }
            }
            if (s == 8) {
#pragma unroll
                for (int k = 0; k < 4; ++k) {
                    const auto r = __builtin_amdgcn_permlane16_swap(__float_as_uint(sv[k]), __float_as_uint(sv[k + 4]), false, false);
                    sv[k] = __uint_as_float(r[0]) + __uint_as_float(r[1]);
                }
#pragma unroll
                for (int k = 0; k < 4; ++k) { sv[k] = row16_sum(sv[k]); PIN(sv[k]); }
            }
            if (s == 9) {
                if ((lane & 15) == 0) *(LAS f32x4*)(sm + G1_SP + lw * 96 + (lane >> 4) * 16) = (f32x4){sv[0], sv[1], sv[2], sv[3]};
                if (lane == 0) { *(LAS f32x4*)(sm + G1_SP + lw * 96 + 64) = (f32x4){cs[0], cs[1], cs[2], cs[3]}; *(LAS f32x4*)(sm + G1_SP + lw * 96 + 80) = (f32x4){cs[4], cs[5], cs[6], cs[7]}; }
                asm volatile("s_waitcnt lgkmcnt(0)" ::: "memory");
            }
            if (s == 10 && lw == 3 && lane < 8) {
                float pm[4], m2 = 0.f, mean = 0.f;
#pragma unroll
                for (int i = 0; i < 4; ++i) {
                    const float a = *(LAS float*)(sm + G1_SP + i * 96 + lane * 4) * (1.0f / 160.0f), q = *(LAS float*)(sm + G1_SP + i * 96 + 32 + lane * 4);
                    pm[i] = *(LAS float*)(sm + G1_SP + i * 96 + 64 + lane * 4) + a;
                    m2 += q - 160.0f * a * a; mean += 0.25f * pm[i];
                }
#pragma unroll
                for (int i = 0; i < 4; ++i) m2 += 160.0f * (pm[i] - mean) * (pm[i] - mean);
                const float var = fmaxf(m2 * (1.0f / (float)D), 0.f);
                const unsigned mb = pk_bf16(mean, rbf(sqrtf(rbf(rbf(var) + 1e-5f))));
                __hip_atomic_store(st + t0 + 8 * cb + lane, (1ull << 32) | mb, __ATOMIC_RELAXED, __HIP_MEMORY_SCOPE_AGENT);
            }
        }
        if (lw == 0) {
            for (unsigned spins = 0; !__all((g0 >> 32) == 1ull && (g1 >> 32) == 1ull) && spins < (1u << 22); ++spins) {
                __builtin_amdgcn_s_sleep(2);
                g0 = __hip_atomic_load(st + t0 + lane, __ATOMIC_RELAXED, __HIP_MEMORY_SCOPE_AGENT);
                g1 = __hip_atomic_load(st + t0 + 64 + lane, __ATOMIC_RELAXED, __HIP_MEMORY_SCOPE_AGENT);
            }
            const float r0 = __builtin_amdgcn_rcpf(bfhi((unsigned)g0)), r1 = __builtin_amdgcn_rcpf(bfhi((unsigned)g1));
            *(LAS f32x2*)(sm + G1_ST + lane * 8) = (f32x2){r0, -bflo((unsigned)g0) * r0};
            *(LAS f32x2*)(sm + G1_ST + (64 + lane) * 8) = (f32x2){r1, -bflo((unsigned)g1) * r1};
        }
        bar();
        return;
    }
    if (w & 1) g1_compute<1>(sm, ws, w >> 1, lane, t0, cb PROF_FWD); else g1_compute<0>(sm, ws, w >> 1, lane, t0, cb PROF_FWD);
}

constexpr int G2_NS = 4, G2_SS = 36 * 1024, G2_W = 16 * 1024;
constexpr int G2_H = G2_NS * G2_SS;
constexpr int K4_LDS = G2_H + 10240;

template <int ND>
__device__ __forceinline__ void g2_compute(lptr sm, int lane, int th, int dl0, int t0, int d0, bf16_t* __restrict__ out PROF_ARG) {
    f32x4 acc[ND][2] = {};
    bf16x8 A[2][ND], B[2][2];
    const lptr pb0 = sm + lane * 16 + 2 * th * 4096, pa0 = sm + lane * 16 + G2_W + dl0 * 1024;
    auto ld = [&](int P, lptr pa, lptr pb, int fl) {
#pragma unroll
        for (int tt = 0; tt < 2; ++tt) B[P][tt] = *(LAS bf16x8*)(pb + (tt * 4 + fl) * 1024);
#pragma unroll
        for (int dt = 0; dt < ND; ++dt) A[P][dt] = *(LAS bf16x8*)(pa + (fl * 5 + dt) * 1024);
        SB();
    };
    auto mm = [&](int P) {
#pragma unroll
        for (int dt = 0; dt < ND; ++dt)
#pragma unroll
            for (int tt = 0; tt < 2; ++tt) { acc[dt][tt] = __builtin_amdgcn_mfma_f32_16x16x32_bf16(A[P][dt], B[P][tt], acc[dt][tt], 0, 0, 0); PIN(acc[dt][tt]); SB(); }
    };
    sbar();
    STAMP(3, 2); CSTAMP(3, 4);
    ld(0, pa0, pb0, 0);
    lptr pa = pa0, pb = pb0;
    int nb = 0;
#pragma unroll 1
    for (int s = 0; s < 12; ++s) {
        ld(1, pa, pb, 1); mm(0);
        ld(0, pa, pb, 2); mm(1);
        ld(1, pa, pb, 3); mm(0);
        if (s < 11) {
            nb = nb + G2_SS == G2_NS * G2_SS ? 0 : nb + G2_SS;
            pa = pa0 + nb; pb = pb0 + nb;
            bar();
            ld(0, pa, pb, 0);
        }
        mm(1);
    }
    STAMP(3, 3); CSTAMP(3, 5);
    bar();
    const int g = lane >> 4, q = (lane & 15) >> 2, p = lane & 3;
#pragma unroll
    for (int dt = 0; dt < ND; ++dt)
#pragma unroll
        for (int tt = 0; tt < 2; ++tt) {
            const int row = 16 * (dl0 + dt) + 4 * g + q, seg = (2 * th + tt) ^ ((row >> 1) & 3);
            const u32x2 hv = tr_read(sm + G2_H + row * 128 + seg * 32 + p * 8);
            const unsigned o01 = pk_bf16(acc[dt][tt][0], acc[dt][tt][1]), o23 = pk_bf16(acc[dt][tt][2], acc[dt][tt][3]);
            const u32x2 r = {pk_bf16(bflo(hv[0]) + bflo(o01), bfhi(hv[0]) + bfhi(o01)), pk_bf16(bflo(hv[1]) + bflo(o23), bfhi(hv[1]) + bfhi(o23))};
            *(LAS u32x2*)(sm + (16 * (2 * th + tt) + (lane & 15)) * 176 + (16 * (dl0 + dt) + 4 * g) * 2) = r;
        }
}

__global__ __launch_bounds__(512, 2) void k4_gemm2(char* __restrict__ ws, bf16_t* __restrict__ out PROF_ARG) {
    extern __shared__ __attribute__((aligned(16))) char smem[];
    lptr sm = (lptr)smem;
    STAMP(3, 0);
    const int tid = threadIdx.x, lane = tid & 63, w = uni(tid >> 6);
    const unsigned sm0 = (unsigned)(size_t)sm;
    const int grp = blockIdx.x & 7, idx = blockIdx.x >> 3;
    const int tb = (grp >> 1) * 8 + (idx & 7), db = (grp & 1) * 4 + (idx >> 3);
    const int t0 = 64 * tb, d0 = 80 * db;
    if (w >= 4) {
        const int lw = w & 3;
        const char* ms = ws + OFF_MF + (size_t)(4 * tb + lw) * 48 * 1024;
        const char* wsrc = ws + OFF_WD + (size_t)lw * (40 * 1024) + (size_t)(5 * db) * 1024;
        const unsigned vl = lane * 16;
        auto stage = [&](int s) {
            const unsigned lb = sm0 + (s % G2_NS) * G2_SS;
#pragma unroll
            for (int j = 0; j < 4; ++j) {
                dma16(ms + (size_t)(4 * s + j) * 1024, vl, lb + (4 * lw + j) * 1024);
                dma16(wsrc + (size_t)(4 * s) * (40 * 1024) + j * 1024, vl, lb + G2_W + (5 * lw + j) * 1024);
            }
            dma16(wsrc + (size_t)(4 * s) * (40 * 1024) + 4 * 1024, vl, lb + G2_W + (5 * lw + 4) * 1024);
        };
        stage(0);
#pragma unroll
        for (int j = 0; j < 3; ++j) {
            const int i = lw + 4 * j < 10 ? lw + 4 * j : lw;
            const int o = i * 1024 + lane * 16, row = o >> 7, c = ((o >> 4) & 7) ^ (((row >> 1) & 3) << 1);
            dma16(ws + OFF_HT, (unsigned)((d0 + row) * (T * 2) + t0 * 2 + c * 16), sm0 + G2_H + i * 1024);
        }
        stage(1); stage(2);
        wait_vm<21>();
        sbar();
#pragma unroll
        for (int s = 0; s < 11; ++s) {
            if (s <= 9) wait_vm<9>(); else wait_vm<0>();
            sbar();
            if (s + 3 < 12) stage(s + 3);
        }
        sbar();
    } else {
        const int th = w & 1;
        if (w >> 1) g2_compute<2>(sm, lane, th, 3, t0, d0, out PROF_FWD); else g2_compute<3>(sm, lane, th, 0, t0, d0, out PROF_FWD);
    }
    bar();
    for (int ch = tid; ch < 640; ch += 512) {
        const int row = ch / 10, c = ch % 10;
        *(u32x4*)(out + (size_t)(t0 + row) * D + d0 + c * 8) = *(LAS u32x4*)(sm + row * 176 + c * 16);
    }
    STAMP(3, 15);
}

extern "C" void kernel_launch(void* const* d_in, const int* in_sizes, int n_in, void* d_out, int out_size, void* d_ws, size_t ws_size, hipStream_t stream) {
    const bf16_t* x = (const bf16_t*)d_in[0];
    const bf16_t* logA = (const bf16_t*)d_in[1];
    const bf16_t* Aim = (const bf16_t*)d_in[2];
    const bf16_t* Cre = (const bf16_t*)d_in[3];
    const bf16_t* Cim = (const bf16_t*)d_in[4];
    const bf16_t* logdt = (const bf16_t*)d_in[5];
    const bf16_t* Dskip = (const bf16_t*)d_in[6];
    const bf16_t* wg = (const bf16_t*)d_in[8];
    const bf16_t* wu = (const bf16_t*)d_in[9];
    const bf16_t* wd = (const bf16_t*)d_in[10];
    char* ws = (char*)d_ws;
    (void)hipFuncSetAttribute((const void*)k1_prep, hipFuncAttributeMaxDynamicSharedMemorySize, K1_LDS);
    (void)hipFuncSetAttribute((const void*)k2_conv, hipFuncAttributeMaxDynamicSharedMemorySize, K2_LDS);
    (void)hipFuncSetAttribute((const void*)k3_gemm1, hipFuncAttributeMaxDynamicSharedMemorySize, K3_LDS);
    (void)hipFuncSetAttribute((const void*)k4_gemm2, hipFuncAttributeMaxDynamicSharedMemorySize, K4_LDS);
    k1_prep<<<K1_NLN + K1_NKG, 256, K1_LDS, stream>>>(x, logA, Aim, Cre, Cim, logdt, ws PROF_PASS);
    k2_conv<<<256, 256, K2_LDS, stream>>>(ws, Dskip, wg, wu, wd PROF_PASS);
    k3_gemm1<<<256, 512, K3_LDS, stream>>>(ws PROF_PASS);
    k4_gemm2<<<256, 512, K4_LDS, stream>>>(ws, (bf16_t*)d_out PROF_PASS);
}
```

```cpp
#include <hip/hip_runtime.h>
#include <stdint.h>

typedef unsigned short bf16_t;
typedef short s16x4 __attribute__((ext_vector_type(4)));
typedef __bf16 bf16x8 __attribute__((ext_vector_type(8)));
typedef __bf16 bf16x2 __attribute__((ext_vector_type(2)));
typedef _Float16 h16x8 __attribute__((ext_vector_type(8)));
typedef _Float16 h16x2 __attribute__((ext_vector_type(2)));
typedef float f32x2 __attribute__((ext_vector_type(2)));
typedef float f32x4 __attribute__((ext_vector_type(4)));
typedef float f32x16 __attribute__((ext_vector_type(16)));
typedef unsigned u32x2 __attribute__((ext_vector_type(2)));
typedef unsigned u32x4 __attribute__((ext_vector_type(4)));
#define LAS __attribute__((address_space(3)))
typedef LAS char* lptr;

constexpr int T = 2048, D = 640, NM = 192, F = 1536;
constexpr size_t OFF_U = 0;
constexpr size_t OFF_X = OFF_U + (size_t)T * D * 2;
constexpr size_t OFF_HT = OFF_X + (size_t)T * D * 2;
constexpr size_t OFF_CS = OFF_HT + (size_t)T * D * 2;
constexpr size_t OFF_MF = OFF_CS + 65536;
constexpr size_t OFF_WD = OFF_MF + (size_t)T * F * 2;
constexpr size_t OFF_WG = OFF_WD + (size_t)F * D * 2;
constexpr size_t OFF_ST = OFF_WG + (size_t)2 * F * D * 2;
constexpr size_t OFF_FL = OFF_ST + (size_t)T * 8;
constexpr size_t OFF_PROF = OFF_FL + 163840;
constexpr int FL_E = 0;
constexpr int FL_REP = 4096;
constexpr int FL_LN = FL_REP, FL_LN_S = 16;
constexpr int FL_H = FL_LN + 1024, FL_H_S = 8;
constexpr int FL_WG = FL_H + 1280, FL_WD = FL_WG + 96 * 8, FL_W_S = 8;
constexpr int FL_M = 64, FL_M_S = 8;
static_assert(FL_WD + 48 * FL_W_S <= 2 * FL_REP && FL_M + 256 * FL_M_S <= FL_REP, "flag map");

#define STAMP(i)
#define STAMPL(i)
#define CSTAMP(i)
#define PROF_ARG
#define PROF_PASS
#define PROF_FWD

__device__ __forceinline__ float bf2f(unsigned v) { return __uint_as_float(v << 16); }
__device__ __forceinline__ float bflo(unsigned v) { return __uint_as_float(v << 16); }
__device__ __forceinline__ float bfhi(unsigned v) { return __uint_as_float(v & 0xffff0000u); }
__device__ __forceinline__ unsigned pk_bf16(float lo, float hi) { f32x2 v = {lo, hi}; return __builtin_bit_cast(unsigned, __builtin_convertvector(v, bf16x2)); }
__device__ __forceinline__ float rbf(float f) { return bflo(pk_bf16(f, f)); }
__device__ __forceinline__ unsigned pk_f16(f32x2 v) { return __builtin_bit_cast(unsigned, __builtin_convertvector(v, h16x2)); }
template <int CTRL> __device__ __forceinline__ float dppf(float x) { return __int_as_float(__builtin_amdgcn_update_dpp(0, __float_as_int(x), CTRL, 0xF, 0xF, true)); }
__device__ __forceinline__ float row16_sum(float x) { x += dppf<0xB1>(x); x += dppf<0x4E>(x); x += dppf<0x141>(x); x += dppf<0x140>(x); return x; }
__device__ __forceinline__ float row16_max(float x) { x = fmaxf(x, dppf<0xB1>(x)); x = fmaxf(x, dppf<0x4E>(x)); x = fmaxf(x, dppf<0x141>(x)); x = fmaxf(x, dppf<0x140>(x)); return x; }
__device__ __forceinline__ float xsum16_32(float x) {
    const unsigned u = __float_as_uint(x);
    const auto r = __builtin_amdgcn_permlane16_swap(u, u, false, false);
    const unsigned v = __float_as_uint(__uint_as_float(r[0]) + __uint_as_float(r[1]));
    const auto q = __builtin_amdgcn_permlane32_swap(v, v, false, false);
    return __uint_as_float(q[0]) + __uint_as_float(q[1]);
}
__device__ __forceinline__ u32x2 tr_read(lptr p) { return __builtin_bit_cast(u32x2, __builtin_amdgcn_ds_read_tr16_b64_v4i16((LAS s16x4*)p)); }
__device__ __forceinline__ u32x4 tr_read2(lptr p, int off2) { const u32x2 r1 = tr_read(p), r2 = tr_read(p + off2); return (u32x4){r1[0], r1[1], r2[0], r2[1]}; }
__device__ __forceinline__ int uni(int v) { return __builtin_amdgcn_readfirstlane(v); }

__device__ __forceinline__ void dma16(const void* sbase, unsigned voff, unsigned ldsaddr) {
    asm volatile("s_mov_b32 m0, %2\n\ts_nop 0\n\tglobal_load_lds_dwordx4 %0, %1" :: "v"(voff), "s"(sbase), "s"(ldsaddr) : "memory");
}
__device__ __forceinline__ void dma4(const void* sbase, unsigned voff, unsigned ldsaddr) {
    asm volatile("s_mov_b32 m0, %2\n\ts_nop 0\n\tglobal_load_lds_dword %0, %1" :: "v"(voff), "s"(sbase), "s"(ldsaddr) : "memory");
}
template <int N> __device__ __forceinline__ void wait_vm() { asm volatile("s_waitcnt vmcnt(%0)" :: "n"(N) : "memory"); }
template <int N> __device__ __forceinline__ void wait_vm_rt(int n) {
    if (n >= N) wait_vm<N>(); else if constexpr (N > 0) wait_vm_rt<N - 1>(n);
}
__device__ __forceinline__ void bar() { asm volatile("s_waitcnt lgkmcnt(0)\n\ts_barrier" ::: "memory"); }
__device__ __forceinline__ void sbar() { asm volatile("s_barrier" ::: "memory"); }
#define SB() __builtin_amdgcn_sched_barrier(0)
#define PIN(x) asm volatile("" : "+v"(x))
__device__ __forceinline__ void st_dev(void* p, u32x4 v) { asm volatile("global_store_dwordx4 %0, %1, off sc1\n\ts_nop 1" :: "v"(p), "v"(v) : "memory"); }
__device__ __forceinline__ void st_dev4(void* p, float v) { asm volatile("global_store_dword %0, %1, off sc1\n\ts_nop 1" :: "v"(p), "v"(v) : "memory"); }
__device__ __forceinline__ void dma16s(const void* sbase, unsigned voff, unsigned ldsaddr) {
    asm volatile("s_mov_b32 m0, %2\n\ts_nop 0\n\tglobal_load_lds_dwordx4 %0, %1 sc1" :: "v"(voff), "s"(sbase), "s"(ldsaddr) : "memory");
}
__device__ __forceinline__ void dma4s(const void* sbase, unsigned voff, unsigned ldsaddr) {
    asm volatile("s_mov_b32 m0, %2\n\ts_nop 0\n\tglobal_load_lds_dword %0, %1 sc1" :: "v"(voff), "s"(sbase), "s"(ldsaddr) : "memory");
}
__device__ __forceinline__ void set_flag(unsigned* f, unsigned tag) { __hip_atomic_store(f, tag, __ATOMIC_RELAXED, __HIP_MEMORY_SCOPE_AGENT); }
__device__ __forceinline__ void set_flag_rep(unsigned* f, unsigned tag, int lane) { __hip_atomic_store(f + lane * FL_REP, tag, __ATOMIC_RELAXED, __HIP_MEMORY_SCOPE_AGENT); }
template <int N, int S>
__device__ __forceinline__ void wait_flags(unsigned* f, unsigned tag, int lane) {
    for (unsigned spins = 0; spins < (1u << 18); ++spins) {
        bool ok = true;
        if (lane < N) ok = __hip_atomic_load(f + lane * S, __ATOMIC_RELAXED, __HIP_MEMORY_SCOPE_AGENT) == tag;
        if (N > 64 && lane + 64 < N) ok &= __hip_atomic_load(f + (64 + lane) * S, __ATOMIC_RELAXED, __HIP_MEMORY_SCOPE_AGENT) == tag;
        if (N > 128 && lane + 128 < N) ok &= __hip_atomic_load(f + (128 + lane) * S, __ATOMIC_RELAXED, __HIP_MEMORY_SCOPE_AGENT) == tag;
        if (__all(ok)) break;
        __builtin_amdgcn_s_sleep(1);
    }
    asm volatile("" ::: "memory");
}

constexpr int G1_NS = 7, G1_SS = 20480, G1_W = 8192;
constexpr int G2_NS = 4, G2_SS = 36 * 1024, G2_W = 16 * 1024;
constexpr int G2_H = G2_NS * G2_SS;
constexpr int G1_CS = G2_H + 10240;
constexpr int G1_ST = G1_CS + 3840;
constexpr int G1_SP = G1_ST + 1024;
constexpr int LDS_BYTES = G1_SP + 512;
constexpr int KG_ROW = 272;
constexpr int KG_WAVE = 57 * KG_ROW;
constexpr int CV_KCH = 8320;
constexpr int CV_K = 4 * G1_SS;
constexpr int CV_X = CV_K + 4 * CV_KCH;
constexpr int CV_U = CV_X + 128 * 128;
constexpr int CV_KC = CV_U + 144 * 128;
static_assert(CV_U >= 8 * KG_WAVE + 7 * KG_ROW && CV_KC + 4 * 1296 + 16 <= G1_CS, "LDS map");
constexpr int LN_ROW = 1344;

__device__ __forceinline__ void ln1_unit(int j, const bf16_t* __restrict__ x, char* __restrict__ ws, lptr sm PROF_ARG) {
    const int tid = threadIdx.x, r = tid >> 4, l16 = tid & 15, lane = tid & 63, w = uni(tid >> 6);
    const int t = 32 * j + r;
    u32x4 v[5];
#pragma unroll
    for (int i = 0; i < 5; ++i) v[i] = *(const u32x4*)(x + (size_t)t * D + (l16 + 16 * i) * 8);
    f32x2 f[20];
    f32x2 s2 = {0.f, 0.f};
#pragma unroll
    for (int i = 0; i < 5; ++i)
#pragma unroll
        for (int e = 0; e < 4; ++e) { f[4 * i + e] = (f32x2){bflo(v[i][e]), bfhi(v[i][e])}; s2 += f[4 * i + e]; }
    const float mean = row16_sum(s2[0] + s2[1]) / (float)D;
    f32x2 q2 = {0.f, 0.f};
#pragma unroll
    for (int i = 0; i < 20; ++i) { const f32x2 dv = f[i] - mean; q2 += dv * dv; }
    const float var = row16_sum(q2[0] + q2[1]) / (float)D;
    const float m = rbf(mean);
    const float rs = 1.0f / rbf(sqrtf(rbf(rbf(var) + 1e-5f)));
#pragma unroll
    for (int i = 0; i < 5; ++i) {
        u32x4 o;
#pragma unroll
        for (int e = 0; e < 4; ++e) {
            const f32x2 df = f[4 * i + e] - m;
            const unsigned dd = pk_bf16(df[0], df[1]);
            const f32x2 uu = (f32x2){bflo(dd), bfhi(dd)} * rs;
            o[e] = pk_bf16(uu[0], uu[1]);
        }
        *(LAS u32x4*)(sm + r * LN_ROW + (l16 + 16 * i) * 16) = o;
        *(LAS u32x4*)(sm + (32 + r) * LN_ROW + (l16 + 16 * i) * 16) = v[i];
    }
    bar();
    STAMP(23);
    const int g = lane >> 4, q4 = (lane & 15) >> 2, p = lane & 3;
    u32x4 v0[5], v1[5];
#pragma unroll
    for (int it = 0; it < 5; ++it) {
        const int item = w + 8 * it;
        const int arr = item / 20, tt = (item / 10) & 1, db = item % 10;
        lptr a = sm + (32 * arr + 16 * tt + q4) * LN_ROW + (64 * db + 16 * g + 4 * p) * 2;
        v0[it] = tr_read2(a, 4 * LN_ROW); v1[it] = tr_read2(a + 8 * LN_ROW, 4 * LN_ROW);
    }
    bar();
    lptr bn = sm + w * 10240;
#pragma unroll
    for (int it = 0; it < 5; ++it) { *(LAS u32x4*)(bn + it * 2048 + lane * 32) = v0[it]; *(LAS u32x4*)(bn + it * 2048 + lane * 32 + 16) = v1[it]; }
    asm volatile("" ::: "memory");
#pragma unroll
    for (int it = 0; it < 5; ++it) { v0[it] = *(LAS u32x4*)(bn + it * 2048 + lane * 16); v1[it] = *(LAS u32x4*)(bn + it * 2048 + 1024 + lane * 16); }
#pragma unroll
    for (int it = 0; it < 5; ++it) {
        const int item = w + 8 * it;
        const int arr = item / 20, tt = (item / 10) & 1, db = item % 10;
        char* dst = ws + (arr ? OFF_X : OFF_U) + ((size_t)(2 * j + tt) * D + 64 * db) * 32 + lane * 16;
        st_dev(dst, v0[it]);
        st_dev(dst + 1024, v1[it]);
    }
    STAMP(24);
}

__device__ __forceinline__ void wgtile_unit(int b, const bf16_t* __restrict__ wg, const bf16_t* __restrict__ wu, char* __restrict__ ws, lptr sm) {
    const int tid = threadIdx.x, lane = tid & 63, w = uni(tid >> 6);
    const int j = b >> 2, kq = b & 3;
    const int c16 = tid & 15, r0 = tid >> 4;
    u32x4 v[5];
    const bf16_t* src = (c16 < 8 ? wg : wu) + (size_t)(160 * kq + r0) * F + 64 * j + (c16 & 7) * 8;
#pragma unroll
    for (int i = 0; i < 5; ++i) v[i] = *(const u32x4*)(src + (size_t)(32 * i) * F);
    f32x2 a[4] = {};
#pragma unroll
    for (int i = 0; i < 5; ++i) {
        const int k = 32 * i + r0;
        *(LAS u32x4*)(sm + k * 256 + (((c16 >> 1) ^ (k & 3)) * 32) + (c16 & 1) * 16) = v[i];
#pragma unroll
        for (int e = 0; e < 4; ++e) a[e] += (f32x2){bflo(v[i][e]), bfhi(v[i][e])};
    }
#pragma unroll
    for (int e = 0; e < 4; ++e)
#pragma unroll
        for (int hf = 0; hf < 2; ++hf) {
            const float s = xsum16_32(a[e][hf]);
            if (lane < 16) *(LAS float*)(sm + 40960 + (w * 128 + c16 * 8 + 2 * e + hf) * 4) = s;
        }
    bar();
    const int g = lane >> 4, q = (lane & 15) >> 2, p = lane & 3;
    u32x4 o[5];
#pragma unroll
    for (int i = 0; i < 5; ++i) {
        const int item = w + 8 * i, ftl = item & 3, sub = item >> 2;
        const int k = 16 * sub + 4 * (g >> 1) + q;
        o[i] = tr_read2(sm + k * 256 + ((((g & 1) * 4 + ftl) ^ q) * 32) + p * 8, 8 * 256);
    }
#pragma unroll
    for (int i = 0; i < 5; ++i) {
        const int item = w + 8 * i, ftl = item & 3, sub = item >> 2;
        const int ftg = 4 * j + ftl, cb = ftg / 6, ft = ftg % 6;
        st_dev(ws + OFF_WG + ((((size_t)cb * 40 + 10 * kq + sub) * 6 + ft) * 64 + lane) * 16, o[i]);
    }
    if (tid < 128) {
        float s = 0.f;
#pragma unroll
        for (int i = 0; i < 8; ++i) s += *(LAS float*)(sm + 40960 + (i * 128 + tid) * 4);
        st_dev4(ws + OFF_CS + (size_t)(kq * 2 * F + (tid >> 6) * F + 64 * j + (tid & 63)) * 4, s);
    }
}

__device__ __forceinline__ void wdtile_unit(int fb, const bf16_t* __restrict__ wd, char* __restrict__ ws, lptr sm) {
    const int tid = threadIdx.x, lane = tid & 63, w = uni(tid >> 6);
    u32x4 v[5];
#pragma unroll
    for (int i = 0; i < 5; ++i) { const int ch = tid + 512 * i, row = ch / 80, c = ch % 80; v[i] = *(const u32x4*)(wd + (size_t)(32 * fb + row) * D + c * 8); }
#pragma unroll
    for (int i = 0; i < 5; ++i) { const int ch = tid + 512 * i, row = ch / 80, c = ch % 80; *(LAS u32x4*)(sm + row * LN_ROW + c * 16) = v[i]; }
    bar();
    const int g = lane >> 4, q = (lane & 15) >> 2, p = lane & 3;
    u32x4 o[5];
#pragma unroll
    for (int i = 0; i < 5; ++i) o[i] = tr_read2(sm + (16 * (g >> 1) + 4 * (g & 1) + q) * LN_ROW + (16 * (w + 8 * i) + 4 * p) * 2, 8 * LN_ROW);
#pragma unroll
    for (int i = 0; i < 5; ++i) st_dev(ws + OFF_WD + (((size_t)fb * 40 + w + 8 * i) * 64 + lane) * 16, o[i]);
}

__device__ __forceinline__ f32x2 cmul(f32x2 a, f32x2 b) {
    const f32x2 t = (f32x2){a[0], a[0]} * b;
    const f32x2 bs = {-b[1], b[0]};
    return __builtin_elementwise_fma((f32x2){a[1], a[1]}, bs, t);
}
__device__ __forceinline__ f32x2 cexp_p(float arl, float air, float p) {
    const float mag = __builtin_amdgcn_exp2f(arl * p);
    float rev = air * p; rev -= rintf(rev);
    return (f32x2){mag * __builtin_amdgcn_cosf(rev), mag * __builtin_amdgcn_sinf(rev)};
}
struct KgenIn { unsigned ldt, la, ai, cr[3], ci[3]; };
__device__ __forceinline__ void kgen_load(KgenIn& k, int d, int third, int lane, const bf16_t* __restrict__ logA, const bf16_t* __restrict__ Aim,
                                          const bf16_t* __restrict__ Cre, const bf16_t* __restrict__ Cim, const bf16_t* __restrict__ logdt) {
    k.ldt = logdt[d]; k.la = logA[d * NM + 64 * third + lane]; k.ai = Aim[d * NM + 64 * third + lane];
#pragma unroll
    for (int i = 0; i < 3; ++i) { k.cr[i] = Cre[d * NM + 64 * i + lane]; k.ci[i] = Cim[d * NM + 64 * i + lane]; }
}
__device__ __forceinline__ void kgen_task(const KgenIn& k, int third, int lane, lptr tab, f32x4 (&acc)[3], float& unscale) {
    const float dt = rbf(expf(bf2f(k.ldt)));
    float mx = 0.f;
#pragma unroll
    for (int i = 0; i < 3; ++i) mx = fmaxf(mx, fmaxf(fabsf(bf2f(k.cr[i])), fabsf(bf2f(k.ci[i]))));
    mx = row16_max(mx);
    {
        const auto r1 = __builtin_amdgcn_permlane16_swap(__float_as_uint(mx), __float_as_uint(mx), false, false);
        mx = fmaxf(__uint_as_float(r1[0]), __uint_as_float(r1[1]));
        const auto r2 = __builtin_amdgcn_permlane32_swap(__float_as_uint(mx), __float_as_uint(mx), false, false);
        mx = fmaxf(__uint_as_float(r2[0]), __uint_as_float(r2[1]));
    }
    mx *= 2.f * dt;
    int e = (int)((__float_as_uint(mx) >> 23) & 0xff) - 126;
    e = e < -100 ? -100 : (e > 100 ? 100 : e);
    const float scale = __uint_as_float((unsigned)(127 - e) << 23);
    unscale = __uint_as_float((unsigned)(127 + e) << 23);
    const float are = -rbf(expf(bf2f(k.la))) * dt, aim = bf2f(k.ai) * dt;
    const float crw = bf2f(third == 0 ? k.cr[0] : (third == 1 ? k.cr[1] : k.cr[2])) * dt, ciw = bf2f(third == 0 ? k.ci[0] : (third == 1 ? k.ci[1] : k.ci[2])) * dt;
    const float arl = are * 1.4426950408889634f, air = aim * 0.15915494309189535f;
    lptr base = tab + 4 * lane;
#define PUT(row, z) *(LAS unsigned*)(base + (row) * KG_ROW) = pk_f16(z)
    const f32x2 w1 = cexp_p(arl, air, 1.f), w8 = cexp_p(arl, air, 8.f), H1 = cexp_p(arl, air, 128.f);
    f32x2 qv = {2.f * scale * crw, -2.f * scale * ciw};
    const f32x2 w8c = {w8[0], -w8[1]};
    f32x2 E[8], H[17];
    E[0] = (f32x2){1.f, 0.f}; E[1] = w1; H[1] = H1;
    PUT(0, qv); PUT(16, E[0]); PUT(17, E[1]); PUT(24, H1);
#pragma unroll
    for (int i = 1; i < 16; ++i) {
        qv = cmul(qv, w8c);
        H[i + 1] = cmul(H[i], H1);
        if (i < 7) E[i + 1] = cmul(E[i], w1);
        PUT(i, qv);
        if (i < 7) PUT(17 + i, E[i + 1]);
        const int hi = i + 1;
        if (hi < 4) PUT(32 + 4 * (hi - 2), H[hi]); else if (hi < 8) PUT(40 + 2 * (hi - 4), H[hi]); else PUT(48 + (hi - 8), H[hi]);
        if (i >= 7 && i < 14) PUT(25 + (i - 7), cmul(E[i - 6], H1));
        if (i >= 7 && i < 10) PUT(33 + (i - 7), cmul(E[2 * (i - 6)], H[2]));
        if (i >= 10 && i < 13) PUT(37 + (i - 10), cmul(E[2 * (i - 9)], H[3]));
        if (i >= 11 && i < 15) PUT(41 + 2 * (i - 11), cmul(H[4 + (i - 11)], E[4]));
    }
#undef PUT
    asm volatile("" ::: "memory");
    lptr rb = tab + (lane & 15) * KG_ROW + (lane >> 4) * 16;
#pragma unroll
    for (int mt = 0; mt < 3; ++mt) acc[mt] = (f32x4){};
#pragma unroll
    for (int ks = 0; ks < 4; ++ks) {
        const h16x8 bq = *(LAS h16x8*)(rb + ks * 64);
#pragma unroll
        for (int mt = 0; mt < 3; ++mt) {
            const h16x8 ar = *(LAS h16x8*)(rb + (16 + 16 * mt) * KG_ROW + ks * 64);
            acc[mt] = __builtin_amdgcn_mfma_f32_16x16x32_f16(ar, bq, acc[mt], 0, 0, 0);
        }
    }
#pragma unroll
    for (int mt = 0; mt < 3; ++mt) PIN(acc[mt]);
    asm volatile("s_waitcnt lgkmcnt(0)" ::: "memory");
}

__device__ __forceinline__ void g1_prefetch_w(char* __restrict__ ws, lptr sm, int cb, int lane, unsigned tag) {
    const unsigned sm0 = (unsigned)(size_t)sm;
    wait_flags<96, FL_W_S>((unsigned*)(ws + OFF_FL) + (blockIdx.x & 7) * FL_REP + FL_WG, tag, lane);
    const char* wsrc = ws + OFF_WG + (size_t)cb * 40 * 6144;
    const int f0 = 96 * cb;
#pragma unroll
    for (int i = 0; i < 3; ++i) {
        const unsigned co = (unsigned)(((i == 0 ? f0 + lane : (i == 1 ? (lane < 32 ? f0 + 64 + lane : F + f0 + lane - 32) : F + f0 + 32 + lane))) * 4);
#pragma unroll
        for (int kq = 0; kq < 4; ++kq) dma4s(ws + OFF_CS + kq * 2 * F * 4, co, sm0 + G1_CS + kq * 768 + i * 256);
    }
#pragma unroll
    for (int s = 0; s < 4; ++s)
#pragma unroll
        for (int i = 0; i < 12; ++i) dma16s(wsrc + (size_t)s * 12288 + i * 1024, lane * 16, sm0 + s * G1_SS + G1_W + i * 1024);
    wait_vm<48>();
#pragma unroll
    for (int i = 0; i < 3; ++i) {
        lptr cp = sm + G1_CS + i * 256 + lane * 4;
        *(LAS float*)(cp + 3072) = (*(LAS float*)cp + *(LAS float*)(cp + 768)) + (*(LAS float*)(cp + 1536) + *(LAS float*)(cp + 2304));
    }
    asm volatile("s_waitcnt vmcnt(0) lgkmcnt(0)" ::: "memory");
}

struct KTab { unsigned short v[2][2048]; };
constexpr int ci_of(int l) {
    if (l < 0) return 641;
    if (l < 256) return l;
    const int s = l < 512 ? 1 : (l < 1024 ? 2 : 3);
    return ((l + (1 << (s - 1)) - 1 + ((l >> s) & 1)) >> s) + 128 * s;
}
constexpr KTab make_tab() { KTab t{}; for (int c = 0; c < 2; ++c) for (int y = 0; y < 2048; ++y) t.v[c][y] = (unsigned short)(2 * ci_of(2047 - c - y)); return t; }
__device__ const __attribute__((aligned(16))) KTab g_ktab = make_tab();

struct Params {
    const bf16_t *x, *logA, *Aim, *Cre, *Cim, *logdt, *Dskip, *wg, *wu, *wd;
    char* ws;
    bf16_t* out;
};

__device__ __forceinline__ void conv_wg(const Params& P, int b, int cb, unsigned tag, lptr sm PROF_ARG) {
    const int tid = threadIdx.x, lane = tid & 63, w = uni(tid >> 6);
    char* ws = P.ws;
    const int d0 = 4 * b;
    const unsigned sm0 = (unsigned)(size_t)sm;
    unsigned* fl = (unsigned*)(ws + OFF_FL);
    const unsigned vo = (unsigned)((lane >> 3) * (D * 32) + d0 * 32 + (((lane & 7) ^ ((lane >> 4) << 1)) * 16));
    KgenIn k1, k2;
    kgen_load(k1, d0 + w / 3, w % 3, lane, P.logA, P.Aim, P.Cre, P.Cim, P.logdt);
    const int t2 = 8 + (w & 3);
    kgen_load(k2, d0 + t2 / 3, t2 % 3, lane, P.logA, P.Aim, P.Cre, P.Cim, P.logdt);
    u32x4 tb[4];
#pragma unroll
    for (int i = 0; i < 4; ++i) tb[i] = *(const u32x4*)(&g_ktab.v[w & 1][8 * (lane + 64 * i)]);
    const unsigned dsk_raw = ((const unsigned*)P.Dskip)[(d0 + (w & 3)) >> 1];
    asm volatile("" : "+v"(k1.ldt), "+v"(k1.la), "+v"(k1.ai), "+v"(k1.cr[0]), "+v"(k1.cr[1]), "+v"(k1.cr[2]), "+v"(k1.ci[0]), "+v"(k1.ci[1]), "+v"(k1.ci[2]));
    asm volatile("" : "+v"(k2.ldt), "+v"(k2.la), "+v"(k2.ai), "+v"(k2.cr[0]), "+v"(k2.cr[1]), "+v"(k2.cr[2]), "+v"(k2.ci[0]), "+v"(k2.ci[1]), "+v"(k2.ci[2]));
    asm volatile("" : "+v"(tb[0]), "+v"(tb[1]), "+v"(tb[2]), "+v"(tb[3]));
    STAMP(1);
    const lptr tab = sm + w * KG_WAVE;
    f32x4 a1[3], a2[3];
    float un1, un2;
    kgen_task(k1, w % 3, lane, tab, a1, un1);
    if (w % 3 == 0 && lane == 0) *(LAS float*)(sm + CV_KC + 4 * 1296 + (w / 3) * 4) = un1;
    if (w < 4) {
        kgen_task(k2, t2 % 3, lane, tab, a2, un2);
        if (w == 1 && lane == 0) *(LAS float*)(sm + CV_KC + 4 * 1296 + 12) = un2;
#pragma unroll
        for (int mt = 0; mt < 3; ++mt) *(LAS f32x4*)(tab + 3072 + (mt * 64 + lane) * 16) = a2[mt];
    } else if (w == 5) {
        const u32x4 z4 = {0u, 0u, 0u, 0u};
        *(LAS u32x4*)(sm + CV_U + lane * 16) = z4; *(LAS u32x4*)(sm + CV_U + 1024 + lane * 16) = z4;
        if (lane < 28) *(LAS unsigned short*)(sm + CV_KC + (lane / 7) * 1296 + 2 * (641 + lane % 7)) = 0;
    }
#pragma unroll
    for (int mt = 0; mt < 3; ++mt) *(LAS f32x4*)(tab + (mt * 64 + lane) * 16) = a1[mt];
    bar();
    STAMP(2);
    {
        const int c = w >> 1, hf = w & 1;
        f32x2 ks[3];
        lptr pt[3];
#pragma unroll
        for (int j = 0; j < 3; ++j) { const int t = 3 * c + j; pt[j] = sm + (t & 7) * KG_WAVE + (t >> 3) * 3072 + lane * 16 + hf * 8; }
        const float un = *(LAS float*)(sm + CV_KC + 4 * 1296 + c * 4);
#pragma unroll
        for (int mt = 0; mt < 3; ++mt) ks[mt] = ((*(LAS f32x2*)(pt[0] + mt * 1024) + *(LAS f32x2*)(pt[1] + mt * 1024)) + *(LAS f32x2*)(pt[2] + mt * 1024)) * un;
        lptr kc = sm + CV_KC + c * 1296;
        const int hq = lane >> 4, col = lane & 15;
        const unsigned p0 = pk_bf16(ks[0][0], ks[0][1]), p1 = pk_bf16(ks[1][0], ks[1][1]), p2 = pk_bf16(ks[2][0], ks[2][1]);
        const int c0 = 128 * (hq >> 1) + 4 * (hq & 1) + 2 * hf + 8 * col;
        *(LAS unsigned*)(kc + 2 * c0) = p0;
        const int c1 = hq < 2 ? 256 + 64 * hq + 2 * hf + 4 * col : 384 + 32 * (2 * (hq - 2) + hf) + 2 * col;
        *(LAS unsigned*)(kc + 2 * c1) = p1;
        const int r2 = 4 * hq + 2 * hf, c2 = 512 + 16 * r2 + col;
        if (r2 < 8) { *(LAS unsigned short*)(kc + 2 * c2) = (unsigned short)p2; *(LAS unsigned short*)(kc + 2 * (c2 + 16)) = (unsigned short)(p2 >> 16); }
        else if (r2 == 8 && col == 0) *(LAS unsigned short*)(kc + 2 * c2) = (unsigned short)p2;
    }
    bar();
    STAMP(13);
    {
        lptr kc = sm + CV_KC + (w >> 1) * 1296;
        u32x4 o[4];
#pragma unroll
        for (int i = 0; i < 4; ++i)
#pragma unroll
            for (int k = 0; k < 4; ++k) {
                const unsigned tk = tb[i][k];
                const unsigned lo = *(LAS unsigned short*)(kc + (tk & 0xffffu)), hi = *(LAS unsigned short*)(kc + (tk >> 16));
                o[i][k] = lo | (hi << 16);
            }
        lptr dst = sm + CV_K + (w >> 1) * CV_KCH + (w & 1) * 4160;
#pragma unroll
        for (int i = 0; i < 4; ++i) *(LAS u32x4*)(dst + (lane + 64 * i) * 16) = o[i];
        if (lane < 4) *(LAS u32x4*)(dst + 4096 + lane * 16) = (u32x4){0u, 0u, 0u, 0u};
    }
    STAMP(14);
    if (w == 4) {
        wait_flags<64, FL_LN_S>(fl + (b & 7) * FL_REP + FL_LN, tag, lane);
        STAMPL(25);
#pragma unroll
        for (int j = 0; j < 16; ++j) dma16s(ws + OFF_U + (size_t)j * (8 * D * 32), vo, sm0 + CV_U + 2048 + j * 1024);
        wait_vm<0>();
    }
    bar();
    STAMP(3); CSTAMP(16);
    if (w < 4) {
        const int n = lane & 15, h = lane >> 4;
        const lptr fb = sm + CV_K + w * CV_KCH + ((n & 1) ? 0 : 4160) + 4 * (((2031 - n) >> 1) + 4 * h) - 512 * 7;
        f32x4 acc[8] = {};
        bf16x8 Fa[2][8], Ga[2][8];
        lptr gp, fp[4];
        auto addr = [&](int rr) {
            const int ln = n + (h >> 1) + 15 - 2 * rr;
            gp = sm + CV_U + 128 * ln + 16 * ((2 * w + (h & 1)) ^ (((ln >> 1) & 3) << 1));
#pragma unroll
            for (int k = 0; k < 4; ++k) { fp[k] = fb - 64 * rr + 1024 * k; asm volatile("" : "+v"(fp[k])); }
        };
        auto ldG = [&](int Q, int c) { Ga[Q][c] = *(LAS bf16x8*)(gp + 2048 * c); };
        u32x4 ftmp[2];
        auto ldF = [&](int Q, int a, int half) {
            lptr q = fp[(7 - a) >> 1] + 512 * ((7 - a) & 1) + 8 * half;
            ftmp[a & 1][2 * half] = *(LAS unsigned*)q; ftmp[a & 1][2 * half + 1] = *(LAS unsigned*)(q + 4);
            if (half) Fa[Q][a] = __builtin_bit_cast(bf16x8, ftmp[a & 1]);
        };
        addr(0);
#pragma unroll
        for (int c = 0; c < 8; ++c) { ldF(0, c, 0); ldF(0, c, 1); ldG(0, c); }
        SB();
#pragma unroll
        for (int rr = 0; rr < 8; ++rr) {
            const int Q = rr & 1;
            int i = 0;
#pragma unroll
            for (int a = 0; a < 8; ++a)
#pragma unroll
                for (int c = a; c < 8; ++c) {
                    acc[c] = __builtin_amdgcn_mfma_f32_16x16x32_bf16(Fa[Q][a], Ga[Q][c - a], acc[c], 0, 0, 0); PIN(acc[c]); SB();
                    if (rr < 7) {
                        if (i == 0) { addr(rr + 1); SB(); }
                        else if (i <= 24) { const int k = (i - 1) / 3, m = (i - 1) % 3; if (m == 0) ldG(Q ^ 1, k); else ldF(Q ^ 1, k, m - 1); SB(); }
                    }
                    ++i;
                }
        }
        STAMP(4); CSTAMP(17);
        bar();
        STAMP(26);
        const int d = d0 + w;
        lptr yb = sm + CV_K + w * CV_KCH;
        const float dsk = (d & 1) ? bfhi(dsk_raw) : bflo(dsk_raw);
        const f32x2 dk2 = {dsk, dsk};
        const int oc = h >> 1;
        const int so = n * 128 + 16 * ((2 * w + oc) ^ (((n >> 1) & 3) << 1)) + 8 * (h & 1);
        u32x2 uv[8], xv[8];
#pragma unroll
        for (int c = 0; c < 8; ++c) { uv[c] = *(LAS u32x2*)(sm + CV_U + 2048 + so + 2048 * c); xv[c] = *(LAS u32x2*)(sm + CV_X + so + 2048 * c); }
#pragma unroll
        for (int c = 0; c < 8; ++c) {
            u32x2 o;
#pragma unroll
            for (int i = 0; i < 2; ++i) {
                const f32x2 uf = {bflo(uv[c][i]), bfhi(uv[c][i])}, xf = {bflo(xv[c][i]), bfhi(xv[c][i])};
                const f32x2 hf = (uf * dk2 + (f32x2){acc[c][2 * i], acc[c][2 * i + 1]}) + xf;
                o[i] = pk_bf16(hf[0], hf[1]);
            }
            *(LAS u32x2*)(yb + (256 * c + 16 * n + 4 * h) * 2) = o;
        }
        asm volatile("" ::: "memory");
#pragma unroll
        for (int j = 0; j < 4; ++j) st_dev(ws + OFF_HT + (size_t)d * (T * 2) + (64 * j + lane) * 16, *(LAS u32x4*)(yb + (64 * j + lane) * 16));
        STAMP(27);
        wait_vm<0>();
    } else {
        if (w == 5) {
#pragma unroll
            for (int j = 0; j < 16; ++j) dma16s(ws + OFF_X + (size_t)j * (8 * D * 32), vo, sm0 + CV_X + j * 1024);
            wait_vm<0>();
        }
        bar();
    }
    bar();
    if (tid < 8) set_flag_rep(fl + FL_H + b * FL_H_S, tag, tid);
    STAMP(5);
    if (w == 6) g1_prefetch_w(ws, sm, cb, lane, tag);
}

__device__ __forceinline__ void spare_wg(const Params& P, int j, int cb, unsigned tag, lptr sm PROF_ARG) {
    const int tid = threadIdx.x, lane = tid & 63, w = uni(tid >> 6);
    unsigned* fl = (unsigned*)(P.ws + OFF_FL);
    if (j < 64) {
        ln1_unit(j, P.x, P.ws, sm PROF_FWD);
        wait_vm<0>();
        bar();
        if (tid < 8) set_flag_rep(fl + FL_LN + j * FL_LN_S, tag, tid);
    }
    STAMP(1);
    const int n_g = j >= 64 ? 2 : (j < 32 ? 1 : 0), g0 = j >= 64 ? 2 * (j - 64) : 64 + j;
    for (int i = 0; i < n_g; ++i) {
        wgtile_unit(g0 + i, P.wg, P.wu, P.ws, sm);
        wait_vm<0>();
        bar();
        if (tid < 8) set_flag_rep(fl + FL_WG + (g0 + i) * FL_W_S, tag, tid);
    }
    STAMP(2);
    if (j >= 64 || (j >= 32 && j < 48)) {
        const int fb = j >= 64 ? j - 64 : j;
        wdtile_unit(fb, P.wd, P.ws, sm);
        wait_vm<0>();
        bar();
        if (tid < 8) set_flag_rep(fl + FL_WD + fb * FL_W_S, tag, tid);
    }
    if (w == 6) g1_prefetch_w(P.ws, sm, cb, lane, tag);
    STAMP(5);
}

template <int WC>
struct G1Compute {
    f32x16 acc[3][2];
    bf16x8 Af[2][3], Bf[2][2];
    template <int P> __device__ __forceinline__ void mm(int ft, int tt) {
        acc[ft][tt] = __builtin_amdgcn_mfma_f32_32x32x16_bf16(Af[P][ft], Bf[P][tt], acc[ft][tt], 0, 0, 0); PIN(acc[ft][tt]); SB();
    }
    __device__ __forceinline__ static bf16x8 rdB(lptr p) { return __builtin_bit_cast(bf16x8, tr_read2(p, 8 * 256)); }
    template <int P, int KK, bool MORE, bool BAR>
    __device__ __forceinline__ void substep(lptr pa, const lptr (&pb)[2]) {
        mm<P>(0, 0);
        if (BAR) { sbar(); SB(); }
        if (MORE) { Bf[P ^ 1][0] = rdB(pb[0] + KK * 16 * 256); Af[P ^ 1][0] = *(LAS bf16x8*)(pa + KK * 6144); SB(); }
        mm<P>(0, 1);
        if (MORE) { Bf[P ^ 1][1] = rdB(pb[1] + KK * 16 * 256); Af[P ^ 1][1] = *(LAS bf16x8*)(pa + KK * 6144 + 1024); SB(); }
        mm<P>(1, 0);
        if (MORE) { Af[P ^ 1][2] = *(LAS bf16x8*)(pa + KK * 6144 + 2048); SB(); }
        mm<P>(1, 1);
        mm<P>(2, 0);
        mm<P>(2, 1);
    }
};

template <int WC>
__device__ __forceinline__ void g1_compute(lptr sm, char* __restrict__ ws, int wr, int lane, int t0, int cb PROF_ARG) {
    const int g = lane >> 4, q = (lane & 15) >> 2, p = lane & 3, hh = g >> 1, krow = 4 * hh + q;
    G1Compute<WC> C;
#pragma unroll
    for (int ft = 0; ft < 3; ++ft)
#pragma unroll
        for (int tt = 0; tt < 2; ++tt) C.acc[ft][tt] = (f32x16){};
    const lptr a0 = sm + G1_W + 3 * WC * 1024 + lane * 16;
    lptr b0[2];
#pragma unroll
    for (int tt = 0; tt < 2; ++tt) b0[tt] = sm + krow * 256 + (((2 * wr + tt) ^ q) * 64) + (g & 1) * 32 + p * 8;
    sbar();
    STAMP(7); CSTAMP(18);
#pragma unroll
    for (int tt = 0; tt < 2; ++tt) C.Bf[0][tt] = C.rdB(b0[tt]);
#pragma unroll
    for (int ft = 0; ft < 3; ++ft) C.Af[0][ft] = *(LAS bf16x8*)(a0 + ft * 1024);
    SB();
    lptr pa = a0, pb[2] = {b0[0], b0[1]};
    int nb = 0;
#pragma unroll 1
    for (int s = 0; s < 19; ++s) {
        C.template substep<0, 1, true, false>(pa, pb);
        nb = nb + G1_SS == G1_NS * G1_SS ? 0 : nb + G1_SS;
        pa = a0 + nb; pb[0] = b0[0] + nb; pb[1] = b0[1] + nb;
        SB();
        C.template substep<1, 0, true, true>(pa, pb);
    }
    C.template substep<0, 1, true, false>(pa, pb);
    C.template substep<1, 0, false, false>(pa, pb);
    STAMP(8); CSTAMP(19);
    bar();
    char* mF = ws + OFF_MF;
    f32x2 nm2[2], k1[2], k2[2], s2[2];
#pragma unroll
    for (int tt = 0; tt < 2; ++tt) {
        const f32x2 sq = *(LAS f32x2*)(sm + G1_ST + (32 * (2 * wr + tt) + (lane & 31)) * 8);
        const float rs = __builtin_amdgcn_rcpf(sq[1]);
        nm2[tt] = (f32x2){-sq[0], -sq[0]};
        k1[tt] = (f32x2){-2.302208198f * rs, -2.302208198f * rs};
        k2[tt] = (f32x2){-0.1029432397f * rs * rs * rs, -0.1029432397f * rs * rs * rs};
        s2[tt] = (f32x2){sq[1] * sq[1], sq[1] * sq[1]};
    }
#pragma unroll
    for (int ft = 0; ft < 3; ++ft) {
        const int fl = 16 * (3 * WC + ft) + 4 * hh;
        lptr cp = sm + G1_CS + 3072 + fl * 4;
        const f32x4 cg0 = *(LAS f32x4*)cp, cg1 = *(LAS f32x4*)(cp + 32), cu0 = *(LAS f32x4*)(cp + 384), cu1 = *(LAS f32x4*)(cp + 416);
#pragma unroll
        for (int tt = 0; tt < 2; ++tt) {
            const int tg = t0 + 32 * (2 * wr + tt) + (lane & 31);
            u32x4 o;
#pragma unroll
            for (int r = 0; r < 4; ++r) {
                const f32x2 cg = r < 2 ? (f32x2){cg0[2 * r], cg0[2 * r + 1]} : (f32x2){cg1[2 * r - 4], cg1[2 * r - 3]};
                const f32x2 cu = r < 2 ? (f32x2){cu0[2 * r], cu0[2 * r + 1]} : (f32x2){cu1[2 * r - 4], cu1[2 * r - 3]};
                const f32x2 ag = {C.acc[ft][tt][2 * r], C.acc[ft][tt][2 * r + 1]}, au = {C.acc[ft][tt][8 + 2 * r], C.acc[ft][tt][9 + 2 * r]};
                const f32x2 G = __builtin_elementwise_fma(nm2[tt], cg, ag), U = __builtin_elementwise_fma(nm2[tt], cu, au);
                const f32x2 ea = G * __builtin_elementwise_fma(G * G, k2[tt], k1[tt]);
                const f32x2 den = __builtin_elementwise_fma((f32x2){__builtin_amdgcn_exp2f(ea[0]), __builtin_amdgcn_exp2f(ea[1])}, s2[tt], s2[tt]);
                const f32x2 mv = (G * U) * (f32x2){__builtin_amdgcn_rcpf(den[0]), __builtin_amdgcn_rcpf(den[1])};
                o[r] = pk_bf16(mv[0], mv[1]);
            }
            const int ftg = 6 * cb + 3 * WC + ft;
            const int lp = (tg & 15) + 16 * (2 * (ftg & 1) + hh);
            st_dev(mF + (((size_t)(tg >> 4) * 48 + (ftg >> 1)) * 64 + lp) * 16, o);
        }
    }
}

__device__ __forceinline__ void g1_loader(char* __restrict__ ws, lptr sm, int lw, int lane, int t0, int cb, unsigned tag) {
    const unsigned sm0 = (unsigned)(size_t)sm;
    {
        const char* hT = ws + OFF_HT;
        const char* wsrc = ws + OFF_WG + (size_t)cb * 40 * 6144 + 3 * lw * 1024;
        unsigned vh[2];
#pragma unroll
        for (int j = 0; j < 2; ++j) {
            const int o = (2 * lw + j) * 1024 + lane * 16, k = o >> 8, c = ((o >> 4) & 15) ^ ((k & 3) << 2);
            vh[j] = (unsigned)(k * (T * 2) + t0 * 2 + c * 16);
        }
        const unsigned vl = lane * 16;
        unsigned long long* st = (unsigned long long*)(ws + OFF_ST);
        auto stage = [&](int s) {
            const unsigned lb = sm0 + (s % G1_NS) * G1_SS;
            const char* hs = hT + (size_t)s * 32 * (T * 2);
            const char* wsx = wsrc + (size_t)s * 12288;
            dma16s(hs, vh[0], lb + (2 * lw) * 1024);
            if (s >= 4) dma16s(wsx, vl, lb + G1_W + (3 * lw) * 1024);
            dma16s(hs, vh[1], lb + (2 * lw + 1) * 1024);
            if (s >= 4) { dma16s(wsx + 1024, vl, lb + G1_W + (3 * lw + 1) * 1024); dma16s(wsx + 2048, vl, lb + G1_W + (3 * lw + 2) * 1024); }
        };
        u32x4 gv[3];
        unsigned long long g0, g1;
        float cs[8], sv[16];
        #pragma unroll
        for (int s = 0; s < 4; ++s) stage(s);
        {
            const char* hp = hT + (size_t)(160 * lw) * (T * 2) + (t0 + 8 * cb) * 2;
            const unsigned go = (unsigned)lane * (T * 2);
            asm volatile("global_load_dwordx4 %0, %1, %2 sc1" : "=v"(gv[0]) : "v"(go), "s"(hp) : "memory");
            asm volatile("global_load_dwordx4 %0, %1, %2 sc1" : "=v"(gv[1]) : "v"(go), "s"(hp + (size_t)64 * (T * 2)) : "memory");
            asm volatile("global_load_dwordx4 %0, %1, %2 sc1" : "=v"(gv[2]) : "v"(lane < 32 ? go : 0u), "s"(hp + (size_t)128 * (T * 2)) : "memory");
        }
        wait_vm<9>();
        sbar();
#pragma unroll
        for (int s = 0; s < 19; ++s) {
            const int last = (s + 3) < 19 ? (s + 3) : 19;
            if (s == 3) asm volatile("s_waitcnt vmcnt(10)" : "+v"(gv[0]), "+v"(gv[1]), "+v"(gv[2]) :: "memory");
            else if (s == 17) asm volatile("s_waitcnt vmcnt(5)" : "+v"(g0), "+v"(g1) :: "memory");
            else wait_vm_rt<30>(s == 0 ? 7 : (s == 1 ? 10 : (s == 2 ? 13 : 5 * (last - (s + 1)) + (s == 15 || s == 16 ? 2 : 0))));
            sbar();
            if (s + 4 < 20) stage(s + 4);
            if (s == 14) {
                asm volatile("global_load_dwordx2 %0, %1, %2 sc1" : "=v"(g0) : "v"(lane * 8), "s"(st + t0) : "memory");
                asm volatile("global_load_dwordx2 %0, %1, %2 sc1" : "=v"(g1) : "v"(lane * 8), "s"(st + t0 + 64) : "memory");
            }
            if (s >= 3 && s <= 6) {
#pragma unroll
                for (int e = 2 * (s - 3); e < 2 * (s - 3) + 2; ++e) {
                    const unsigned c32 = (unsigned)__builtin_amdgcn_readlane((int)gv[0][e >> 1], 0);
                    cs[e] = (e & 1) ? bfhi(c32) : bflo(c32);
                    const float d0 = ((e & 1) ? bfhi(gv[0][e >> 1]) : bflo(gv[0][e >> 1])) - cs[e], d1 = ((e & 1) ? bfhi(gv[1][e >> 1]) : bflo(gv[1][e >> 1])) - cs[e];
                    const float d2 = lane < 32 ? ((e & 1) ? bfhi(gv[2][e >> 1]) : bflo(gv[2][e >> 1])) - cs[e] : 0.f;
                    sv[e] = d0 + d1 + d2; sv[8 + e] = d0 * d0 + d1 * d1 + d2 * d2;
                    PIN(sv[e]); PIN(sv[8 + e]);
                }
            }
            if (s == 7) {
#pragma unroll
                for (int j = 0; j < 8; ++j) {
                    const auto r = __builtin_amdgcn_permlane32_swap(__float_as_uint(sv[j]), __float_as_uint(sv[j + 8]), false, false);
                    sv[j] = __uint_as_float(r[0]) + __uint_as_float(r[1]); PIN(sv[j]);
                }
            }
            if (s == 8) {
#pragma unroll
                for (int k = 0; k < 4; ++k) {
                    const auto r = __builtin_amdgcn_permlane16_swap(__float_as_uint(sv[k]), __float_as_uint(sv[k + 4]), false, false);
                    sv[k] = __uint_as_float(r[0]) + __uint_as_float(r[1]);
                }
#pragma unroll
                for (int k = 0; k < 4; ++k) { sv[k] = row16_sum(sv[k]); PIN(sv[k]); }
            }
            if (s == 9) {
                if ((lane & 15) == 0) *(LAS f32x4*)(sm + G1_SP + lw * 96 + (lane >> 4) * 16) = (f32x4){sv[0], sv[1], sv[2], sv[3]};
                if (lane == 0) { *(LAS f32x4*)(sm + G1_SP + lw * 96 + 64) = (f32x4){cs[0], cs[1], cs[2], cs[3]}; *(LAS f32x4*)(sm + G1_SP + lw * 96 + 80) = (f32x4){cs[4], cs[5], cs[6], cs[7]}; }
                asm volatile("s_waitcnt lgkmcnt(0)" ::: "memory");
            }
            if (s == 10 && lw == 3 && lane < 8) {
                float pm[4], m2 = 0.f, mean = 0.f;
#pragma unroll
                for (int i = 0; i < 4; ++i) {
                    const float a = *(LAS float*)(sm + G1_SP + i * 96 + lane * 4) * (1.0f / 160.0f), q = *(LAS float*)(sm + G1_SP + i * 96 + 32 + lane * 4);
                    pm[i] = *(LAS float*)(sm + G1_SP + i * 96 + 64 + lane * 4) + a;
                    m2 += q - 160.0f * a * a; mean += 0.25f * pm[i];
                }
#pragma unroll
                for (int i = 0; i < 4; ++i) m2 += 160.0f * (pm[i] - mean) * (pm[i] - mean);
                const float var = fmaxf(m2 * (1.0f / (float)D), 0.f);
                const unsigned mb = pk_bf16(mean, rbf(sqrtf(rbf(rbf(var) + 1e-5f))));
                __hip_atomic_store(st + t0 + 8 * cb + lane, ((unsigned long long)tag << 32) | mb, __ATOMIC_RELAXED, __HIP_MEMORY_SCOPE_AGENT);
            }
        }
        if (lw == 0) {
            for (unsigned spins = 0; !__all((unsigned)(g0 >> 32) == tag && (unsigned)(g1 >> 32) == tag) && spins < (1u << 18); ++spins) {
                __builtin_amdgcn_s_sleep(2);
                g0 = __hip_atomic_load(st + t0 + lane, __ATOMIC_RELAXED, __HIP_MEMORY_SCOPE_AGENT);
                g1 = __hip_atomic_load(st + t0 + 64 + lane, __ATOMIC_RELAXED, __HIP_MEMORY_SCOPE_AGENT);
            }
            *(LAS f32x2*)(sm + G1_ST + lane * 8) = (f32x2){bflo((unsigned)g0), bfhi((unsigned)g0)};
            *(LAS f32x2*)(sm + G1_ST + (64 + lane) * 8) = (f32x2){bflo((unsigned)g1), bfhi((unsigned)g1)};
        }
        bar();
    }
}

template <int ND>
__device__ __forceinline__ void g2_compute(lptr sm, int lane, int th, int dl0, int t0, int d0, bf16_t* __restrict__ out PROF_ARG) {
    f32x4 acc[ND][2] = {};
    bf16x8 A[2][ND], B[2][2];
    const lptr pb0 = sm + lane * 16 + 2 * th * 4096, pa0 = sm + lane * 16 + G2_W + dl0 * 1024;
    auto ld = [&](int P, lptr pa, lptr pb, int fl) {
#pragma unroll
        for (int tt = 0; tt < 2; ++tt) B[P][tt] = *(LAS bf16x8*)(pb + (tt * 4 + fl) * 1024);
#pragma unroll
        for (int dt = 0; dt < ND; ++dt) A[P][dt] = *(LAS bf16x8*)(pa + (fl * 5 + dt) * 1024);
        SB();
    };
    auto mm = [&](int P) {
#pragma unroll
        for (int dt = 0; dt < ND; ++dt)
#pragma unroll
            for (int tt = 0; tt < 2; ++tt) { acc[dt][tt] = __builtin_amdgcn_mfma_f32_16x16x32_bf16(A[P][dt], B[P][tt], acc[dt][tt], 0, 0, 0); PIN(acc[dt][tt]); SB(); }
    };
    sbar();
    STAMP(11); CSTAMP(20);
    ld(0, pa0, pb0, 0);
    lptr pa = pa0, pb = pb0;
    int nb = 0;
#pragma unroll 1
    for (int s = 0; s < 12; ++s) {
        ld(1, pa, pb, 1); mm(0);
        ld(0, pa, pb, 2); mm(1);
        ld(1, pa, pb, 3); mm(0);
        if (s < 11) {
            nb = nb + G2_SS == G2_NS * G2_SS ? 0 : nb + G2_SS;
            pa = pa0 + nb; pb = pb0 + nb;
            bar();
            ld(0, pa, pb, 0);
        }
        mm(1);
    }
    STAMP(12); CSTAMP(21);
    bar();
    const int g = lane >> 4, q = (lane & 15) >> 2, p = lane & 3;
#pragma unroll
    for (int dt = 0; dt < ND; ++dt)
#pragma unroll
        for (int tt = 0; tt < 2; ++tt) {
            const int row = 16 * (dl0 + dt) + 4 * g + q, seg = (2 * th + tt) ^ ((row >> 1) & 3);
            const u32x2 hv = tr_read(sm + G2_H + row * 128 + seg * 32 + p * 8);
            const unsigned o01 = pk_bf16(acc[dt][tt][0], acc[dt][tt][1]), o23 = pk_bf16(acc[dt][tt][2], acc[dt][tt][3]);
            const u32x2 r = {pk_bf16(bflo(hv[0]) + bflo(o01), bfhi(hv[0]) + bfhi(o01)), pk_bf16(bflo(hv[1]) + bflo(o23), bfhi(hv[1]) + bfhi(o23))};
            *(LAS u32x2*)(sm + (16 * (2 * th + tt) + (lane & 15)) * 176 + (16 * (dl0 + dt) + 4 * g) * 2) = r;
        }
}

extern "C" __global__ void s4d_geglu_block_end();
extern "C" __global__ __launch_bounds__(512, 2) void s4d_geglu_block(Params P PROF_ARG) {
    extern __shared__ __attribute__((aligned(16))) char smem[];
    lptr sm = (lptr)smem;
    STAMP(0);
    if (uni(threadIdx.x >> 6) == 7) {
        unsigned long long pc; unsigned off, len;
        asm volatile("s_getpc_b64 %0\n.Lpc_anchor_%=:\n\ts_mov_b32 %1, .Lpc_anchor_%=-s4d_geglu_block\n\ts_mov_b32 %2, s4d_geglu_block_end-s4d_geglu_block" : "=s"(pc), "=s"(off), "=s"(len));
        const unsigned o = (blockIdx.x >> 3) * 2048 + (threadIdx.x & 63) * 32;
        if (o < len) dma4((const void*)(pc - off), o, (unsigned)(size_t)sm + G1_ST);
    }
    const int tid = threadIdx.x, lane = tid & 63, w = uni(tid >> 6), b = blockIdx.x;
    const unsigned sm0 = (unsigned)(size_t)sm;
    char* ws = P.ws;
    unsigned* fl = (unsigned*)(ws + OFF_FL);
    const unsigned tag = fl[FL_E] + 1u;
    const int grp = b & 7, idx = b >> 3, grp0 = grp;
    const int rb = (grp & 1) * 8 + (idx & 7), cb = (grp >> 1) * 4 + (idx >> 3);
    const int tb = (grp >> 1) * 8 + (idx & 7), db = (grp & 1) * 4 + (idx >> 3);

    if (b < 160) conv_wg(P, b, cb, tag, sm PROF_FWD); else spare_wg(P, b - 160, cb, tag, sm PROF_FWD);

    if (w == 4) wait_flags<160, FL_H_S>(fl + grp0 * FL_REP + FL_H, tag, lane);
    if (w == 7) wait_flags<48, FL_W_S>(fl + grp0 * FL_REP + FL_WD, tag, lane);
    bar();
    STAMP(6);
    if (w >= 4) g1_loader(ws, sm, w & 3, lane, 128 * rb, cb, tag);
    else if (w & 1) g1_compute<1>(sm, ws, w >> 1, lane, 128 * rb, cb PROF_FWD); else g1_compute<0>(sm, ws, w >> 1, lane, 128 * rb, cb PROF_FWD);

    const int t0 = 64 * tb, d0 = 80 * db;
    if (w >= 4) {
        const int lw = w & 3;
        const char* ms = ws + OFF_MF + (size_t)(4 * tb + lw) * 48 * 1024;
        const char* wsrc = ws + OFF_WD + (size_t)lw * (40 * 1024) + (size_t)(5 * db) * 1024;
        const unsigned vl = lane * 16;
        auto stage_w = [&](int s) {
#pragma unroll
            for (int j = 0; j < 5; ++j) dma16s(wsrc + (size_t)(4 * s) * (40 * 1024) + j * 1024, vl, sm0 + (s % G2_NS) * G2_SS + G2_W + (5 * lw + j) * 1024);
        };
        auto stage_m = [&](int s) {
#pragma unroll
            for (int j = 0; j < 4; ++j) dma16s(ms + (size_t)(4 * s + j) * 1024, vl, sm0 + (s % G2_NS) * G2_SS + (4 * lw + j) * 1024);
        };
        stage_w(0); stage_w(1); stage_w(2);
#pragma unroll
        for (int j = 0; j < 3; ++j) {
            const int i = lw + 4 * j < 10 ? lw + 4 * j : lw;
            const int o = i * 1024 + lane * 16, row = o >> 7, c = ((o >> 4) & 7) ^ (((row >> 1) & 3) << 1);
            dma16s(ws + OFF_HT, (unsigned)((d0 + row) * (T * 2) + t0 * 2 + c * 16), sm0 + G2_H + i * 1024);
        }
        sbar();
        if (lw == 0) wait_flags<16, FL_M_S>(fl + FL_M + 16 * (tb >> 1) * FL_M_S, tag, lane);
        sbar();
        STAMPL(10);
        stage_m(0); stage_m(1); stage_m(2);
        wait_vm<8>();
        sbar();
#pragma unroll
        for (int s = 0; s < 11; ++s) {
            if (s == 0) wait_vm<4>(); else if (s <= 9) wait_vm<9>(); else wait_vm<0>();
            sbar();
            if (s + 3 < 12) { stage_m(s + 3); stage_w(s + 3); }
        }
        sbar();
    } else {
        STAMP(28);
        wait_vm<0>();
        sbar();
        if (tid == 0) set_flag(fl + FL_M + (16 * rb + cb) * FL_M_S, tag);
        STAMP(9);
        sbar();
        const int th = w & 1;
        if (w >> 1) g2_compute<2>(sm, lane, th, 3, t0, d0, P.out PROF_FWD); else g2_compute<3>(sm, lane, th, 0, t0, d0, P.out PROF_FWD);
    }
    bar();
    for (int ch = tid; ch < 640; ch += 512) {
        const int row = ch / 10, c = ch % 10;
        *(u32x4*)(P.out + (size_t)(t0 + row) * D + d0 + c * 8) = *(LAS u32x4*)(sm + row * 176 + c * 16);
    }
    if (b == 0 && tid == 0) fl[FL_E] = tag;
    STAMP(15);
}

extern "C" __global__ void s4d_geglu_block_end() {}

extern "C" void kernel_launch(void* const* d_in, const int* in_sizes, int n_in, void* d_out, int out_size, void* d_ws, size_t ws_size, hipStream_t stream) {
    Params P;
    P.x = (const bf16_t*)d_in[0]; P.logA = (const bf16_t*)d_in[1]; P.Aim = (const bf16_t*)d_in[2]; P.Cre = (const bf16_t*)d_in[3]; P.Cim = (const bf16_t*)d_in[4];
    P.logdt = (const bf16_t*)d_in[5]; P.Dskip = (const bf16_t*)d_in[6]; P.wg = (const bf16_t*)d_in[8]; P.wu = (const bf16_t*)d_in[9]; P.wd = (const bf16_t*)d_in[10];
    P.ws = (char*)d_ws; P.out = (bf16_t*)d_out;
    (void)hipFuncSetAttribute((const void*)s4d_geglu_block, hipFuncAttributeMaxDynamicSharedMemorySize, LDS_BYTES);
    s4d_geglu_block<<<256, 512, LDS_BYTES, stream>>>(P);
}
```

```cpp
#include <hip/hip_runtime.h>
#include <stdint.h>

typedef unsigned short bf16_t;
typedef short s16x4 __attribute__((ext_vector_type(4)));
typedef __bf16 bf16x8 __attribute__((ext_vector_type(8)));
typedef __bf16 bf16x2 __attribute__((ext_vector_type(2)));
typedef _Float16 h16x8 __attribute__((ext_vector_type(8)));
typedef _Float16 h16x2 __attribute__((ext_vector_type(2)));
typedef float f32x2 __attribute__((ext_vector_type(2)));
typedef float f32x4 __attribute__((ext_vector_type(4)));
typedef float f32x16 __attribute__((ext_vector_type(16)));
typedef unsigned u32x2 __attribute__((ext_vector_type(2)));
typedef unsigned u32x4 __attribute__((ext_vector_type(4)));
#define LAS __attribute__((address_space(3)))
typedef LAS char* lptr;

constexpr int T = 2048, D = 640, NM = 192, F = 1536;
constexpr size_t OFF_S1 = 0;
constexpr size_t OFF_HT = OFF_S1 + (size_t)T * 8;
constexpr size_t OFF_CS = OFF_HT + (size_t)T * D * 2;
constexpr size_t OFF_MF = OFF_CS + 65536;
constexpr size_t OFF_WD = OFF_MF + (size_t)T * F * 2;
constexpr size_t OFF_WG = OFF_WD + (size_t)F * D * 2;
constexpr size_t OFF_ST = OFF_WG + (size_t)2 * F * D * 2;
constexpr size_t OFF_FL = OFF_ST + (size_t)T * 8;
constexpr size_t OFF_PROF = OFF_FL + 163840;
constexpr int FL_E = 0;
constexpr int FL_REP = 4096;
constexpr int FL_LN = FL_REP, FL_LN_S = 16;
constexpr int FL_H = FL_LN + 1024, FL_H_S = 8;
constexpr int FL_WG = FL_H + 1280, FL_WD = FL_WG + 96 * 8, FL_W_S = 8;
constexpr int FL_M = 64, FL_M_S = 8;
static_assert(FL_WD + 48 * FL_W_S <= 2 * FL_REP && FL_M + 256 * FL_M_S <= FL_REP, "flag map");

#define STAMP(i)
#define STAMPL(i)
#define CSTAMP(i)
#define PROF_ARG
#define PROF_PASS
#define PROF_FWD

__device__ __forceinline__ float bf2f(unsigned v) { return __uint_as_float(v << 16); }
__device__ __forceinline__ float bflo(unsigned v) { return __uint_as_float(v << 16); }
__device__ __forceinline__ float bfhi(unsigned v) { return __uint_as_float(v & 0xffff0000u); }
__device__ __forceinline__ unsigned pk_bf16(float lo, float hi) { f32x2 v = {lo, hi}; return __builtin_bit_cast(unsigned, __builtin_convertvector(v, bf16x2)); }
__device__ __forceinline__ float rbf(float f) { return bflo(pk_bf16(f, f)); }
__device__ __forceinline__ unsigned pk_f16(f32x2 v) { return __builtin_bit_cast(unsigned, __builtin_convertvector(v, h16x2)); }
template <int CTRL> __device__ __forceinline__ float dppf(float x) { return __int_as_float(__builtin_amdgcn_update_dpp(0, __float_as_int(x), CTRL, 0xF, 0xF, true)); }
__device__ __forceinline__ float row16_sum(float x) { x += dppf<0xB1>(x); x += dppf<0x4E>(x); x += dppf<0x141>(x); x += dppf<0x140>(x); return x; }
__device__ __forceinline__ float row16_max(float x) { x = fmaxf(x, dppf<0xB1>(x)); x = fmaxf(x, dppf<0x4E>(x)); x = fmaxf(x, dppf<0x141>(x)); x = fmaxf(x, dppf<0x140>(x)); return x; }
__device__ __forceinline__ float xsum16_32(float x) {
    const unsigned u = __float_as_uint(x);
    const auto r = __builtin_amdgcn_permlane16_swap(u, u, false, false);
    const unsigned v = __float_as_uint(__uint_as_float(r[0]) + __uint_as_float(r[1]));
    const auto q = __builtin_amdgcn_permlane32_swap(v, v, false, false);
    return __uint_as_float(q[0]) + __uint_as_float(q[1]);
}
__device__ __forceinline__ u32x2 tr_read(lptr p) { return __builtin_bit_cast(u32x2, __builtin_amdgcn_ds_read_tr16_b64_v4i16((LAS s16x4*)p)); }
__device__ __forceinline__ u32x4 tr_read2(lptr p, int off2) { const u32x2 r1 = tr_read(p), r2 = tr_read(p + off2); return (u32x4){r1[0], r1[1], r2[0], r2[1]}; }
__device__ __forceinline__ int uni(int v) { return __builtin_amdgcn_readfirstlane(v); }

__device__ __forceinline__ void dma16(const void* sbase, unsigned voff, unsigned ldsaddr) {
    asm volatile("s_mov_b32 m0, %2\n\ts_nop 0\n\tglobal_load_lds_dwordx4 %0, %1" :: "v"(voff), "s"(sbase), "s"(ldsaddr) : "memory");
}
__device__ __forceinline__ void dma4(const void* sbase, unsigned voff, unsigned ldsaddr) {
    asm volatile("s_mov_b32 m0, %2\n\ts_nop 0\n\tglobal_load_lds_dword %0, %1" :: "v"(voff), "s"(sbase), "s"(ldsaddr) : "memory");
}
template <int N> __device__ __forceinline__ void wait_vm() { asm volatile("s_waitcnt vmcnt(%0)" :: "n"(N) : "memory"); }
template <int N> __device__ __forceinline__ void wait_vm_rt(int n) {
    if (n >= N) wait_vm<N>(); else if constexpr (N > 0) wait_vm_rt<N - 1>(n);
}
__device__ __forceinline__ void bar() { asm volatile("s_waitcnt lgkmcnt(0)\n\ts_barrier" ::: "memory"); }
__device__ __forceinline__ void sbar() { asm volatile("s_barrier" ::: "memory"); }
#define SB() __builtin_amdgcn_sched_barrier(0)
#define PIN(x) asm volatile("" : "+v"(x))
__device__ __forceinline__ void st_dev(void* p, u32x4 v) { asm volatile("global_store_dwordx4 %0, %1, off sc1\n\ts_nop 1" :: "v"(p), "v"(v) : "memory"); }
__device__ __forceinline__ void st_dev4(void* p, float v) { asm volatile("global_store_dword %0, %1, off sc1\n\ts_nop 1" :: "v"(p), "v"(v) : "memory"); }
__device__ __forceinline__ void dma16s(const void* sbase, unsigned voff, unsigned ldsaddr) {
    asm volatile("s_mov_b32 m0, %2\n\ts_nop 0\n\tglobal_load_lds_dwordx4 %0, %1 sc1" :: "v"(voff), "s"(sbase), "s"(ldsaddr) : "memory");
}
__device__ __forceinline__ void dma4s(const void* sbase, unsigned voff, unsigned ldsaddr) {
    asm volatile("s_mov_b32 m0, %2\n\ts_nop 0\n\tglobal_load_lds_dword %0, %1 sc1" :: "v"(voff), "s"(sbase), "s"(ldsaddr) : "memory");
}
__device__ __forceinline__ void set_flag(unsigned* f, unsigned tag) { __hip_atomic_store(f, tag, __ATOMIC_RELAXED, __HIP_MEMORY_SCOPE_AGENT); }
__device__ __forceinline__ void set_flag_rep(unsigned* f, unsigned tag, int lane) { __hip_atomic_store(f + lane * FL_REP, tag, __ATOMIC_RELAXED, __HIP_MEMORY_SCOPE_AGENT); }
template <int N, int S>
__device__ __forceinline__ void wait_flags(unsigned* f, unsigned tag, int lane) {
    for (unsigned spins = 0; spins < (1u << 18); ++spins) {
        bool ok = true;
        if (lane < N) ok = __hip_atomic_load(f + lane * S, __ATOMIC_RELAXED, __HIP_MEMORY_SCOPE_AGENT) == tag;
        if (N > 64 && lane + 64 < N) ok &= __hip_atomic_load(f + (64 + lane) * S, __ATOMIC_RELAXED, __HIP_MEMORY_SCOPE_AGENT) == tag;
        if (N > 128 && lane + 128 < N) ok &= __hip_atomic_load(f + (128 + lane) * S, __ATOMIC_RELAXED, __HIP_MEMORY_SCOPE_AGENT) == tag;
        if (__all(ok)) break;
        __builtin_amdgcn_s_sleep(1);
    }
    asm volatile("" ::: "memory");
}

__device__ __forceinline__ unsigned epoch_tag(const char* ws) {
    unsigned e;
    asm volatile("s_load_dword %0, %1, 0x0\n\ts_waitcnt lgkmcnt(0)" : "=s"(e) : "s"(ws + OFF_FL + FL_E * 4) : "memory");
    return e + 1u;
}

constexpr int G1_NS = 7, G1_SS = 20480, G1_W = 8192;
constexpr int G2_NS = 4, G2_SS = 36 * 1024, G2_W = 16 * 1024;
constexpr int G2_H = G2_NS * G2_SS;
constexpr int G1_CS = G2_H + 10240;
constexpr int G1_ST = G1_CS + 3840;
constexpr int G1_SP = G1_ST + 1024;
constexpr int LDS_BYTES = 163840;
static_assert(G1_SP + 512 <= LDS_BYTES, "LDS map");
constexpr int KG_ROW = 272;
constexpr int KG_WAVE = 57 * KG_ROW;
constexpr int CV_KCH = 8320;
__host__ __device__ constexpr int cv_k(int c) { return (4 + c) * KG_WAVE + 3072; }
__host__ __device__ constexpr int cv_kc(int c) { return cv_k(c) + CV_KCH; }
static_assert(3072 + CV_KCH + 1296 <= KG_WAVE, "per-channel area");
constexpr int CV_X = 8 * KG_WAVE + 7 * KG_ROW + 16;
constexpr int CV_U = CV_X + 128 * 128;
constexpr int CV_M = CV_U + 144 * 128;
static_assert(CV_X % 128 == 0 || true, "");
static_assert(CV_M + 32 + 256 <= 163840 && CV_X % 16 == 0, "LDS map");
constexpr int LN_ROW = 1344;

__device__ __forceinline__ unsigned ln1_unit(int j, const bf16_t* __restrict__ x, char* __restrict__ ws PROF_ARG) {
    const int tid = threadIdx.x, r = tid >> 4, l16 = tid & 15, lane = tid & 63, w = uni(tid >> 6);
    const int t = 32 * j + r;
    u32x4 v[5];
#pragma unroll
    for (int i = 0; i < 5; ++i) v[i] = *(const u32x4*)(x + (size_t)t * D + (l16 + 16 * i) * 8);
    const unsigned tag = epoch_tag(ws);
    f32x2 f[20];
    f32x2 s2 = {0.f, 0.f};
#pragma unroll
    for (int i = 0; i < 5; ++i)
#pragma unroll
        for (int e = 0; e < 4; ++e) { f[4 * i + e] = (f32x2){bflo(v[i][e]), bfhi(v[i][e])}; s2 += f[4 * i + e]; }
    const float mean = row16_sum(s2[0] + s2[1]) * (1.0f / (float)D);
    f32x2 q2 = {0.f, 0.f};
#pragma unroll
    for (int i = 0; i < 20; ++i) { const f32x2 dv = f[i] - mean; q2 += dv * dv; }
    const float var = row16_sum(q2[0] + q2[1]) * (1.0f / (float)D);
    const float rs = __builtin_amdgcn_rcpf(rbf(sqrtf(rbf(rbf(var) + 1e-5f)))), nm = -rbf(mean) * rs;
    u32x4 o;
#pragma unroll
    for (int i = 0; i < 4; ++i) {
        const unsigned a = (unsigned)__builtin_amdgcn_readlane((int)__float_as_uint(rs), 16 * i), c = (unsigned)__builtin_amdgcn_readlane((int)__float_as_uint(nm), 16 * i);
        o[i] = lane == 0 ? a : c;
    }
    if (lane < 2) st_dev(ws + OFF_S1 + (size_t)lane * (T * 4) + (32 * j + 4 * w) * 4, o);
    STAMP(24);
    return tag;
}

__device__ __forceinline__ void wgtile_unit(int b, const bf16_t* __restrict__ wg, const bf16_t* __restrict__ wu, char* __restrict__ ws, lptr sm) {
    const int tid = threadIdx.x, lane = tid & 63, w = uni(tid >> 6);
    const int j = b >> 2, kq = b & 3;
    const int c16 = tid & 15, r0 = tid >> 4;
    u32x4 v[5];
    const bf16_t* src = (c16 < 8 ? wg : wu) + (size_t)(160 * kq + r0) * F + 64 * j + (c16 & 7) * 8;
#pragma unroll
    for (int i = 0; i < 5; ++i) v[i] = *(const u32x4*)(src + (size_t)(32 * i) * F);
    f32x2 a[4] = {};
#pragma unroll
    for (int i = 0; i < 5; ++i) {
        const int k = 32 * i + r0;
        *(LAS u32x4*)(sm + k * 256 + (((c16 >> 1) ^ (k & 3)) * 32) + (c16 & 1) * 16) = v[i];
#pragma unroll
        for (int e = 0; e < 4; ++e) a[e] += (f32x2){bflo(v[i][e]), bfhi(v[i][e])};
    }
#pragma unroll
    for (int e = 0; e < 4; ++e)
#pragma unroll
        for (int hf = 0; hf < 2; ++hf) {
            const float s = xsum16_32(a[e][hf]);
            if (lane < 16) *(LAS float*)(sm + 40960 + (w * 128 + c16 * 8 + 2 * e + hf) * 4) = s;
        }
    bar();
    const int g = lane >> 4, q = (lane & 15) >> 2, p = lane & 3;
    u32x4 o[5];
#pragma unroll
    for (int i = 0; i < 5; ++i) {
        const int item = w + 8 * i, ftl = item & 3, sub = item >> 2;
        const int k = 16 * sub + 4 * (g >> 1) + q;
        o[i] = tr_read2(sm + k * 256 + ((((g & 1) * 4 + ftl) ^ q) * 32) + p * 8, 8 * 256);
    }
#pragma unroll
    for (int i = 0; i < 5; ++i) {
        const int item = w + 8 * i, ftl = item & 3, sub = item >> 2;
        const int ftg = 4 * j + ftl, cb = ftg / 6, ft = ftg % 6;
        st_dev(ws + OFF_WG + ((((size_t)cb * 40 + 10 * kq + sub) * 6 + ft) * 64 + lane) * 16, o[i]);
    }
    if (tid < 128) {
        float s = 0.f;
#pragma unroll
        for (int i = 0; i < 8; ++i) s += *(LAS float*)(sm + 40960 + (i * 128 + tid) * 4);
        st_dev4(ws + OFF_CS + (size_t)(kq * 2 * F + (tid >> 6) * F + 64 * j + (tid & 63)) * 4, s);
    }
}

__device__ __forceinline__ void wdtile_unit(int fb, const bf16_t* __restrict__ wd, char* __restrict__ ws, lptr sm) {
    const int tid = threadIdx.x, lane = tid & 63, w = uni(tid >> 6);
    u32x4 v[5];
#pragma unroll
    for (int i = 0; i < 5; ++i) { const int ch = tid + 512 * i, row = ch / 80, c = ch % 80; v[i] = *(const u32x4*)(wd + (size_t)(32 * fb + row) * D + c * 8); }
#pragma unroll
    for (int i = 0; i < 5; ++i) { const int ch = tid + 512 * i, row = ch / 80, c = ch % 80; *(LAS u32x4*)(sm + row * LN_ROW + c * 16) = v[i]; }
    bar();
    const int g = lane >> 4, q = (lane & 15) >> 2, p = lane & 3;
    u32x4 o[5];
#pragma unroll
    for (int i = 0; i < 5; ++i) o[i] = tr_read2(sm + (16 * (g >> 1) + 4 * (g & 1) + q) * LN_ROW + (16 * (w + 8 * i) + 4 * p) * 2, 8 * LN_ROW);
#pragma unroll
    for (int i = 0; i < 5; ++i) st_dev(ws + OFF_WD + (((size_t)fb * 40 + w + 8 * i) * 64 + lane) * 16, o[i]);
}

__device__ __forceinline__ f32x2 cmul(f32x2 a, f32x2 b) {
    const f32x2 t = (f32x2){a[0], a[0]} * b;
    const f32x2 bs = {-b[1], b[0]};
    return __builtin_elementwise_fma((f32x2){a[1], a[1]}, bs, t);
}
__device__ __forceinline__ f32x2 cexp_p(float arl, float air, float p) {
    const float mag = __builtin_amdgcn_exp2f(arl * p);
    float rev = air * p; rev -= rintf(rev);
    return (f32x2){mag * __builtin_amdgcn_cosf(rev), mag * __builtin_amdgcn_sinf(rev)};
}
struct KgenIn { unsigned ldt, la, ai, cr[3], ci[3]; };
__device__ __forceinline__ void kgen_load(KgenIn& k, int d, int third, int lane, const bf16_t* __restrict__ logA, const bf16_t* __restrict__ Aim,
                                          const bf16_t* __restrict__ Cre, const bf16_t* __restrict__ Cim, const bf16_t* __restrict__ logdt) {
    k.ldt = logdt[d]; k.la = logA[d * NM + 64 * third + lane]; k.ai = Aim[d * NM + 64 * third + lane];
#pragma unroll
    for (int i = 0; i < 3; ++i) { k.cr[i] = Cre[d * NM + 64 * i + lane]; k.ci[i] = Cim[d * NM + 64 * i + lane]; }
}
__device__ __forceinline__ void kgen_task(const KgenIn& k, int third, int lane, lptr tab, f32x4 (&acc)[3], float& unscale) {
    const float dt = rbf(expf(bf2f(k.ldt)));
    float mx = 0.f;
#pragma unroll
    for (int i = 0; i < 3; ++i) mx = fmaxf(mx, fmaxf(fabsf(bf2f(k.cr[i])), fabsf(bf2f(k.ci[i]))));
    mx = row16_max(mx);
    {
        const auto r1 = __builtin_amdgcn_permlane16_swap(__float_as_uint(mx), __float_as_uint(mx), false, false);
        mx = fmaxf(__uint_as_float(r1[0]), __uint_as_float(r1[1]));
        const auto r2 = __builtin_amdgcn_permlane32_swap(__float_as_uint(mx), __float_as_uint(mx), false, false);
        mx = fmaxf(__uint_as_float(r2[0]), __uint_as_float(r2[1]));
    }
    mx *= 2.f * dt;
    int e = (int)((__float_as_uint(mx) >> 23) & 0xff) - 126;
    e = e < -100 ? -100 : (e > 100 ? 100 : e);
    const float scale = __uint_as_float((unsigned)(127 - e) << 23);
    unscale = __uint_as_float((unsigned)(127 + e) << 23);
    const float are = -rbf(expf(bf2f(k.la))) * dt, aim = bf2f(k.ai) * dt;
    const float crw = bf2f(third == 0 ? k.cr[0] : (third == 1 ? k.cr[1] : k.cr[2])) * dt, ciw = bf2f(third == 0 ? k.ci[0] : (third == 1 ? k.ci[1] : k.ci[2])) * dt;
    const float arl = are * 1.4426950408889634f, air = aim * 0.15915494309189535f;
    lptr base = tab + 4 * lane;
#define PUT(row, z) *(LAS unsigned*)(base + (row) * KG_ROW) = pk_f16(z)
    const f32x2 w1 = cexp_p(arl, air, 1.f), w8 = cexp_p(arl, air, 8.f), H1 = cexp_p(arl, air, 128.f);
    f32x2 qv = {2.f * scale * crw, -2.f * scale * ciw};
    const f32x2 w8c = {w8[0], -w8[1]};
    f32x2 E[8], H[17];
    E[0] = (f32x2){1.f, 0.f}; E[1] = w1; H[1] = H1;
    PUT(0, qv); PUT(16, E[0]); PUT(17, E[1]); PUT(24, H1);
#pragma unroll
    for (int i = 1; i < 16; ++i) {
        qv = cmul(qv, w8c);
        H[i + 1] = cmul(H[i], H1);
        if (i < 7) E[i + 1] = cmul(E[i], w1);
        PUT(i, qv);
        if (i < 7) PUT(17 + i, E[i + 1]);
        const int hi = i + 1;
        if (hi < 4) PUT(32 + 4 * (hi - 2), H[hi]); else if (hi < 8) PUT(40 + 2 * (hi - 4), H[hi]); else PUT(48 + (hi - 8), H[hi]);
        if (i >= 7 && i < 14) PUT(25 + (i - 7), cmul(E[i - 6], H1));
        if (i >= 7 && i < 10) PUT(33 + (i - 7), cmul(E[2 * (i - 6)], H[2]));
        if (i >= 10 && i < 13) PUT(37 + (i - 10), cmul(E[2 * (i - 9)], H[3]));
        if (i >= 11 && i < 15) PUT(41 + 2 * (i - 11), cmul(H[4 + (i - 11)], E[4]));
    }
#undef PUT
    asm volatile("" ::: "memory");
    lptr rb = tab + (lane & 15) * KG_ROW + (lane >> 4) * 16;
#pragma unroll
    for (int mt = 0; mt < 3; ++mt) acc[mt] = (f32x4){};
#pragma unroll
    for (int ks = 0; ks < 4; ++ks) {
        const h16x8 bq = *(LAS h16x8*)(rb + ks * 64);
#pragma unroll
        for (int mt = 0; mt < 3; ++mt) {
            const h16x8 ar = *(LAS h16x8*)(rb + (16 + 16 * mt) * KG_ROW + ks * 64);
            acc[mt] = __builtin_amdgcn_mfma_f32_16x16x32_f16(ar, bq, acc[mt], 0, 0, 0);
        }
    }
#pragma unroll
    for (int mt = 0; mt < 3; ++mt) PIN(acc[mt]);
    asm volatile("s_waitcnt lgkmcnt(0)" ::: "memory");
}

__device__ __forceinline__ void g1_prefetch_w(char* __restrict__ ws, lptr sm, int cb, int lane, unsigned tag) {
    const unsigned sm0 = (unsigned)(size_t)sm;
    wait_flags<96, FL_W_S>((unsigned*)(ws + OFF_FL) + (blockIdx.x & 7) * FL_REP + FL_WG, tag, lane);
    const char* wsrc = ws + OFF_WG + (size_t)cb * 40 * 6144;
    const int f0 = 96 * cb;
#pragma unroll
    for (int i = 0; i < 3; ++i) {
        const unsigned co = (unsigned)(((i == 0 ? f0 + lane : (i == 1 ? (lane < 32 ? f0 + 64 + lane : F + f0 + lane - 32) : F + f0 + 32 + lane))) * 4);
#pragma unroll
        for (int kq = 0; kq < 4; ++kq) dma4s(ws + OFF_CS + kq * 2 * F * 4, co, sm0 + G1_CS + kq * 768 + i * 256);
    }
#pragma unroll
    for (int s = 0; s < 4; ++s)
#pragma unroll
        for (int i = 0; i < 12; ++i) dma16s(wsrc + (size_t)s * 12288 + i * 1024, lane * 16, sm0 + s * G1_SS + G1_W + i * 1024);
    wait_vm<48>();
#pragma unroll
    for (int i = 0; i < 3; ++i) {
        lptr cp = sm + G1_CS + i * 256 + lane * 4;
        *(LAS float*)(cp + 3072) = (*(LAS float*)cp + *(LAS float*)(cp + 768)) + (*(LAS float*)(cp + 1536) + *(LAS float*)(cp + 2304));
    }
    asm volatile("s_waitcnt vmcnt(0) lgkmcnt(0)" ::: "memory");
}

struct KTab { unsigned short v[2048]; };
constexpr int ci_of(int l) {
    if (l < 0) return 641;
    if (l < 256) return l;
    const int s = l < 512 ? 1 : (l < 1024 ? 2 : 3);
    return ((l + (1 << (s - 1)) - 1 + ((l >> s) & 1)) >> s) + 128 * s;
}
constexpr KTab make_tab() { KTab t{}; for (int y = 0; y < 2048; ++y) t.v[y] = (unsigned short)(2 * ci_of(2047 - y)); return t; }
__device__ const __attribute__((aligned(16))) KTab g_ktab = make_tab();

struct Params {
    const bf16_t *x, *logA, *Aim, *Cre, *Cim, *logdt, *Dskip, *wg, *wu, *wd;
    char* ws;
    bf16_t* out;
};

__device__ __forceinline__ unsigned conv_wg(const Params& P, int b, int cb, lptr sm PROF_ARG) {
    const int tid = threadIdx.x, lane = tid & 63, w = uni(tid >> 6);
    char* ws = P.ws;
    const int d0 = 4 * ((b & 7) * 20 + (b >> 3));
    const unsigned sm0 = (unsigned)(size_t)sm;
    unsigned* fl = (unsigned*)(ws + OFF_FL);
    KgenIn k1, k2;
    u32x4 tb[4];
    u32x2 xg[8];
    kgen_load(k1, d0 + w / 3, w % 3, lane, P.logA, P.Aim, P.Cre, P.Cim, P.logdt);
    const unsigned tag = epoch_tag(ws);
    if (w < 4) {
        const int t2 = 8 + w;
        kgen_load(k2, d0 + t2 / 3, t2 % 3, lane, P.logA, P.Aim, P.Cre, P.Cim, P.logdt);
#pragma unroll
        for (int i = 0; i < 4; ++i) tb[i] = *(const u32x4*)(&g_ktab.v[8 * (lane + 64 * i)]);
    }
    const unsigned dsk_raw = ((const unsigned*)P.Dskip)[(d0 + (w & 3)) >> 1];
    asm volatile("" : "+v"(k1.ldt), "+v"(k1.la), "+v"(k1.ai), "+v"(k1.cr[0]), "+v"(k1.cr[1]), "+v"(k1.cr[2]), "+v"(k1.ci[0]), "+v"(k1.ci[1]), "+v"(k1.ci[2]) :: "memory");
    STAMP(1);
    const lptr tab = sm + w * KG_WAVE;
    LAS unsigned* cnt = (LAS unsigned*)(sm + CV_M + 16);
    if (w == 7 && lane == 0) *cnt = 0;
    bar();
    if (w >= 4) {
#pragma unroll
        for (int i = 0; i < 8; ++i) xg[i] = *(const u32x2*)(P.x + (size_t)(8 * (tid - 256) + i) * D + d0);
        asm volatile("" ::: "memory");
    }
    f32x4 a1[3], a2[3];
    {
        int third = w % 3, chn = w / 3;
#pragma unroll 1
        for (int rnd = 0; rnd < 2; ++rnd) {
            float un;
            kgen_task(k1, third, lane, tab, a2, un);
            if (third == 0 && lane == 0) *(LAS float*)(sm + CV_M + chn * 4) = un;
            if (rnd == 1 || w >= 4) break;
#pragma unroll
            for (int mt = 0; mt < 3; ++mt) a1[mt] = a2[mt];
            k1 = k2; third = (8 + w) % 3; chn = (8 + w) / 3;
        }
    }
    if (w < 4) {
#pragma unroll
        for (int mt = 0; mt < 3; ++mt) { *(LAS f32x4*)(tab + (mt * 64 + lane) * 16) = a1[mt]; *(LAS f32x4*)(tab + 3072 + (mt * 64 + lane) * 16) = a2[mt]; }
        asm volatile("s_waitcnt lgkmcnt(0)" ::: "memory");
        if (lane == 0) __hip_atomic_fetch_add(cnt, 1u, __ATOMIC_RELAXED, __HIP_MEMORY_SCOPE_WORKGROUP);
        while (__hip_atomic_load(cnt, __ATOMIC_RELAXED, __HIP_MEMORY_SCOPE_WORKGROUP) < 8u) __builtin_amdgcn_s_sleep(0);
        asm volatile("" ::: "memory");
        STAMP(2);
        f32x4 ks[3];
        lptr pt[3];
#pragma unroll
        for (int j = 0; j < 3; ++j) { const int t = 3 * w + j; pt[j] = sm + (t & 7) * KG_WAVE + (t >> 3) * 3072 + lane * 16; }
        const float un = *(LAS float*)(sm + CV_M + w * 4);
#pragma unroll
        for (int mt = 0; mt < 3; ++mt) ks[mt] = ((*(LAS f32x4*)(pt[0] + mt * 1024) + *(LAS f32x4*)(pt[1] + mt * 1024)) + *(LAS f32x4*)(pt[2] + mt * 1024)) * un;
        lptr kc = sm + cv_kc(0) + w * KG_WAVE;
        const int hq = lane >> 4, col = lane & 15;
        const u32x2 p0 = {pk_bf16(ks[0][0], ks[0][1]), pk_bf16(ks[0][2], ks[0][3])}, p1 = {pk_bf16(ks[1][0], ks[1][1]), pk_bf16(ks[1][2], ks[1][3])};
        const u32x2 p2 = {pk_bf16(ks[2][0], ks[2][1]), pk_bf16(ks[2][2], ks[2][3])};
        *(LAS u32x2*)(kc + 2 * (128 * (hq >> 1) + 4 * (hq & 1) + 8 * col)) = p0;
        if (hq < 2) *(LAS u32x2*)(kc + 2 * (256 + 64 * hq + 4 * col)) = p1;
        else { *(LAS unsigned*)(kc + 2 * (384 + 64 * (hq - 2) + 2 * col)) = p1[0]; *(LAS unsigned*)(kc + 2 * (416 + 64 * (hq - 2) + 2 * col)) = p1[1]; }
        if (hq < 2) {
            lptr k2p = kc + 2 * (512 + 64 * hq + col);
            *(LAS unsigned short*)k2p = (unsigned short)p2[0]; *(LAS unsigned short*)(k2p + 32) = (unsigned short)(p2[0] >> 16);
            *(LAS unsigned short*)(k2p + 64) = (unsigned short)p2[1]; *(LAS unsigned short*)(k2p + 96) = (unsigned short)(p2[1] >> 16);
        } else if (lane == 32) *(LAS unsigned short*)(kc + 2 * 640) = (unsigned short)p2[0];
        else if (lane >= 48 && lane < 55) *(LAS unsigned short*)(kc + 2 * (641 + lane - 48)) = 0;
        asm volatile("" ::: "memory");
        STAMP(13);
        u32x4 o[4];
#pragma unroll
        for (int i = 0; i < 4; ++i)
#pragma unroll
            for (int k = 0; k < 4; ++k) {
                const unsigned tk = tb[i][k];
                const unsigned lo = *(LAS unsigned short*)(kc + (tk & 0xffffu)), hi = *(LAS unsigned short*)(kc + (tk >> 16));
                o[i][k] = lo | (hi << 16);
            }
        lptr dst = sm + cv_k(0) + w * KG_WAVE;
#pragma unroll
        for (int i = 0; i < 4; ++i) {
            *(LAS u32x4*)(dst + (lane + 64 * i) * 16) = o[i];
            unsigned nx = (unsigned)__builtin_amdgcn_update_dpp(0, (int)o[i][0], 0x130, 0xF, 0xF, false);
            const unsigned wrap = i < 3 ? (unsigned)__builtin_amdgcn_readlane((int)o[i < 3 ? i + 1 : 3][0], 0) : 0u;
            nx = lane == 63 ? wrap : nx;
            const u32x4 od = {__builtin_amdgcn_alignbit(o[i][1], o[i][0], 16), __builtin_amdgcn_alignbit(o[i][2], o[i][1], 16),
                              __builtin_amdgcn_alignbit(o[i][3], o[i][2], 16), __builtin_amdgcn_alignbit(nx, o[i][3], 16)};
            *(LAS u32x4*)(dst + 4160 + (lane + 64 * i) * 16) = od;
        }
        if (lane < 8) *(LAS u32x4*)(dst + (lane >> 2) * 4160 + 4096 + (lane & 3) * 16) = (u32x4){0u, 0u, 0u, 0u};
        STAMP(14);
    } else {
#pragma unroll
        for (int mt = 0; mt < 3; ++mt) *(LAS f32x4*)(tab + (mt * 64 + lane) * 16) = a2[mt];
        asm volatile("s_waitcnt lgkmcnt(0)" ::: "memory");
        if (lane == 0) __hip_atomic_fetch_add(cnt, 1u, __ATOMIC_RELAXED, __HIP_MEMORY_SCOPE_WORKGROUP);
        const int gm = tid - 256, ln = gm >> 1, sw = ((ln >> 1) & 3) << 1;
        u32x4 xc[4];
#pragma unroll
        for (int ch = 0; ch < 4; ++ch) {
#pragma unroll
            for (int k = 0; k < 4; ++k) xc[ch][k] = __builtin_amdgcn_perm(xg[2 * k + 1][ch >> 1], xg[2 * k][ch >> 1], (ch & 1) ? 0x07060302u : 0x05040100u);
            *(LAS u32x4*)(sm + CV_X + ln * 128 + 16 * ((2 * ch + (gm & 1)) ^ sw)) = xc[ch];
        }
        if (gm < 128) *(LAS u32x4*)(sm + CV_U + gm * 16) = (u32x4){0u, 0u, 0u, 0u};
        wait_flags<16, FL_LN_S>(fl + (b & 7) * FL_REP + FL_LN + 16 * (w - 4) * FL_LN_S, tag, lane);
        STAMPL(25);
        f32x4 r0, r1, n0, n1;
        asm volatile("global_load_dwordx4 %0, %4, %5 sc1\n\tglobal_load_dwordx4 %1, %4, %5 offset:16 sc1\n\tglobal_load_dwordx4 %2, %4, %6 sc1\n\tglobal_load_dwordx4 %3, %4, %6 offset:16 sc1\n\ts_waitcnt vmcnt(0)"
                     : "=&v"(r0), "=&v"(r1), "=&v"(n0), "=&v"(n1) : "v"(gm * 32), "s"(ws + OFF_S1), "s"(ws + OFF_S1 + T * 4) : "memory");
#pragma unroll
        for (int ch = 0; ch < 4; ++ch) {
            u32x4 u;
#pragma unroll
            for (int k = 0; k < 4; ++k) {
                const f32x2 rs = k < 2 ? (f32x2){r0[2 * k], r0[2 * k + 1]} : (f32x2){r1[2 * k - 4], r1[2 * k - 3]}, nm = k < 2 ? (f32x2){n0[2 * k], n0[2 * k + 1]} : (f32x2){n1[2 * k - 4], n1[2 * k - 3]};
                const f32x2 a = __builtin_elementwise_fma((f32x2){bflo(xc[ch][k]), bfhi(xc[ch][k])}, rs, nm);
                u[k] = pk_bf16(a[0], a[1]);
            }
            *(LAS u32x4*)(sm + CV_U + 2048 + ln * 128 + 16 * ((2 * ch + (gm & 1)) ^ sw)) = u;
        }
    }
    bar();
    STAMP(3); CSTAMP(16);
    if (w < 4) {
        const int n = lane & 15, h = lane >> 4;
        const lptr fb = sm + cv_k(0) + w * KG_WAVE + ((n & 1) ? 0 : 4160) + 4 * (((2031 - n) >> 1) + 4 * h) - 512 * 7;
        f32x4 acc[8] = {};
        bf16x8 Fa[2][8], Ga[2][8];
        lptr gp, fp[4];
        auto addr = [&](int rr) {
            const int ln = n + (h >> 1) + 15 - 2 * rr;
            gp = sm + CV_U + 128 * ln + 16 * ((2 * w + (h & 1)) ^ (((ln >> 1) & 3) << 1));
#pragma unroll
            for (int k = 0; k < 4; ++k) { fp[k] = fb - 64 * rr + 1024 * k; asm volatile("" : "+v"(fp[k])); }
        };
        auto ldG = [&](int Q, int c) { Ga[Q][c] = *(LAS bf16x8*)(gp + 2048 * c); };
        u32x4 ftmp[2];
        auto ldF = [&](int Q, int a, int half) {
            lptr q = fp[(7 - a) >> 1] + 512 * ((7 - a) & 1) + 8 * half;
            ftmp[a & 1][2 * half] = *(LAS unsigned*)q; ftmp[a & 1][2 * half + 1] = *(LAS unsigned*)(q + 4);
            if (half) Fa[Q][a] = __builtin_bit_cast(bf16x8, ftmp[a & 1]);
        };
        addr(0);
#pragma unroll
        for (int c = 0; c < 8; ++c) { ldF(0, c, 0); ldF(0, c, 1); ldG(0, c); }
        SB();
#pragma unroll
        for (int rr = 0; rr < 8; ++rr) {
            const int Q = rr & 1;
            int i = 0;
#pragma unroll
            for (int a = 0; a < 8; ++a)
#pragma unroll
                for (int c = a; c < 8; ++c) {
                    acc[c] = __builtin_amdgcn_mfma_f32_16x16x32_bf16(Fa[Q][a], Ga[Q][c - a], acc[c], 0, 0, 0); PIN(acc[c]); SB();
                    if (rr < 7) {
                        if (i == 0) { addr(rr + 1); SB(); }
                        else if (i <= 24) { const int k = (i - 1) / 3, m = (i - 1) % 3; if (m == 0) ldG(Q ^ 1, k); else ldF(Q ^ 1, k, m - 1); SB(); }
                    }
                    ++i;
                }
        }
        STAMP(4); CSTAMP(17);
        const int d = d0 + w;
        lptr yb = sm + cv_k(0) + w * KG_WAVE;
        const float dsk = (d & 1) ? bfhi(dsk_raw) : bflo(dsk_raw);
        const f32x2 dk2 = {dsk, dsk};
        const int oc = h >> 1;
        const int so = n * 128 + 16 * ((2 * w + oc) ^ (((n >> 1) & 3) << 1)) + 8 * (h & 1);
        u32x2 uv[8], xv[8];
#pragma unroll
        for (int c = 0; c < 8; ++c) { uv[c] = *(LAS u32x2*)(sm + CV_U + 2048 + so + 2048 * c); xv[c] = *(LAS u32x2*)(sm + CV_X + so + 2048 * c); }
#pragma unroll
        for (int c = 0; c < 8; ++c) {
            u32x2 o;
#pragma unroll
            for (int i = 0; i < 2; ++i) {
                const f32x2 uf = {bflo(uv[c][i]), bfhi(uv[c][i])}, xf = {bflo(xv[c][i]), bfhi(xv[c][i])};
                const f32x2 hf = (uf * dk2 + (f32x2){acc[c][2 * i], acc[c][2 * i + 1]}) + xf;
                o[i] = pk_bf16(hf[0], hf[1]);
            }
            *(LAS u32x2*)(yb + (256 * c + 16 * n + 4 * h) * 2) = o;
        }
        asm volatile("" ::: "memory");
#pragma unroll
        for (int j = 0; j < 4; ++j) st_dev(ws + OFF_HT + (size_t)d * (T * 2) + (64 * j + lane) * 16, *(LAS u32x4*)(yb + (64 * j + lane) * 16));
        STAMP(27);
        wait_vm<0>();
    }
    bar();
    if (tid < 8) set_flag_rep(fl + FL_H + b * FL_H_S, tag, tid);
    STAMP(5);
    if (w == 6) g1_prefetch_w(ws, sm, cb, lane, tag);
    return tag;
}

__device__ __forceinline__ unsigned spare_wg(const Params& P, int j, int cb, lptr sm PROF_ARG) {
    const int tid = threadIdx.x, lane = tid & 63, w = uni(tid >> 6);
    unsigned* fl = (unsigned*)(P.ws + OFF_FL);
    unsigned tag;
    if (j < 64) tag = ln1_unit(j, P.x, P.ws PROF_FWD); else tag = epoch_tag(P.ws);
    if (j < 64) {
        wait_vm<0>();
        bar();
        if (tid < 8) set_flag_rep(fl + FL_LN + j * FL_LN_S, tag, tid);
    }
    STAMP(1);
    const int n_g = j >= 64 ? 2 : (j < 32 ? 1 : 0), g0 = j >= 64 ? 2 * (j - 64) : 64 + j;
    for (int i = 0; i < n_g; ++i) {
        wgtile_unit(g0 + i, P.wg, P.wu, P.ws, sm);
        wait_vm<0>();
        bar();
        if (tid < 8) set_flag_rep(fl + FL_WG + (g0 + i) * FL_W_S, tag, tid);
    }
    STAMP(2);
    if (j >= 64 || (j >= 32 && j < 48)) {
        const int fb = j >= 64 ? j - 64 : j;
        wdtile_unit(fb, P.wd, P.ws, sm);
        wait_vm<0>();
        bar();
        if (tid < 8) set_flag_rep(fl + FL_WD + fb * FL_W_S, tag, tid);
    }
    if (w == 6) g1_prefetch_w(P.ws, sm, cb, lane, tag);
    STAMP(5);
    return tag;
}

template <int WC>
struct G1Compute {
    f32x16 acc[3][2];
    bf16x8 Af[2][3], Bf[2][2];
    template <int P> __device__ __forceinline__ void mm(int ft, int tt) {
        acc[ft][tt] = __builtin_amdgcn_mfma_f32_32x32x16_bf16(Af[P][ft], Bf[P][tt], acc[ft][tt], 0, 0, 0); PIN(acc[ft][tt]); SB();
    }
    __device__ __forceinline__ static bf16x8 rdB(lptr p) { return __builtin_bit_cast(bf16x8, tr_read2(p, 8 * 256)); }
    template <int P, int KK, bool MORE, bool BAR>
    __device__ __forceinline__ void substep(lptr pa, const lptr (&pb)[2]) {
        mm<P>(0, 0);
        if (BAR) { sbar(); SB(); }
        if (MORE) { Bf[P ^ 1][0] = rdB(pb[0] + KK * 16 * 256); Af[P ^ 1][0] = *(LAS bf16x8*)(pa + KK * 6144); SB(); }
        mm<P>(0, 1);
        if (MORE) { Bf[P ^ 1][1] = rdB(pb[1] + KK * 16 * 256); Af[P ^ 1][1] = *(LAS bf16x8*)(pa + KK * 6144 + 1024); SB(); }
        mm<P>(1, 0);
        if (MORE) { Af[P ^ 1][2] = *(LAS bf16x8*)(pa + KK * 6144 + 2048); SB(); }
        mm<P>(1, 1);
        mm<P>(2, 0);
        mm<P>(2, 1);
    }
};

template <int WC>
__device__ __forceinline__ void g1_compute(lptr sm, char* __restrict__ ws, int wr, int lane, int t0, int cb PROF_ARG) {
    const int g = lane >> 4, q = (lane & 15) >> 2, p = lane & 3, hh = g >> 1, krow = 4 * hh + q;
    G1Compute<WC> C;
#pragma unroll
    for (int ft = 0; ft < 3; ++ft)
#pragma unroll
        for (int tt = 0; tt < 2; ++tt) C.acc[ft][tt] = (f32x16){};
    const lptr a0 = sm + G1_W + 3 * WC * 1024 + lane * 16;
    lptr b0[2];
#pragma unroll
    for (int tt = 0; tt < 2; ++tt) b0[tt] = sm + krow * 256 + (((2 * wr + tt) ^ q) * 64) + (g & 1) * 32 + p * 8;
    sbar();
    STAMP(7); CSTAMP(18);
#pragma unroll
    for (int tt = 0; tt < 2; ++tt) C.Bf[0][tt] = C.rdB(b0[tt]);
#pragma unroll
    for (int ft = 0; ft < 3; ++ft) C.Af[0][ft] = *(LAS bf16x8*)(a0 + ft * 1024);
    SB();
    lptr pa = a0, pb[2] = {b0[0], b0[1]};
    int nb = 0;
#pragma unroll 1
    for (int s = 0; s < 19; ++s) {
        C.template substep<0, 1, true, false>(pa, pb);
        nb = nb + G1_SS == G1_NS * G1_SS ? 0 : nb + G1_SS;
        pa = a0 + nb; pb[0] = b0[0] + nb; pb[1] = b0[1] + nb;
        SB();
        C.template substep<1, 0, true, true>(pa, pb);
    }
    C.template substep<0, 1, true, false>(pa, pb);
    C.template substep<1, 0, false, false>(pa, pb);
    STAMP(8); CSTAMP(19);
    bar();
    char* mF = ws + OFF_MF;
    f32x2 nm2[2], k1[2], k2[2], s2[2];
#pragma unroll
    for (int tt = 0; tt < 2; ++tt) {
        const f32x2 sq = *(LAS f32x2*)(sm + G1_ST + (32 * (2 * wr + tt) + (lane & 31)) * 8);
        const float rs = __builtin_amdgcn_rcpf(sq[1]);
        nm2[tt] = (f32x2){-sq[0], -sq[0]};
        k1[tt] = (f32x2){-2.302208198f * rs, -2.302208198f * rs};
        k2[tt] = (f32x2){-0.1029432397f * rs * rs * rs, -0.1029432397f * rs * rs * rs};
        s2[tt] = (f32x2){sq[1] * sq[1], sq[1] * sq[1]};
    }
#pragma unroll
    for (int ft = 0; ft < 3; ++ft) {
        const int fl = 16 * (3 * WC + ft) + 4 * hh;
        lptr cp = sm + G1_CS + 3072 + fl * 4;
        const f32x4 cg0 = *(LAS f32x4*)cp, cg1 = *(LAS f32x4*)(cp + 32), cu0 = *(LAS f32x4*)(cp + 384), cu1 = *(LAS f32x4*)(cp + 416);
#pragma unroll
        for (int tt = 0; tt < 2; ++tt) {
            const int tg = t0 + 32 * (2 * wr + tt) + (lane & 31);
            u32x4 o;
#pragma unroll
            for (int r = 0; r < 4; ++r) {
                const f32x2 cg = r < 2 ? (f32x2){cg0[2 * r], cg0[2 * r + 1]} : (f32x2){cg1[2 * r - 4], cg1[2 * r - 3]};
                const f32x2 cu = r < 2 ? (f32x2){cu0[2 * r], cu0[2 * r + 1]} : (f32x2){cu1[2 * r - 4], cu1[2 * r - 3]};
                const f32x2 ag = {C.acc[ft][tt][2 * r], C.acc[ft][tt][2 * r + 1]}, au = {C.acc[ft][tt][8 + 2 * r], C.acc[ft][tt][9 + 2 * r]};
                const f32x2 G = __builtin_elementwise_fma(nm2[tt], cg, ag), U = __builtin_elementwise_fma(nm2[tt], cu, au);
                const f32x2 ea = G * __builtin_elementwise_fma(G * G, k2[tt], k1[tt]);
                const f32x2 den = __builtin_elementwise_fma((f32x2){__builtin_amdgcn_exp2f(ea[0]), __builtin_amdgcn_exp2f(ea[1])}, s2[tt], s2[tt]);
                const f32x2 mv = (G * U) * (f32x2){__builtin_amdgcn_rcpf(den[0]), __builtin_amdgcn_rcpf(den[1])};
                o[r] = pk_bf16(mv[0], mv[1]);
            }
            const int ftg = 6 * cb + 3 * WC + ft;
            const int lp = (tg & 15) + 16 * (2 * (ftg & 1) + hh);
            st_dev(mF + (((size_t)(tg >> 4) * 48 + (ftg >> 1)) * 64 + lp) * 16, o);
        }
    }
}

__device__ __forceinline__ void g1_loader(char* __restrict__ ws, lptr sm, int lw, int lane, int t0, int cb, unsigned tag) {
    const unsigned sm0 = (unsigned)(size_t)sm;
    {
        const char* hT = ws + OFF_HT;
        const char* wsrc = ws + OFF_WG + (size_t)cb * 40 * 6144 + 3 * lw * 1024;
        unsigned vh[2];
#pragma unroll
        for (int j = 0; j < 2; ++j) {
            const int o = (2 * lw + j) * 1024 + lane * 16, k = o >> 8, c = ((o >> 4) & 15) ^ ((k & 3) << 2);
            vh[j] = (unsigned)(k * (T * 2) + t0 * 2 + c * 16);
        }
        const unsigned vl = lane * 16;
        unsigned long long* st = (unsigned long long*)(ws + OFF_ST);
        auto stage = [&](int s) {
            const unsigned lb = sm0 + (s % G1_NS) * G1_SS;
            const char* hs = hT + (size_t)s * 32 * (T * 2);
            const char* wsx = wsrc + (size_t)s * 12288;
            dma16s(hs, vh[0], lb + (2 * lw) * 1024);
            if (s >= 4) dma16s(wsx, vl, lb + G1_W + (3 * lw) * 1024);
            dma16s(hs, vh[1], lb + (2 * lw + 1) * 1024);
            if (s >= 4) { dma16s(wsx + 1024, vl, lb + G1_W + (3 * lw + 1) * 1024); dma16s(wsx + 2048, vl, lb + G1_W + (3 * lw + 2) * 1024); }
        };
        u32x4 gv[3];
        unsigned long long g0, g1;
        float cs[8], sv[16];
#pragma unroll
        for (int s = 0; s < 4; ++s) stage(s);
        {
            const char* hp = hT + (size_t)(160 * lw) * (T * 2) + (t0 + 8 * cb) * 2;
            const unsigned go = (unsigned)lane * (T * 2);
            asm volatile("global_load_dwordx4 %0, %1, %2 sc1" : "=v"(gv[0]) : "v"(go), "s"(hp) : "memory");
            asm volatile("global_load_dwordx4 %0, %1, %2 sc1" : "=v"(gv[1]) : "v"(go), "s"(hp + (size_t)64 * (T * 2)) : "memory");
            asm volatile("global_load_dwordx4 %0, %1, %2 sc1" : "=v"(gv[2]) : "v"(lane < 32 ? go : 0u), "s"(hp + (size_t)128 * (T * 2)) : "memory");
        }
        wait_vm<9>();
        sbar();
#pragma unroll
        for (int s = 0; s < 19; ++s) {
            const int last = (s + 3) < 19 ? (s + 3) : 19;
            if (s == 3) asm volatile("s_waitcnt vmcnt(10)" : "+v"(gv[0]), "+v"(gv[1]), "+v"(gv[2]) :: "memory");
            else if (s == 17) asm volatile("s_waitcnt vmcnt(5)" : "+v"(g0), "+v"(g1) :: "memory");
            else wait_vm_rt<30>(s == 0 ? 7 : (s == 1 ? 10 : (s == 2 ? 13 : 5 * (last - (s + 1)) + (s == 15 || s == 16 ? 2 : 0))));
            sbar();
            if (s + 4 < 20) stage(s + 4);
            if (s == 14) {
                asm volatile("global_load_dwordx2 %0, %1, %2 sc1" : "=v"(g0) : "v"(lane * 8), "s"(st + t0) : "memory");
                asm volatile("global_load_dwordx2 %0, %1, %2 sc1" : "=v"(g1) : "v"(lane * 8), "s"(st + t0 + 64) : "memory");
            }
            if (s >= 3 && s <= 6) {
#pragma unroll
                for (int e = 2 * (s - 3); e < 2 * (s - 3) + 2; ++e) {
                    const unsigned c32 = (unsigned)__builtin_amdgcn_readlane((int)gv[0][e >> 1], 0);
                    cs[e] = (e & 1) ? bfhi(c32) : bflo(c32);
                    const float d0 = ((e & 1) ? bfhi(gv[0][e >> 1]) : bflo(gv[0][e >> 1])) - cs[e], d1 = ((e & 1) ? bfhi(gv[1][e >> 1]) : bflo(gv[1][e >> 1])) - cs[e];
                    const float d2 = lane < 32 ? ((e & 1) ? bfhi(gv[2][e >> 1]) : bflo(gv[2][e >> 1])) - cs[e] : 0.f;
                    sv[e] = d0 + d1 + d2; sv[8 + e] = d0 * d0 + d1 * d1 + d2 * d2;
                    PIN(sv[e]); PIN(sv[8 + e]);
                }
            }
            if (s == 7) {
#pragma unroll
                for (int j = 0; j < 8; ++j) {
                    const auto r = __builtin_amdgcn_permlane32_swap(__float_as_uint(sv[j]), __float_as_uint(sv[j + 8]), false, false);
                    sv[j] = __uint_as_float(r[0]) + __uint_as_float(r[1]); PIN(sv[j]);
                }
            }
            if (s == 8) {
#pragma unroll
                for (int k = 0; k < 4; ++k) {
                    const auto r = __builtin_amdgcn_permlane16_swap(__float_as_uint(sv[k]), __float_as_uint(sv[k + 4]), false, false);
                    sv[k] = __uint_as_float(r[0]) + __uint_as_float(r[1]);
                }
#pragma unroll
                for (int k = 0; k < 4; ++k) { sv[k] = row16_sum(sv[k]); PIN(sv[k]); }
            }
            if (s == 9) {
                if ((lane & 15) == 0) *(LAS f32x4*)(sm + G1_SP + lw * 96 + (lane >> 4) * 16) = (f32x4){sv[0], sv[1], sv[2], sv[3]};
                if (lane == 0) { *(LAS f32x4*)(sm + G1_SP + lw * 96 + 64) = (f32x4){cs[0], cs[1], cs[2], cs[3]}; *(LAS f32x4*)(sm + G1_SP + lw * 96 + 80) = (f32x4){cs[4], cs[5], cs[6], cs[7]}; }
                asm volatile("s_waitcnt lgkmcnt(0)" ::: "memory");
            }
            if (s == 10 && lw == 3 && lane < 8) {
                float pm[4], m2 = 0.f, mean = 0.f;
#pragma unroll
                for (int i = 0; i < 4; ++i) {
                    const float a = *(LAS float*)(sm + G1_SP + i * 96 + lane * 4) * (1.0f / 160.0f), q = *(LAS float*)(sm + G1_SP + i * 96 + 32 + lane * 4);
                    pm[i] = *(LAS float*)(sm + G1_SP + i * 96 + 64 + lane * 4) + a;
                    m2 += q - 160.0f * a * a; mean += 0.25f * pm[i];
                }
#pragma unroll
                for (int i = 0; i < 4; ++i) m2 += 160.0f * (pm[i] - mean) * (pm[i] - mean);
                const float var = fmaxf(m2 * (1.0f / (float)D), 0.f);
                const unsigned mb = pk_bf16(mean, rbf(sqrtf(rbf(rbf(var) + 1e-5f))));
                __hip_atomic_store(st + t0 + 8 * cb + lane, ((unsigned long long)tag << 32) | mb, __ATOMIC_RELAXED, __HIP_MEMORY_SCOPE_AGENT);
            }
        }
        if (lw == 0) {
            for (unsigned spins = 0; !__all((unsigned)(g0 >> 32) == tag && (unsigned)(g1 >> 32) == tag) && spins < (1u << 18); ++spins) {
                __builtin_amdgcn_s_sleep(2);
                g0 = __hip_atomic_load(st + t0 + lane, __ATOMIC_RELAXED, __HIP_MEMORY_SCOPE_AGENT);
                g1 = __hip_atomic_load(st + t0 + 64 + lane, __ATOMIC_RELAXED, __HIP_MEMORY_SCOPE_AGENT);
            }
            *(LAS f32x2*)(sm + G1_ST + lane * 8) = (f32x2){bflo((unsigned)g0), bfhi((unsigned)g0)};
            *(LAS f32x2*)(sm + G1_ST + (64 + lane) * 8) = (f32x2){bflo((unsigned)g1), bfhi((unsigned)g1)};
        }
        bar();
    }
}

template <int ND>
__device__ __forceinline__ void g2_compute(lptr sm, int lane, int th, int dl0, int t0, int d0, bf16_t* __restrict__ out PROF_ARG) {
    f32x4 acc[ND][2] = {};
    bf16x8 A[2][ND], B[2][2];
    const lptr pb0 = sm + lane * 16 + 2 * th * 4096, pa0 = sm + lane * 16 + G2_W + dl0 * 1024;
    auto ld = [&](int P, lptr pa, lptr pb, int fl) {
#pragma unroll
        for (int tt = 0; tt < 2; ++tt) B[P][tt] = *(LAS bf16x8*)(pb + (tt * 4 + fl) * 1024);
#pragma unroll
        for (int dt = 0; dt < ND; ++dt) A[P][dt] = *(LAS bf16x8*)(pa + (fl * 5 + dt) * 1024);
        SB();
    };
    auto mm = [&](int P) {
#pragma unroll
        for (int dt = 0; dt < ND; ++dt)
#pragma unroll
            for (int tt = 0; tt < 2; ++tt) { acc[dt][tt] = __builtin_amdgcn_mfma_f32_16x16x32_bf16(A[P][dt], B[P][tt], acc[dt][tt], 0, 0, 0); PIN(acc[dt][tt]); SB(); }
    };
    sbar();
    STAMP(11); CSTAMP(20);
    ld(0, pa0, pb0, 0);
    lptr pa = pa0, pb = pb0;
    int nb = 0;
#pragma unroll 1
    for (int s = 0; s < 12; ++s) {
        ld(1, pa, pb, 1); mm(0);
        ld(0, pa, pb, 2); mm(1);
        ld(1, pa, pb, 3); mm(0);
        if (s < 11) {
            nb = nb + G2_SS == G2_NS * G2_SS ? 0 : nb + G2_SS;
            pa = pa0 + nb; pb = pb0 + nb;
            bar();
            ld(0, pa, pb, 0);
        }
        mm(1);
    }
    STAMP(12); CSTAMP(21);
    bar();
    const int g = lane >> 4, q = (lane & 15) >> 2, p = lane & 3;
#pragma unroll
    for (int dt = 0; dt < ND; ++dt)
#pragma unroll
        for (int tt = 0; tt < 2; ++tt) {
            const int row = 16 * (dl0 + dt) + 4 * g + q, seg = (2 * th + tt) ^ ((row >> 1) & 3);
            const u32x2 hv = tr_read(sm + G2_H + row * 128 + seg * 32 + p * 8);
            const unsigned o01 = pk_bf16(acc[dt][tt][0], acc[dt][tt][1]), o23 = pk_bf16(acc[dt][tt][2], acc[dt][tt][3]);
            const u32x2 r = {pk_bf16(bflo(hv[0]) + bflo(o01), bfhi(hv[0]) + bfhi(o01)), pk_bf16(bflo(hv[1]) + bflo(o23), bfhi(hv[1]) + bfhi(o23))};
            *(LAS u32x2*)(sm + (16 * (2 * th + tt) + (lane & 15)) * 176 + (16 * (dl0 + dt) + 4 * g) * 2) = r;
        }
}

extern "C" __global__ void s4d_geglu_block_end();
extern "C" __global__ __launch_bounds__(512, 2) void s4d_geglu_block(Params P PROF_ARG) {
    extern __shared__ __attribute__((aligned(16))) char smem[];
    lptr sm = (lptr)smem;
    STAMP(0);
    if (uni(threadIdx.x >> 6) == 7) {
        unsigned long long pc; unsigned off, len;
        asm volatile("s_getpc_b64 %0\n.Lpc_anchor_%=:\n\ts_mov_b32 %1, .Lpc_anchor_%=-s4d_geglu_block\n\ts_mov_b32 %2, s4d_geglu_block_end-s4d_geglu_block" : "=s"(pc), "=s"(off), "=s"(len));
        const unsigned o = (blockIdx.x >> 3) * 2048 + (threadIdx.x & 63) * 32;
        if (o < len) dma4((const void*)(pc - off), o, (unsigned)(size_t)sm + LDS_BYTES - 256);
    }
    const int tid = threadIdx.x, lane = tid & 63, w = uni(tid >> 6), b = blockIdx.x;
    const unsigned sm0 = (unsigned)(size_t)sm;
    char* ws = P.ws;
    unsigned* fl = (unsigned*)(ws + OFF_FL);
    unsigned tag;
    const int grp = b & 7, idx = b >> 3, grp0 = grp;
    const int rb = (grp & 1) * 8 + (idx & 7), cb = (grp >> 1) * 4 + (idx >> 3);
    const int tb = (grp >> 1) * 8 + (idx & 7), db = (grp & 1) * 4 + (idx >> 3);

    if (b < 160) tag = conv_wg(P, b, cb, sm PROF_FWD); else tag = spare_wg(P, b - 160, cb, sm PROF_FWD);

    if (w == 4) wait_flags<160, FL_H_S>(fl + grp0 * FL_REP + FL_H, tag, lane);
    if (w == 7) wait_flags<48, FL_W_S>(fl + grp0 * FL_REP + FL_WD, tag, lane);
    bar();
    STAMP(6);
    if (w >= 4) g1_loader(ws, sm, w & 3, lane, 128 * rb, cb, tag);
    else if (w & 1) g1_compute<1>(sm, ws, w >> 1, lane, 128 * rb, cb PROF_FWD); else g1_compute<0>(sm, ws, w >> 1, lane, 128 * rb, cb PROF_FWD);

    const int t0 = 64 * tb, d0 = 80 * db;
    if (w >= 4) {
        const int lw = w & 3;
        const char* ms = ws + OFF_MF + (size_t)(4 * tb + lw) * 48 * 1024;
        const char* wsrc = ws + OFF_WD + (size_t)lw * (40 * 1024) + (size_t)(5 * db) * 1024;
        const unsigned vl = lane * 16;
        auto stage_w = [&](int s) {
#pragma unroll
            for (int j = 0; j < 5; ++j) dma16s(wsrc + (size_t)(4 * s) * (40 * 1024) + j * 1024, vl, sm0 + (s % G2_NS) * G2_SS + G2_W + (5 * lw + j) * 1024);
        };
        auto stage_m = [&](int s) {
#pragma unroll
            for (int j = 0; j < 4; ++j) dma16s(ms + (size_t)(4 * s + j) * 1024, vl, sm0 + (s % G2_NS) * G2_SS + (4 * lw + j) * 1024);
        };
        stage_w(0); stage_w(1); stage_w(2);
#pragma unroll
        for (int j = 0; j < 3; ++j) {
            const int i = lw + 4 * j < 10 ? lw + 4 * j : lw;
            const int o = i * 1024 + lane * 16, row = o >> 7, c = ((o >> 4) & 7) ^ (((row >> 1) & 3) << 1);
            dma16s(ws + OFF_HT, (unsigned)((d0 + row) * (T * 2) + t0 * 2 + c * 16), sm0 + G2_H + i * 1024);
        }
        sbar();
        if (lw == 0) wait_flags<16, FL_M_S>(fl + FL_M + 16 * (tb >> 1) * FL_M_S, tag, lane);
        sbar();
        STAMPL(10);
        stage_m(0); stage_m(1); stage_m(2);
        wait_vm<8>();
        sbar();
#pragma unroll
        for (int s = 0; s < 11; ++s) {
            if (s == 0) wait_vm<4>(); else if (s <= 9) wait_vm<9>(); else wait_vm<0>();
            sbar();
            if (s + 3 < 12) { stage_m(s + 3); stage_w(s + 3); }
        }
        sbar();
    } else {
        STAMP(28);
        wait_vm<0>();
        sbar();
        if (tid == 0) set_flag(fl + FL_M + (16 * rb + cb) * FL_M_S, tag);
        STAMP(9);
        sbar();
        const int th = w & 1;
        if (w >> 1) g2_compute<2>(sm, lane, th, 3, t0, d0, P.out PROF_FWD); else g2_compute<3>(sm, lane, th, 0, t0, d0, P.out PROF_FWD);
    }
    bar();
    for (int ch = tid; ch < 640; ch += 512) {
        const int row = ch / 10, c = ch % 10;
        *(u32x4*)(P.out + (size_t)(t0 + row) * D + d0 + c * 8) = *(LAS u32x4*)(sm + row * 176 + c * 16);
    }
    if (b == 0 && tid == 0) fl[FL_E] = tag;
    STAMP(15);
}

extern "C" __global__ void s4d_geglu_block_end() {}

extern "C" void kernel_launch(void* const* d_in, const int* in_sizes, int n_in, void* d_out, int out_size, void* d_ws, size_t ws_size, hipStream_t stream) {
    Params P;
    P.x = (const bf16_t*)d_in[0]; P.logA = (const bf16_t*)d_in[1]; P.Aim = (const bf16_t*)d_in[2]; P.Cre = (const bf16_t*)d_in[3]; P.Cim = (const bf16_t*)d_in[4];
    P.logdt = (const bf16_t*)d_in[5]; P.Dskip = (const bf16_t*)d_in[6]; P.wg = (const bf16_t*)d_in[8]; P.wu = (const bf16_t*)d_in[9]; P.wd = (const bf16_t*)d_in[10];
    P.ws = (char*)d_ws; P.out = (bf16_t*)d_out;
    (void)hipFuncSetAttribute((const void*)s4d_geglu_block, hipFuncAttributeMaxDynamicSharedMemorySize, LDS_BYTES);
    s4d_geglu_block<<<256, 512, LDS_BYTES, stream>>>(P);
}
```

```cpp
#include <hip/hip_runtime.h>
#include <stdint.h>

typedef unsigned short bf16_t;
typedef short s16x4 __attribute__((ext_vector_type(4)));
typedef __bf16 bf16x8 __attribute__((ext_vector_type(8)));
typedef __bf16 bf16x2 __attribute__((ext_vector_type(2)));
typedef _Float16 h16x8 __attribute__((ext_vector_type(8)));
typedef _Float16 h16x2 __attribute__((ext_vector_type(2)));
typedef float f32x2 __attribute__((ext_vector_type(2)));
typedef float f32x4 __attribute__((ext_vector_type(4)));
typedef float f32x16 __attribute__((ext_vector_type(16)));
typedef unsigned u32x2 __attribute__((ext_vector_type(2)));
typedef unsigned u32x4 __attribute__((ext_vector_type(4)));
#define LAS __attribute__((address_space(3)))
typedef LAS char* lptr;

constexpr int T = 2048, D = 640, NM = 192, F = 1536;
constexpr size_t OFF_S1 = 0;
constexpr size_t OFF_HT = OFF_S1 + (size_t)T * 8;
constexpr size_t OFF_CS = OFF_HT + (size_t)T * D * 2;
constexpr size_t OFF_MF = OFF_CS + 65536;
constexpr size_t OFF_WD = OFF_MF + (size_t)T * F * 2;
constexpr size_t OFF_WG = OFF_WD + (size_t)F * D * 2;
constexpr size_t OFF_ST = OFF_WG + (size_t)2 * F * D * 2;
constexpr size_t OFF_FL = OFF_ST + (size_t)T * 8;
constexpr size_t OFF_PROF = OFF_FL + 196608;
constexpr int FL_E = 0;
constexpr int FL_REP = 4096;
constexpr int FL_LN = FL_REP, FL_LN_S = 16;
constexpr int FL_WG = FL_LN + 1024, FL_WD = FL_WG + 96 * 8, FL_W_S = 8;
constexpr int FL_M = 64, FL_M_S = 8;
constexpr int FL_HC = 9 * FL_REP;
static_assert(FL_WD + 48 * FL_W_S <= 2 * FL_REP && FL_M + 256 * FL_M_S <= FL_REP, "flag map");

#define STAMP(i)
#define STAMPL(i)
#define CSTAMP(i)
#define PROF_ARG
#define PROF_PASS
#define PROF_FWD

__device__ __forceinline__ float bf2f(unsigned v) { return __uint_as_float(v << 16); }
__device__ __forceinline__ float bflo(unsigned v) { return __uint_as_float(v << 16); }
__device__ __forceinline__ float bfhi(unsigned v) { return __uint_as_float(v & 0xffff0000u); }
__device__ __forceinline__ unsigned pk_bf16(float lo, float hi) { f32x2 v = {lo, hi}; return __builtin_bit_cast(unsigned, __builtin_convertvector(v, bf16x2)); }
__device__ __forceinline__ float rbf(float f) { return bflo(pk_bf16(f, f)); }
__device__ __forceinline__ unsigned pk_f16(f32x2 v) { return __builtin_bit_cast(unsigned, __builtin_convertvector(v, h16x2)); }
template <int CTRL> __device__ __forceinline__ float dppf(float x) { return __int_as_float(__builtin_amdgcn_update_dpp(0, __float_as_int(x), CTRL, 0xF, 0xF, true)); }
__device__ __forceinline__ float row16_sum(float x) { x += dppf<0xB1>(x); x += dppf<0x4E>(x); x += dppf<0x141>(x); x += dppf<0x140>(x); return x; }
__device__ __forceinline__ float row16_max(float x) { x = fmaxf(x, dppf<0xB1>(x)); x = fmaxf(x, dppf<0x4E>(x)); x = fmaxf(x, dppf<0x141>(x)); x = fmaxf(x, dppf<0x140>(x)); return x; }
__device__ __forceinline__ float xsum16_32(float x) {
    const unsigned u = __float_as_uint(x);
    const auto r = __builtin_amdgcn_permlane16_swap(u, u, false, false);
    const unsigned v = __float_as_uint(__uint_as_float(r[0]) + __uint_as_float(r[1]));
    const auto q = __builtin_amdgcn_permlane32_swap(v, v, false, false);
    return __uint_as_float(q[0]) + __uint_as_float(q[1]);
}
__device__ __forceinline__ u32x2 tr_read(lptr p) { return __builtin_bit_cast(u32x2, __builtin_amdgcn_ds_read_tr16_b64_v4i16((LAS s16x4*)p)); }
__device__ __forceinline__ u32x4 tr_read2(lptr p, int off2) { const u32x2 r1 = tr_read(p), r2 = tr_read(p + off2); return (u32x4){r1[0], r1[1], r2[0], r2[1]}; }
__device__ __forceinline__ int uni(int v) { return __builtin_amdgcn_readfirstlane(v); }

__device__ __forceinline__ void dma16(const void* sbase, unsigned voff, unsigned ldsaddr) {
    asm volatile("s_mov_b32 m0, %2\n\ts_nop 0\n\tglobal_load_lds_dwordx4 %0, %1" :: "v"(voff), "s"(sbase), "s"(ldsaddr) : "memory");
}
__device__ __forceinline__ void dma4(const void* sbase, unsigned voff, unsigned ldsaddr) {
    asm volatile("s_mov_b32 m0, %2\n\ts_nop 0\n\tglobal_load_lds_dword %0, %1" :: "v"(voff), "s"(sbase), "s"(ldsaddr) : "memory");
}
template <int N> __device__ __forceinline__ void wait_vm() { asm volatile("s_waitcnt vmcnt(%0)" :: "n"(N) : "memory"); }
template <int N> __device__ __forceinline__ void wait_vm_rt(int n) {
    if (n >= N) wait_vm<N>(); else if constexpr (N > 0) wait_vm_rt<N - 1>(n);
}
__device__ __forceinline__ void bar() { asm volatile("s_waitcnt lgkmcnt(0)\n\ts_barrier" ::: "memory"); }
__device__ __forceinline__ void sbar() { asm volatile("s_barrier" ::: "memory"); }
#define SB() __builtin_amdgcn_sched_barrier(0)
#define PIN(x) asm volatile("" : "+v"(x))
__device__ __forceinline__ void st_dev(void* p, u32x4 v) { asm volatile("global_store_dwordx4 %0, %1, off sc1\n\ts_nop 1" :: "v"(p), "v"(v) : "memory"); }
__device__ __forceinline__ void st_dev_o(const void* sbase, unsigned voff, u32x4 v) { asm volatile("global_store_dwordx4 %0, %1, %2 sc1\n\ts_nop 1" :: "v"(voff), "v"(v), "s"(sbase) : "memory"); }
__device__ __forceinline__ void st_dev4(void* p, float v) { asm volatile("global_store_dword %0, %1, off sc1\n\ts_nop 1" :: "v"(p), "v"(v) : "memory"); }
__device__ __forceinline__ void dma16s(const void* sbase, unsigned voff, unsigned ldsaddr) {
    asm volatile("s_mov_b32 m0, %2\n\ts_nop 0\n\tglobal_load_lds_dwordx4 %0, %1 sc1" :: "v"(voff), "s"(sbase), "s"(ldsaddr) : "memory");
}
__device__ __forceinline__ void dma4s(const void* sbase, unsigned voff, unsigned ldsaddr) {
    asm volatile("s_mov_b32 m0, %2\n\ts_nop 0\n\tglobal_load_lds_dword %0, %1 sc1" :: "v"(voff), "s"(sbase), "s"(ldsaddr) : "memory");
}
__device__ __forceinline__ void set_flag(unsigned* f, unsigned tag) { __hip_atomic_store(f, tag, __ATOMIC_RELAXED, __HIP_MEMORY_SCOPE_AGENT); }
__device__ __forceinline__ void set_flag_rep(unsigned* f, unsigned tag, int lane) { __hip_atomic_store(f + lane * FL_REP, tag, __ATOMIC_RELAXED, __HIP_MEMORY_SCOPE_AGENT); }
template <int N, int S>
__device__ __forceinline__ void wait_flags(unsigned* f, unsigned tag, int lane) {
    for (unsigned spins = 0; spins < (1u << 18); ++spins) {
        bool ok = true;
        if (lane < N) ok = __hip_atomic_load(f + lane * S, __ATOMIC_RELAXED, __HIP_MEMORY_SCOPE_AGENT) == tag;
        if (N > 64 && lane + 64 < N) ok &= __hip_atomic_load(f + (64 + lane) * S, __ATOMIC_RELAXED, __HIP_MEMORY_SCOPE_AGENT) == tag;
        if (N > 128 && lane + 128 < N) ok &= __hip_atomic_load(f + (128 + lane) * S, __ATOMIC_RELAXED, __HIP_MEMORY_SCOPE_AGENT) == tag;
        if (__all(ok)) break;
        __builtin_amdgcn_s_sleep(1);
    }
    asm volatile("" ::: "memory");
}

__device__ __forceinline__ unsigned epoch_tag(const char* ws) {
    unsigned e;
    asm volatile("s_load_dword %0, %1, 0x0\n\ts_waitcnt lgkmcnt(0)" : "=s"(e) : "s"(ws + OFF_FL + FL_E * 4) : "memory");
    return e + 1u;
}

constexpr int G1_NS = 7, G1_SS = 20480, G1_W = 8192;
constexpr int G2_NS = 4, G2_SS = 36 * 1024, G2_W = 16 * 1024;
constexpr int G2_H = G2_NS * G2_SS;
constexpr int G1_CS = G2_H + 10240;
constexpr int G1_ST = G1_CS + 3840;
constexpr int G1_SP = G1_ST + 1024;
constexpr int LDS_BYTES = 163840;
static_assert(G1_SP + 512 <= LDS_BYTES, "LDS map");
constexpr int KG_ROW = 272;
constexpr int KG_WAVE = 57 * KG_ROW;
constexpr int CV_KCH = 8320;
__host__ __device__ constexpr int cv_k(int c) { return (4 + c) * KG_WAVE + 3072; }
__host__ __device__ constexpr int cv_kc(int c) { return cv_k(c) + CV_KCH; }
static_assert(3072 + CV_KCH + 1296 <= KG_WAVE, "per-channel area");
constexpr int CV_X = 8 * KG_WAVE + 7 * KG_ROW + 16;
constexpr int CV_U = CV_X + 128 * 128;
constexpr int CV_M = CV_U + 144 * 128;
static_assert(CV_X % 128 == 0 || true, "");
static_assert(CV_M + 32 + 256 <= 163840 && CV_X % 16 == 0, "LDS map");
constexpr int LN_ROW = 1344;

__device__ __forceinline__ unsigned ln1_unit(int j, const bf16_t* __restrict__ x, char* __restrict__ ws PROF_ARG) {
    const int tid = threadIdx.x, r = tid >> 4, l16 = tid & 15, lane = tid & 63, w = uni(tid >> 6);
    const int t = 32 * j + r;
    u32x4 v[5];
#pragma unroll
    for (int i = 0; i < 5; ++i) v[i] = *(const u32x4*)(x + (size_t)t * D + (l16 + 16 * i) * 8);
    const unsigned tag = epoch_tag(ws);
    f32x2 f[20];
    f32x2 s2 = {0.f, 0.f};
#pragma unroll
    for (int i = 0; i < 5; ++i)
#pragma unroll
        for (int e = 0; e < 4; ++e) { f[4 * i + e] = (f32x2){bflo(v[i][e]), bfhi(v[i][e])}; s2 += f[4 * i + e]; }
    const float mean = row16_sum(s2[0] + s2[1]) * (1.0f / (float)D);
    f32x2 q2 = {0.f, 0.f};
#pragma unroll
    for (int i = 0; i < 20; ++i) { const f32x2 dv = f[i] - mean; q2 += dv * dv; }
    const float var = row16_sum(q2[0] + q2[1]) * (1.0f / (float)D);
    const float rs = __builtin_amdgcn_rcpf(rbf(sqrtf(rbf(rbf(var) + 1e-5f)))), nm = -rbf(mean) * rs;
    u32x4 o;
#pragma unroll
    for (int i = 0; i < 4; ++i) {
        const unsigned a = (unsigned)__builtin_amdgcn_readlane((int)__float_as_uint(rs), 16 * i), c = (unsigned)__builtin_amdgcn_readlane((int)__float_as_uint(nm), 16 * i);
        o[i] = lane == 0 ? a : c;
    }
    if (lane < 2) st_dev(ws + OFF_S1 + (size_t)lane * (T * 4) + (32 * j + 4 * w) * 4, o);
    if (tid < 4) set_flag((unsigned*)(ws + OFF_FL) + FL_HC + (4 * j + tid) * 32, tag << 3);
    STAMP(24);
    return tag;
}

__device__ __forceinline__ void wgtile_unit(int b, const bf16_t* __restrict__ wg, const bf16_t* __restrict__ wu, char* __restrict__ ws, lptr sm) {
    const int tid = threadIdx.x, lane = tid & 63, w = uni(tid >> 6);
    const int j = b >> 2, kq = b & 3;
    const int c16 = tid & 15, r0 = tid >> 4;
    u32x4 v[5];
    const bf16_t* src = (c16 < 8 ? wg : wu) + (size_t)(160 * kq + r0) * F + 64 * j + (c16 & 7) * 8;
#pragma unroll
    for (int i = 0; i < 5; ++i) v[i] = *(const u32x4*)(src + (size_t)(32 * i) * F);
    f32x2 a[4] = {};
#pragma unroll
    for (int i = 0; i < 5; ++i) {
        const int k = 32 * i + r0;
        *(LAS u32x4*)(sm + k * 256 + (((c16 >> 1) ^ (k & 3)) * 32) + (c16 & 1) * 16) = v[i];
#pragma unroll
        for (int e = 0; e < 4; ++e) a[e] += (f32x2){bflo(v[i][e]), bfhi(v[i][e])};
    }
#pragma unroll
    for (int e = 0; e < 4; ++e)
#pragma unroll
        for (int hf = 0; hf < 2; ++hf) {
            const float s = xsum16_32(a[e][hf]);
            if (lane < 16) *(LAS float*)(sm + 40960 + (w * 128 + c16 * 8 + 2 * e + hf) * 4) = s;
        }
    bar();
    const int g = lane >> 4, q = (lane & 15) >> 2, p = lane & 3;
    u32x4 o[5];
#pragma unroll
    for (int i = 0; i < 5; ++i) {
        const int item = w + 8 * i, ftl = item & 3, sub = item >> 2;
        const int k = 16 * sub + 4 * (g >> 1) + q;
        o[i] = tr_read2(sm + k * 256 + ((((g & 1) * 4 + ftl) ^ q) * 32) + p * 8, 8 * 256);
    }
#pragma unroll
    for (int i = 0; i < 5; ++i) {
        const int item = w + 8 * i, ftl = item & 3, sub = item >> 2;
        const int ftg = 4 * j + ftl, cb = ftg / 6, ft = ftg % 6;
        st_dev(ws + OFF_WG + ((((size_t)cb * 40 + 10 * kq + sub) * 6 + ft) * 64 + lane) * 16, o[i]);
    }
    if (tid < 128) {
        float s = 0.f;
#pragma unroll
        for (int i = 0; i < 8; ++i) s += *(LAS float*)(sm + 40960 + (i * 128 + tid) * 4);
        st_dev4(ws + OFF_CS + (size_t)(kq * 2 * F + (tid >> 6) * F + 64 * j + (tid & 63)) * 4, s);
    }
}

__device__ __forceinline__ void wdtile_unit(int fb, const bf16_t* __restrict__ wd, char* __restrict__ ws, lptr sm) {
    const int tid = threadIdx.x, lane = tid & 63, w = uni(tid >> 6);
    u32x4 v[5];
#pragma unroll
    for (int i = 0; i < 5; ++i) { const int ch = tid + 512 * i, row = ch / 80, c = ch % 80; v[i] = *(const u32x4*)(wd + (size_t)(32 * fb + row) * D + c * 8); }
#pragma unroll
    for (int i = 0; i < 5; ++i) { const int ch = tid + 512 * i, row = ch / 80, c = ch % 80; *(LAS u32x4*)(sm + row * LN_ROW + c * 16) = v[i]; }
    bar();
    const int g = lane >> 4, q = (lane & 15) >> 2, p = lane & 3;
    u32x4 o[5];
#pragma unroll
    for (int i = 0; i < 5; ++i) o[i] = tr_read2(sm + (16 * (g >> 1) + 4 * (g & 1) + q) * LN_ROW + (16 * (w + 8 * i) + 4 * p) * 2, 8 * LN_ROW);
#pragma unroll
    for (int i = 0; i < 5; ++i) st_dev(ws + OFF_WD + (((size_t)fb * 40 + w + 8 * i) * 64 + lane) * 16, o[i]);
}

__device__ __forceinline__ f32x2 cmul(f32x2 a, f32x2 b) {
    const f32x2 t = (f32x2){a[0], a[0]} * b;
    const f32x2 bs = {-b[1], b[0]};
    return __builtin_elementwise_fma((f32x2){a[1], a[1]}, bs, t);
}
__device__ __forceinline__ unsigned cmul_h(unsigned a, unsigned b) {
    unsigned t, r;
    asm("v_pk_mul_f16 %0, %1, %2 op_sel:[0,0] op_sel_hi:[0,1]" : "=v"(t) : "v"(a), "v"(b));
    asm("v_pk_fma_f16 %0, %1, %2, %3 op_sel:[1,1,0] op_sel_hi:[1,0,1] neg_lo:[0,1,0]" : "=v"(r) : "v"(a), "v"(b), "v"(t));
    return r;
}
__device__ __forceinline__ f32x2 cexp_p(float arl, float air, float p) {
    const float mag = __builtin_amdgcn_exp2f(arl * p);
    float rev = air * p; rev -= rintf(rev);
    return (f32x2){mag * __builtin_amdgcn_cosf(rev), mag * __builtin_amdgcn_sinf(rev)};
}
struct KgenIn { unsigned ldt, la, ai, cr[3], ci[3]; };
__device__ __forceinline__ void kgen_load(KgenIn& k, int d, int third, int lane, const bf16_t* __restrict__ logA, const bf16_t* __restrict__ Aim,
                                          const bf16_t* __restrict__ Cre, const bf16_t* __restrict__ Cim, const bf16_t* __restrict__ logdt) {
    k.ldt = logdt[d]; k.la = logA[d * NM + 64 * third + lane]; k.ai = Aim[d * NM + 64 * third + lane];
#pragma unroll
    for (int i = 0; i < 3; ++i) { k.cr[i] = Cre[d * NM + 64 * i + lane]; k.ci[i] = Cim[d * NM + 64 * i + lane]; }
}
__device__ __forceinline__ void kgen_task(const KgenIn& k, int third, int lane, lptr tab, f32x4 (&acc)[3], float& unscale) {
    const float dt = rbf(expf(bf2f(k.ldt)));
    float mx = 0.f;
#pragma unroll
    for (int i = 0; i < 3; ++i) mx = fmaxf(mx, fmaxf(fabsf(bf2f(k.cr[i])), fabsf(bf2f(k.ci[i]))));
    mx = row16_max(mx);
    {
        const auto r1 = __builtin_amdgcn_permlane16_swap(__float_as_uint(mx), __float_as_uint(mx), false, false);
        mx = fmaxf(__uint_as_float(r1[0]), __uint_as_float(r1[1]));
        const auto r2 = __builtin_amdgcn_permlane32_swap(__float_as_uint(mx), __float_as_uint(mx), false, false);
        mx = fmaxf(__uint_as_float(r2[0]), __uint_as_float(r2[1]));
    }
    mx *= 2.f * dt;
    int e = (int)((__float_as_uint(mx) >> 23) & 0xff) - 126;
    e = e < -100 ? -100 : (e > 100 ? 100 : e);
    const float scale = __uint_as_float((unsigned)(127 - e) << 23);
    unscale = __uint_as_float((unsigned)(127 + e) << 23);
    const float are = -rbf(__builtin_amdgcn_exp2f(bf2f(k.la) * 1.4426950408889634f)) * dt, aim = bf2f(k.ai) * dt;
    const float crw = bf2f(third == 0 ? k.cr[0] : (third == 1 ? k.cr[1] : k.cr[2])) * dt, ciw = bf2f(third == 0 ? k.ci[0] : (third == 1 ? k.ci[1] : k.ci[2])) * dt;
    const float arl = are * 1.4426950408889634f, air = aim * 0.15915494309189535f;
    lptr base = tab + 4 * lane;
#define PUT(row, z) *(LAS unsigned*)(base + (row) * KG_ROW) = pk_f16(z)
#define PUTH(row, z) *(LAS unsigned*)(base + (row) * KG_ROW) = (z)
    const f32x2 w1 = cexp_p(arl, air, 1.f), w8 = cexp_p(arl, air, 8.f), H1 = cexp_p(arl, air, 128.f);
    f32x2 qv = {2.f * scale * crw, -2.f * scale * ciw};
    const f32x2 w8c = {w8[0], -w8[1]};
    f32x2 Ec = w1, Hc = H1;
    unsigned Eh[8], Hh[8];
    Eh[1] = pk_f16(w1); Hh[1] = pk_f16(H1);
    PUT(0, qv); PUTH(16, 0x00003c00u); PUTH(17, Eh[1]); PUTH(24, Hh[1]);
#pragma unroll
    for (int i = 1; i < 16; ++i) {
        qv = cmul(qv, w8c);
        Hc = cmul(Hc, H1);
        if (i < 7) Ec = cmul(Ec, w1);
        PUT(i, qv);
        if (i < 7) { Eh[i + 1] = pk_f16(Ec); PUTH(17 + i, Eh[i + 1]); }
        const int hi = i + 1;
        if (hi < 8) { Hh[hi] = pk_f16(Hc); PUTH(hi < 4 ? 32 + 4 * (hi - 2) : 40 + 2 * (hi - 4), Hh[hi]); } else PUT(48 + (hi - 8), Hc);
        if (i >= 7 && i < 14) PUTH(25 + (i - 7), cmul_h(Eh[i - 6], Hh[1]));
        if (i >= 7 && i < 10) PUTH(33 + (i - 7), cmul_h(Eh[2 * (i - 6)], Hh[2]));
        if (i >= 10 && i < 13) PUTH(37 + (i - 10), cmul_h(Eh[2 * (i - 9)], Hh[3]));
        if (i >= 11 && i < 15) PUTH(41 + 2 * (i - 11), cmul_h(Hh[4 + (i - 11)], Eh[4]));
    }
#undef PUTH
#undef PUT
    asm volatile("" ::: "memory");
    lptr rb = tab + (lane & 15) * KG_ROW + (lane >> 4) * 16;
#pragma unroll
    for (int mt = 0; mt < 3; ++mt) acc[mt] = (f32x4){};
#pragma unroll
    for (int ks = 0; ks < 4; ++ks) {
        const h16x8 bq = *(LAS h16x8*)(rb + ks * 64);
#pragma unroll
        for (int mt = 0; mt < 3; ++mt) {
            const h16x8 ar = *(LAS h16x8*)(rb + (16 + 16 * mt) * KG_ROW + ks * 64);
            acc[mt] = __builtin_amdgcn_mfma_f32_16x16x32_f16(ar, bq, acc[mt], 0, 0, 0);
        }
    }
#pragma unroll
    for (int mt = 0; mt < 3; ++mt) PIN(acc[mt]);
    asm volatile("s_waitcnt lgkmcnt(0)" ::: "memory");
}

__device__ __forceinline__ void g1_prefetch_w(char* __restrict__ ws, lptr sm, int cb, int lw, int lane) {
    const unsigned sm0 = (unsigned)(size_t)sm;
    const char* wsrc = ws + OFF_WG + (size_t)cb * 40 * 6144 + 3 * lw * 1024;
#pragma unroll
    for (int s = 0; s < 4; ++s)
#pragma unroll
        for (int i = 0; i < 3; ++i) dma16s(wsrc + (size_t)s * 12288 + i * 1024, lane * 16, sm0 + s * G1_SS + G1_W + (3 * lw + i) * 1024);
    if (lw < 3) {
        const int f0 = 96 * cb;
        const unsigned co = (unsigned)(((lw == 0 ? f0 + lane : (lw == 1 ? (lane < 32 ? f0 + 64 + lane : F + f0 + lane - 32) : F + f0 + 32 + lane))) * 4);
#pragma unroll
        for (int kq = 0; kq < 4; ++kq) dma4s(ws + OFF_CS + kq * 2 * F * 4, co, sm0 + G1_CS + kq * 768 + lw * 256);
    }
}

struct KTab { unsigned short v[2048]; };
constexpr int ci_of(int l) {
    if (l < 0) return 641;
    if (l < 256) return l;
    const int s = l < 512 ? 1 : (l < 1024 ? 2 : 3);
    return ((l + (1 << (s - 1)) - 1 + ((l >> s) & 1)) >> s) + 128 * s;
}
constexpr KTab make_tab() { KTab t{}; for (int y = 0; y < 2048; ++y) t.v[y] = (unsigned short)(2 * ci_of(2047 - y)); return t; }
__device__ const __attribute__((aligned(16))) KTab g_ktab = make_tab();

struct Params {
    const bf16_t *x, *logA, *Aim, *Cre, *Cim, *logdt, *Dskip, *wg, *wu, *wd;
    char* ws;
    bf16_t* out;
};

__device__ __forceinline__ unsigned conv_wg(const Params& P, int b, int cb, lptr sm PROF_ARG) {
    const int tid = threadIdx.x, lane = tid & 63, w = uni(tid >> 6);
    char* ws = P.ws;
    const int d0 = 4 * ((b & 7) * 20 + (b >> 3));
    const unsigned sm0 = (unsigned)(size_t)sm;
    unsigned* fl = (unsigned*)(ws + OFF_FL);
    KgenIn k1, k2;
    u32x4 tb[4];
    u32x2 xg[8];
    kgen_load(k1, d0 + w / 3, w % 3, lane, P.logA, P.Aim, P.Cre, P.Cim, P.logdt);
    const unsigned tag = epoch_tag(ws);
    if (w < 4) {
        const int t2 = 8 + w;
        kgen_load(k2, d0 + t2 / 3, t2 % 3, lane, P.logA, P.Aim, P.Cre, P.Cim, P.logdt);
#pragma unroll
        for (int i = 0; i < 4; ++i) tb[i] = *(const u32x4*)(&g_ktab.v[8 * (lane + 64 * i)]);
    }
    const unsigned dsk_raw = ((const unsigned*)P.Dskip)[(d0 + (w & 3)) >> 1];
    asm volatile("" : "+v"(k1.ldt), "+v"(k1.la), "+v"(k1.ai), "+v"(k1.cr[0]), "+v"(k1.cr[1]), "+v"(k1.cr[2]), "+v"(k1.ci[0]), "+v"(k1.ci[1]), "+v"(k1.ci[2]) :: "memory");
    STAMP(1);
    const lptr tab = sm + w * KG_WAVE;
    LAS unsigned* cnt = (LAS unsigned*)(sm + CV_M + 16);
    if (w == 7 && lane == 0) *cnt = 0;
    bar();
    if (w >= 4) {
#pragma unroll
        for (int i = 0; i < 8; ++i) xg[i] = *(const u32x2*)(P.x + (size_t)(8 * (tid - 256) + i) * D + d0);
        asm volatile("" ::: "memory");
    }
    f32x4 a1[3], a2[3];
    {
        int third = w % 3, chn = w / 3;
#pragma unroll 1
        for (int rnd = 0; rnd < 2; ++rnd) {
            float un;
            kgen_task(k1, third, lane, tab, a2, un);
            if (third == 0 && lane == 0) *(LAS float*)(sm + CV_M + chn * 4) = un;
            if (rnd == 1 || w >= 4) break;
#pragma unroll
            for (int mt = 0; mt < 3; ++mt) a1[mt] = a2[mt];
            k1 = k2; third = (8 + w) % 3; chn = (8 + w) / 3;
        }
    }
    if (w < 4) {
#pragma unroll
        for (int mt = 0; mt < 3; ++mt) { *(LAS f32x4*)(tab + (mt * 64 + lane) * 16) = a1[mt]; *(LAS f32x4*)(tab + 3072 + (mt * 64 + lane) * 16) = a2[mt]; }
        asm volatile("s_waitcnt lgkmcnt(0)" ::: "memory");
        if (lane == 0) __hip_atomic_fetch_add(cnt, 1u, __ATOMIC_RELAXED, __HIP_MEMORY_SCOPE_WORKGROUP);
        while (__hip_atomic_load(cnt, __ATOMIC_RELAXED, __HIP_MEMORY_SCOPE_WORKGROUP) < 8u) __builtin_amdgcn_s_sleep(0);
        asm volatile("" ::: "memory");
        STAMP(2);
        f32x4 ks[3];
        lptr pt[3];
#pragma unroll
        for (int j = 0; j < 3; ++j) { const int t = 3 * w + j; pt[j] = sm + (t & 7) * KG_WAVE + (t >> 3) * 3072 + lane * 16; }
        const float un = *(LAS float*)(sm + CV_M + w * 4);
#pragma unroll
        for (int mt = 0; mt < 3; ++mt) ks[mt] = ((*(LAS f32x4*)(pt[0] + mt * 1024) + *(LAS f32x4*)(pt[1] + mt * 1024)) + *(LAS f32x4*)(pt[2] + mt * 1024)) * un;
        lptr kc = sm + cv_kc(0) + w * KG_WAVE;
        const int hq = lane >> 4, col = lane & 15;
        const u32x2 p0 = {pk_bf16(ks[0][0], ks[0][1]), pk_bf16(ks[0][2], ks[0][3])}, p1 = {pk_bf16(ks[1][0], ks[1][1]), pk_bf16(ks[1][2], ks[1][3])};
        const u32x2 p2 = {pk_bf16(ks[2][0], ks[2][1]), pk_bf16(ks[2][2], ks[2][3])};
        *(LAS u32x2*)(kc + 2 * (128 * (hq >> 1) + 4 * (hq & 1) + 8 * col)) = p0;
        if (hq < 2) *(LAS u32x2*)(kc + 2 * (256 + 64 * hq + 4 * col)) = p1;
        else { *(LAS unsigned*)(kc + 2 * (384 + 64 * (hq - 2) + 2 * col)) = p1[0]; *(LAS unsigned*)(kc + 2 * (416 + 64 * (hq - 2) + 2 * col)) = p1[1]; }
        if (hq < 2) {
            lptr k2p = kc + 2 * (512 + 64 * hq + col);
            *(LAS unsigned short*)k2p = (unsigned short)p2[0]; *(LAS unsigned short*)(k2p + 32) = (unsigned short)(p2[0] >> 16);
            *(LAS unsigned short*)(k2p + 64) = (unsigned short)p2[1]; *(LAS unsigned short*)(k2p + 96) = (unsigned short)(p2[1] >> 16);
        } else if (lane == 32) *(LAS unsigned short*)(kc + 2 * 640) = (unsigned short)p2[0];
        else if (lane >= 48 && lane < 55) *(LAS unsigned short*)(kc + 2 * (641 + lane - 48)) = 0;
        asm volatile("" ::: "memory");
        STAMP(13);
        u32x4 o[4];
#pragma unroll
        for (int i = 0; i < 4; ++i)
#pragma unroll
            for (int k = 0; k < 4; ++k) {
                const unsigned tk = tb[i][k];
                const unsigned lo = *(LAS unsigned short*)(kc + (tk & 0xffffu)), hi = *(LAS unsigned short*)(kc + (tk >> 16));
                o[i][k] = lo | (hi << 16);
            }
        lptr dst = sm + cv_k(0) + w * KG_WAVE;
#pragma unroll
        for (int i = 0; i < 4; ++i) {
            *(LAS u32x4*)(dst + (lane + 64 * i) * 16) = o[i];
            unsigned nx = (unsigned)__builtin_amdgcn_update_dpp(0, (int)o[i][0], 0x130, 0xF, 0xF, false);
            const unsigned wrap = i < 3 ? (unsigned)__builtin_amdgcn_readlane((int)o[i < 3 ? i + 1 : 3][0], 0) : 0u;
            nx = lane == 63 ? wrap : nx;
            const u32x4 od = {__builtin_amdgcn_alignbit(o[i][1], o[i][0], 16), __builtin_amdgcn_alignbit(o[i][2], o[i][1], 16),
                              __builtin_amdgcn_alignbit(o[i][3], o[i][2], 16), __builtin_amdgcn_alignbit(nx, o[i][3], 16)};
            *(LAS u32x4*)(dst + 4160 + (lane + 64 * i) * 16) = od;
        }
        if (lane < 8) *(LAS u32x4*)(dst + (lane >> 2) * 4160 + 4096 + (lane & 3) * 16) = (u32x4){0u, 0u, 0u, 0u};
        STAMP(14);
    } else {
#pragma unroll
        for (int mt = 0; mt < 3; ++mt) *(LAS f32x4*)(tab + (mt * 64 + lane) * 16) = a2[mt];
        asm volatile("s_waitcnt lgkmcnt(0)" ::: "memory");
        if (lane == 0) __hip_atomic_fetch_add(cnt, 1u, __ATOMIC_RELAXED, __HIP_MEMORY_SCOPE_WORKGROUP);
        const int gm = tid - 256, ln = gm >> 1, sw = ((ln >> 1) & 3) << 1;
        u32x4 xc[4];
#pragma unroll
        for (int ch = 0; ch < 4; ++ch) {
#pragma unroll
            for (int k = 0; k < 4; ++k) xc[ch][k] = __builtin_amdgcn_perm(xg[2 * k + 1][ch >> 1], xg[2 * k][ch >> 1], (ch & 1) ? 0x07060302u : 0x05040100u);
            *(LAS u32x4*)(sm + CV_X + ln * 128 + 16 * ((2 * ch + (gm & 1)) ^ sw)) = xc[ch];
        }
        if (gm < 128) *(LAS u32x4*)(sm + CV_U + gm * 16) = (u32x4){0u, 0u, 0u, 0u};
        wait_flags<16, FL_LN_S>(fl + (b & 7) * FL_REP + FL_LN + 16 * (w - 4) * FL_LN_S, tag, lane);
        STAMPL(25);
        f32x4 r0, r1, n0, n1;
        asm volatile("global_load_dwordx4 %0, %4, %5 sc1\n\tglobal_load_dwordx4 %1, %4, %5 offset:16 sc1\n\tglobal_load_dwordx4 %2, %4, %6 sc1\n\tglobal_load_dwordx4 %3, %4, %6 offset:16 sc1\n\ts_waitcnt vmcnt(0)"
                     : "=&v"(r0), "=&v"(r1), "=&v"(n0), "=&v"(n1) : "v"(gm * 32), "s"(ws + OFF_S1), "s"(ws + OFF_S1 + T * 4) : "memory");
#pragma unroll
        for (int ch = 0; ch < 4; ++ch) {
            u32x4 u;
#pragma unroll
            for (int k = 0; k < 4; ++k) {
                const f32x2 rs = k < 2 ? (f32x2){r0[2 * k], r0[2 * k + 1]} : (f32x2){r1[2 * k - 4], r1[2 * k - 3]}, nm = k < 2 ? (f32x2){n0[2 * k], n0[2 * k + 1]} : (f32x2){n1[2 * k - 4], n1[2 * k - 3]};
                const f32x2 a = __builtin_elementwise_fma((f32x2){bflo(xc[ch][k]), bfhi(xc[ch][k])}, rs, nm);
                u[k] = pk_bf16(a[0], a[1]);
            }
            *(LAS u32x4*)(sm + CV_U + 2048 + ln * 128 + 16 * ((2 * ch + (gm & 1)) ^ sw)) = u;
        }
    }
    bar();
    STAMP(3); CSTAMP(16);
    if (w < 4) {
        const int n = lane & 15, h = lane >> 4;
        const lptr fb = sm + cv_k(0) + w * KG_WAVE + ((n & 1) ? 0 : 4160) + 4 * (((2031 - n) >> 1) + 4 * h) - 512 * 7;
        f32x4 acc[8] = {};
        bf16x8 Fa[2][8], Ga[2][8];
        lptr gp, fp[4];
        auto addr = [&](int rr) {
            const int ln = n + (h >> 1) + 15 - 2 * rr;
            gp = sm + CV_U + 128 * ln + 16 * ((2 * w + (h & 1)) ^ (((ln >> 1) & 3) << 1));
#pragma unroll
            for (int k = 0; k < 4; ++k) { fp[k] = fb - 64 * rr + 1024 * k; asm volatile("" : "+v"(fp[k])); }
        };
        auto ldG = [&](int Q, int c) { Ga[Q][c] = *(LAS bf16x8*)(gp + 2048 * c); };
        u32x4 ftmp[2];
        auto ldF = [&](int Q, int a, int half) {
            lptr q = fp[(7 - a) >> 1] + 512 * ((7 - a) & 1) + 8 * half;
            ftmp[a & 1][2 * half] = *(LAS unsigned*)q; ftmp[a & 1][2 * half + 1] = *(LAS unsigned*)(q + 4);
            if (half) Fa[Q][a] = __builtin_bit_cast(bf16x8, ftmp[a & 1]);
        };
        const int so = n * 128 + 16 * ((2 * w + (h >> 1)) ^ (((n >> 1) & 3) << 1)) + 8 * (h & 1);
        u32x2 uv[8], xv[8];
        addr(0);
#pragma unroll
        for (int c = 0; c < 8; ++c) { ldF(0, c, 0); ldF(0, c, 1); ldG(0, c); }
        SB();
#pragma unroll
        for (int rr = 0; rr < 8; ++rr) {
            const int Q = rr & 1;
            int i = 0;
#pragma unroll
            for (int a = 0; a < 8; ++a)
#pragma unroll
                for (int c = a; c < 8; ++c) {
                    acc[c] = __builtin_amdgcn_mfma_f32_16x16x32_bf16(Fa[Q][a], Ga[Q][c - a], acc[c], 0, 0, 0); PIN(acc[c]); SB();
                    if (rr < 7) {
                        if (i == 0) { addr(rr + 1); SB(); }
                        else if (i <= 24) { const int k = (i - 1) / 3, m = (i - 1) % 3; if (m == 0) ldG(Q ^ 1, k); else ldF(Q ^ 1, k, m - 1); SB(); }
                    } else if (i < 16) {
                        if (i & 1) xv[i >> 1] = *(LAS u32x2*)(sm + CV_X + so + 2048 * (i >> 1)); else uv[i >> 1] = *(LAS u32x2*)(sm + CV_U + 2048 + so + 2048 * (i >> 1));
                        SB();
                    }
                    ++i;
                }
        }
        STAMP(4); CSTAMP(17);
        const int d = d0 + w;
        lptr yb = sm + cv_k(0) + w * KG_WAVE;
        const float dsk = (d & 1) ? bfhi(dsk_raw) : bflo(dsk_raw);
        const f32x2 dk2 = {dsk, dsk};
#pragma unroll
        for (int c2 = 0; c2 < 8; c2 += 2) {
            f32x2 hf[4];
#pragma unroll
            for (int i = 0; i < 4; ++i) { const unsigned e = uv[c2 + (i >> 1)][i & 1]; hf[i] = __builtin_elementwise_fma((f32x2){bflo(e), bfhi(e)}, dk2, (f32x2){acc[c2 + (i >> 1)][2 * (i & 1)], acc[c2 + (i >> 1)][2 * (i & 1) + 1]}); }
#pragma unroll
            for (int i = 0; i < 4; ++i) { const unsigned e = xv[c2 + (i >> 1)][i & 1]; hf[i] += (f32x2){bflo(e), bfhi(e)}; }
#pragma unroll
            for (int j = 0; j < 2; ++j) *(LAS u32x2*)(yb + (256 * (c2 + j) + 16 * n + 4 * h) * 2) = (u32x2){pk_bf16(hf[2 * j][0], hf[2 * j][1]), pk_bf16(hf[2 * j + 1][0], hf[2 * j + 1][1])};
        }
        asm volatile("" ::: "memory");
#pragma unroll
        for (int j = 0; j < 4; ++j) st_dev_o(ws + OFF_HT + (size_t)d * (T * 2) + j * 1024, lane * 16, *(LAS u32x4*)(yb + (64 * j + lane) * 16));
        STAMP(27);
        wait_vm<0>();
    } else if (w == 6) wait_flags<96, FL_W_S>(fl + (b & 7) * FL_REP + FL_WG, tag, lane);
    bar();
    if (tid < 8) __hip_atomic_fetch_add(fl + FL_HC + (tid * 32 + (b & 31)) * 32, 1u, __ATOMIC_RELAXED, __HIP_MEMORY_SCOPE_AGENT);
    STAMP(5);
    return tag;
}

__device__ __forceinline__ unsigned spare_wg(const Params& P, int j, int cb, lptr sm PROF_ARG) {
    const int tid = threadIdx.x, lane = tid & 63, w = uni(tid >> 6);
    unsigned* fl = (unsigned*)(P.ws + OFF_FL);
    unsigned tag;
    if (j < 64) tag = ln1_unit(j, P.x, P.ws PROF_FWD); else tag = epoch_tag(P.ws);
    if (j < 64) {
        wait_vm<0>();
        bar();
        if (tid < 8) set_flag_rep(fl + FL_LN + j * FL_LN_S, tag, tid);
    }
    STAMP(1);
    const int n_g = j >= 64 ? 2 : (j < 32 ? 1 : 0), g0 = j >= 64 ? 2 * (j - 64) : 64 + j;
    for (int i = 0; i < n_g; ++i) {
        wgtile_unit(g0 + i, P.wg, P.wu, P.ws, sm);
        wait_vm<0>();
        bar();
        if (tid < 8) set_flag_rep(fl + FL_WG + (g0 + i) * FL_W_S, tag, tid);
    }
    STAMP(2);
    if (j >= 64 || (j >= 32 && j < 48)) {
        const int fb = j >= 64 ? j - 64 : j;
        wdtile_unit(fb, P.wd, P.ws, sm);
        wait_vm<0>();
        bar();
        if (tid < 8) set_flag_rep(fl + FL_WD + fb * FL_W_S, tag, tid);
    }
    if (w == 6) wait_flags<96, FL_W_S>(fl + (blockIdx.x & 7) * FL_REP + FL_WG, tag, lane);
    bar();
    STAMP(5);
    return tag;
}

template <int WC>
struct G1Compute {
    f32x16 acc[3][2];
    bf16x8 Af[2][3], Bf[2][2];
    template <int P> __device__ __forceinline__ void mm(int ft, int tt) {
        acc[ft][tt] = __builtin_amdgcn_mfma_f32_32x32x16_bf16(Af[P][ft], Bf[P][tt], acc[ft][tt], 0, 0, 0); PIN(acc[ft][tt]); SB();
    }
    __device__ __forceinline__ static bf16x8 rdB(lptr p) { return __builtin_bit_cast(bf16x8, tr_read2(p, 8 * 256)); }
    template <int P, int KK, bool MORE, bool BAR>
    __device__ __forceinline__ void substep(lptr pa, const lptr (&pb)[2]) {
        mm<P>(0, 0);
        if (BAR) { sbar(); SB(); }
        if (MORE) { Bf[P ^ 1][0] = rdB(pb[0] + KK * 16 * 256); Af[P ^ 1][0] = *(LAS bf16x8*)(pa + KK * 6144); SB(); }
        mm<P>(0, 1);
        if (MORE) { Bf[P ^ 1][1] = rdB(pb[1] + KK * 16 * 256); Af[P ^ 1][1] = *(LAS bf16x8*)(pa + KK * 6144 + 1024); SB(); }
        mm<P>(1, 0);
        if (MORE) { Af[P ^ 1][2] = *(LAS bf16x8*)(pa + KK * 6144 + 2048); SB(); }
        mm<P>(1, 1);
        mm<P>(2, 0);
        mm<P>(2, 1);
    }
};

template <int WC>
__device__ __forceinline__ void g1_compute(lptr sm, char* __restrict__ ws, int wr, int lane, int t0, int cb PROF_ARG) {
    const int g = lane >> 4, q = (lane & 15) >> 2, p = lane & 3, hh = g >> 1, krow = 4 * hh + q;
    G1Compute<WC> C;
#pragma unroll
    for (int ft = 0; ft < 3; ++ft)
#pragma unroll
        for (int tt = 0; tt < 2; ++tt) C.acc[ft][tt] = (f32x16){};
    const lptr a0 = sm + G1_W + 3 * WC * 1024 + lane * 16;
    lptr b0[2];
#pragma unroll
    for (int tt = 0; tt < 2; ++tt) b0[tt] = sm + krow * 256 + (((2 * wr + tt) ^ q) * 64) + (g & 1) * 32 + p * 8;
    sbar();
    STAMP(7); CSTAMP(18);
#pragma unroll
    for (int tt = 0; tt < 2; ++tt) C.Bf[0][tt] = C.rdB(b0[tt]);
#pragma unroll
    for (int ft = 0; ft < 3; ++ft) C.Af[0][ft] = *(LAS bf16x8*)(a0 + ft * 1024);
    SB();
    lptr pa = a0, pb[2] = {b0[0], b0[1]};
    int nb = 0;
#pragma unroll 1
    for (int s = 0; s < 19; ++s) {
        C.template substep<0, 1, true, false>(pa, pb);
        nb = nb + G1_SS == G1_NS * G1_SS ? 0 : nb + G1_SS;
        pa = a0 + nb; pb[0] = b0[0] + nb; pb[1] = b0[1] + nb;
        SB();
        C.template substep<1, 0, true, true>(pa, pb);
    }
    C.template substep<0, 1, true, false>(pa, pb);
    C.template substep<1, 0, false, false>(pa, pb);
    STAMP(8); CSTAMP(19);
    bar();
    char* mF = ws + OFF_MF;
    f32x2 nm2[2], k1[2], k2[2], s2[2];
#pragma unroll
    for (int tt = 0; tt < 2; ++tt) {
        const f32x2 sq = *(LAS f32x2*)(sm + G1_ST + (32 * (2 * wr + tt) + (lane & 31)) * 8);
        const float rs = __builtin_amdgcn_rcpf(sq[1]);
        nm2[tt] = (f32x2){-sq[0], -sq[0]};
        k1[tt] = (f32x2){-2.302208198f * rs, -2.302208198f * rs};
        k2[tt] = (f32x2){-0.1029432397f * rs * rs * rs, -0.1029432397f * rs * rs * rs};
        s2[tt] = (f32x2){sq[1] * sq[1], sq[1] * sq[1]};
    }
    const unsigned mo = (unsigned)((((t0 + 64 * wr + (lane & 31)) >> 4) * 48 * 64 + ((lane & 15) + 16 * hh)) * 16);
#pragma unroll
    for (int ft = 0; ft < 3; ++ft) {
        const int fl = 16 * (3 * WC + ft) + 4 * hh;
        lptr cp = sm + G1_CS + 3072 + fl * 4;
        const f32x4 cg0 = *(LAS f32x4*)cp, cg1 = *(LAS f32x4*)(cp + 32), cu0 = *(LAS f32x4*)(cp + 384), cu1 = *(LAS f32x4*)(cp + 416);
        const int ftg = 6 * cb + 3 * WC + ft;
        const char* mb = mF + (size_t)((ftg >> 1) * 64 + 32 * (ftg & 1)) * 16;
#pragma unroll
        for (int tt = 0; tt < 2; ++tt) {
            f32x2 G[4], U[4], E[4];
#pragma unroll
            for (int r = 0; r < 4; ++r) {
                const f32x2 cg = r < 2 ? (f32x2){cg0[2 * r], cg0[2 * r + 1]} : (f32x2){cg1[2 * r - 4], cg1[2 * r - 3]};
                G[r] = __builtin_elementwise_fma(nm2[tt], cg, (f32x2){C.acc[ft][tt][2 * r], C.acc[ft][tt][2 * r + 1]}); PIN(G[r]);
            }
            SB();
#pragma unroll
            for (int r = 0; r < 4; ++r) { E[r] = G[r] * G[r]; PIN(E[r]); }
            SB();
#pragma unroll
            for (int r = 0; r < 4; ++r) { E[r] = __builtin_elementwise_fma(E[r], k2[tt], k1[tt]); PIN(E[r]); }
            SB();
#pragma unroll
            for (int r = 0; r < 4; ++r) { E[r] = G[r] * E[r]; PIN(E[r]); }
            SB();
#pragma unroll
            for (int r = 0; r < 4; ++r) {
                const f32x2 cu = r < 2 ? (f32x2){cu0[2 * r], cu0[2 * r + 1]} : (f32x2){cu1[2 * r - 4], cu1[2 * r - 3]};
                E[r] = (f32x2){__builtin_amdgcn_exp2f(E[r][0]), __builtin_amdgcn_exp2f(E[r][1])}; PIN(E[r]); SB();
                U[r] = __builtin_elementwise_fma(nm2[tt], cu, (f32x2){C.acc[ft][tt][8 + 2 * r], C.acc[ft][tt][9 + 2 * r]}); PIN(U[r]); SB();
            }
#pragma unroll
            for (int r = 0; r < 4; ++r) { E[r] = __builtin_elementwise_fma(E[r], s2[tt], s2[tt]); PIN(E[r]); }
            SB();
#pragma unroll
            for (int r = 0; r < 4; ++r) {
                E[r] = (f32x2){__builtin_amdgcn_rcpf(E[r][0]), __builtin_amdgcn_rcpf(E[r][1])}; PIN(E[r]); SB();
                G[r] = G[r] * U[r]; PIN(G[r]); SB();
            }
#pragma unroll
            for (int r = 0; r < 4; ++r) { G[r] = G[r] * E[r]; PIN(G[r]); }
            SB();
            u32x4 o;
#pragma unroll
            for (int r = 0; r < 4; ++r) o[r] = pk_bf16(G[r][0], G[r][1]);
            st_dev_o(mb, mo + tt * (2 * 48 * 1024), o);
            SB();
        }
    }
}

__device__ __forceinline__ void g1_loader(char* __restrict__ ws, lptr sm, int lw, int lane, int t0, int cb, unsigned tag) {
    const unsigned sm0 = (unsigned)(size_t)sm;
    {
        const char* hT = ws + OFF_HT;
        const char* wsrc = ws + OFF_WG + (size_t)cb * 40 * 6144 + 3 * lw * 1024;
        unsigned vh[2];
#pragma unroll
        for (int j = 0; j < 2; ++j) {
            const int o = (2 * lw + j) * 1024 + lane * 16, k = o >> 8, c = ((o >> 4) & 15) ^ ((k & 3) << 2);
            vh[j] = (unsigned)(k * (T * 2) + t0 * 2 + c * 16);
        }
        const unsigned vl = lane * 16;
        unsigned long long* st = (unsigned long long*)(ws + OFF_ST);
        auto stage = [&](int s) {
            const unsigned lb = sm0 + (s % G1_NS) * G1_SS;
            const char* hs = hT + (size_t)s * 32 * (T * 2);
            const char* wsx = wsrc + (size_t)s * 12288;
            dma16s(hs, vh[0], lb + (2 * lw) * 1024);
            if (s >= 4) dma16s(wsx, vl, lb + G1_W + (3 * lw) * 1024);
            dma16s(hs, vh[1], lb + (2 * lw + 1) * 1024);
            if (s >= 4) { dma16s(wsx + 1024, vl, lb + G1_W + (3 * lw + 1) * 1024); dma16s(wsx + 2048, vl, lb + G1_W + (3 * lw + 2) * 1024); }
        };
        u32x4 gv[3];
        unsigned long long g0, g1;
        float cs[8], sv[16];
#pragma unroll
        for (int s = 0; s < 4; ++s) stage(s);
        {
            const char* hp = hT + (size_t)(160 * lw) * (T * 2) + (t0 + 8 * cb) * 2;
            const unsigned go = (unsigned)lane * (T * 2);
            asm volatile("global_load_dwordx4 %0, %1, %2 sc1" : "=v"(gv[0]) : "v"(go), "s"(hp) : "memory");
            asm volatile("global_load_dwordx4 %0, %1, %2 sc1" : "=v"(gv[1]) : "v"(go), "s"(hp + (size_t)64 * (T * 2)) : "memory");
            asm volatile("global_load_dwordx4 %0, %1, %2 sc1" : "=v"(gv[2]) : "v"(lane < 32 ? go : 0u), "s"(hp + (size_t)128 * (T * 2)) : "memory");
        }
        wait_vm<9>();
        sbar();
#pragma unroll
        for (int s = 0; s < 19; ++s) {
            const int last = (s + 3) < 19 ? (s + 3) : 19;
            if (s == 3) asm volatile("s_waitcnt vmcnt(10)" : "+v"(gv[0]), "+v"(gv[1]), "+v"(gv[2]) :: "memory");
            else if (s == 17) asm volatile("s_waitcnt vmcnt(5)" : "+v"(g0), "+v"(g1) :: "memory");
            else wait_vm_rt<30>(s == 0 ? 7 : (s == 1 ? 10 : (s == 2 ? 13 : 5 * (last - (s + 1)) + (s == 15 || s == 16 ? 2 : 0))));
            sbar();
            if (s + 4 < 20) stage(s + 4);
            if (s == 11 && lw < 3) {
                lptr cp = sm + G1_CS + lw * 256 + lane * 4;
                *(LAS float*)(cp + 3072) = (*(LAS float*)cp + *(LAS float*)(cp + 768)) + (*(LAS float*)(cp + 1536) + *(LAS float*)(cp + 2304));
                asm volatile("s_waitcnt lgkmcnt(0)" ::: "memory");
            }
            if (s == 14) {
                asm volatile("global_load_dwordx2 %0, %1, %2 sc1" : "=v"(g0) : "v"(lane * 8), "s"(st + t0) : "memory");
                asm volatile("global_load_dwordx2 %0, %1, %2 sc1" : "=v"(g1) : "v"(lane * 8), "s"(st + t0 + 64) : "memory");
            }
            if (s >= 3 && s <= 6) {
#pragma unroll
                for (int e = 2 * (s - 3); e < 2 * (s - 3) + 2; ++e) {
                    const unsigned c32 = (unsigned)__builtin_amdgcn_readlane((int)gv[0][e >> 1], 0);
                    cs[e] = (e & 1) ? bfhi(c32) : bflo(c32);
                    const float d0 = ((e & 1) ? bfhi(gv[0][e >> 1]) : bflo(gv[0][e >> 1])) - cs[e], d1 = ((e & 1) ? bfhi(gv[1][e >> 1]) : bflo(gv[1][e >> 1])) - cs[e];
                    const float d2 = lane < 32 ? ((e & 1) ? bfhi(gv[2][e >> 1]) : bflo(gv[2][e >> 1])) - cs[e] : 0.f;
                    sv[e] = d0 + d1 + d2; sv[8 + e] = d0 * d0 + d1 * d1 + d2 * d2;
                    PIN(sv[e]); PIN(sv[8 + e]);
                }
            }
            if (s == 7) {
#pragma unroll
                for (int j = 0; j < 8; ++j) {
                    const auto r = __builtin_amdgcn_permlane32_swap(__float_as_uint(sv[j]), __float_as_uint(sv[j + 8]), false, false);
                    sv[j] = __uint_as_float(r[0]) + __uint_as_float(r[1]); PIN(sv[j]);
                }
            }
            if (s == 8) {
#pragma unroll
                for (int k = 0; k < 4; ++k) {
                    const auto r = __builtin_amdgcn_permlane16_swap(__float_as_uint(sv[k]), __float_as_uint(sv[k + 4]), false, false);
                    sv[k] = __uint_as_float(r[0]) + __uint_as_float(r[1]);
                }
#pragma unroll
                for (int k = 0; k < 4; ++k) { sv[k] = row16_sum(sv[k]); PIN(sv[k]); }
            }
            if (s == 9) {
                if ((lane & 15) == 0) *(LAS f32x4*)(sm + G1_SP + lw * 96 + (lane >> 4) * 16) = (f32x4){sv[0], sv[1], sv[2], sv[3]};
                if (lane == 0) { *(LAS f32x4*)(sm + G1_SP + lw * 96 + 64) = (f32x4){cs[0], cs[1], cs[2], cs[3]}; *(LAS f32x4*)(sm + G1_SP + lw * 96 + 80) = (f32x4){cs[4], cs[5], cs[6], cs[7]}; }
                asm volatile("s_waitcnt lgkmcnt(0)" ::: "memory");
            }
            if (s == 10 && lw == 3 && lane < 8) {
                float pm[4], m2 = 0.f, mean = 0.f;
#pragma unroll
                for (int i = 0; i < 4; ++i) {
                    const float a = *(LAS float*)(sm + G1_SP + i * 96 + lane * 4) * (1.0f / 160.0f), q = *(LAS float*)(sm + G1_SP + i * 96 + 32 + lane * 4);
                    pm[i] = *(LAS float*)(sm + G1_SP + i * 96 + 64 + lane * 4) + a;
                    m2 += q - 160.0f * a * a; mean += 0.25f * pm[i];
                }
#pragma unroll
                for (int i = 0; i < 4; ++i) m2 += 160.0f * (pm[i] - mean) * (pm[i] - mean);
                const float var = fmaxf(m2 * (1.0f / (float)D), 0.f);
                const unsigned mb = pk_bf16(mean, rbf(sqrtf(rbf(rbf(var) + 1e-5f))));
                __hip_atomic_store(st + t0 + 8 * cb + lane, ((unsigned long long)tag << 32) | mb, __ATOMIC_RELAXED, __HIP_MEMORY_SCOPE_AGENT);
            }
        }
        if (lw == 0) {
            for (unsigned spins = 0; !__all((unsigned)(g0 >> 32) == tag && (unsigned)(g1 >> 32) == tag) && spins < (1u << 18); ++spins) {
                __builtin_amdgcn_s_sleep(2);
                g0 = __hip_atomic_load(st + t0 + lane, __ATOMIC_RELAXED, __HIP_MEMORY_SCOPE_AGENT);
                g1 = __hip_atomic_load(st + t0 + 64 + lane, __ATOMIC_RELAXED, __HIP_MEMORY_SCOPE_AGENT);
            }
            *(LAS f32x2*)(sm + G1_ST + lane * 8) = (f32x2){bflo((unsigned)g0), bfhi((unsigned)g0)};
            *(LAS f32x2*)(sm + G1_ST + (64 + lane) * 8) = (f32x2){bflo((unsigned)g1), bfhi((unsigned)g1)};
        }
        bar();
    }
}

template <int ND>
__device__ __forceinline__ void g2_compute(lptr sm, int lane, int th, int dl0, int t0, int d0, bf16_t* __restrict__ out PROF_ARG) {
    f32x4 acc[ND][2] = {};
    bf16x8 A[2][ND], B[2][2];
    const lptr pb0 = sm + lane * 16 + 2 * th * 4096, pa0 = sm + lane * 16 + G2_W + dl0 * 1024;
    auto ld = [&](int P, lptr pa, lptr pb, int fl) {
#pragma unroll
        for (int tt = 0; tt < 2; ++tt) B[P][tt] = *(LAS bf16x8*)(pb + (tt * 4 + fl) * 1024);
#pragma unroll
        for (int dt = 0; dt < ND; ++dt) A[P][dt] = *(LAS bf16x8*)(pa + (fl * 5 + dt) * 1024);
        SB();
    };
    auto mm = [&](int P) {
#pragma unroll
        for (int dt = 0; dt < ND; ++dt)
#pragma unroll
            for (int tt = 0; tt < 2; ++tt) { acc[dt][tt] = __builtin_amdgcn_mfma_f32_16x16x32_bf16(A[P][dt], B[P][tt], acc[dt][tt], 0, 0, 0); PIN(acc[dt][tt]); SB(); }
    };
    sbar();
    STAMP(11); CSTAMP(20);
    ld(0, pa0, pb0, 0);
    lptr pa = pa0, pb = pb0;
    int nb = 0;
#pragma unroll 1
    for (int s = 0; s < 12; ++s) {
        ld(1, pa, pb, 1); mm(0);
        ld(0, pa, pb, 2); mm(1);
        ld(1, pa, pb, 3); mm(0);
        if (s < 11) {
            nb = nb + G2_SS == G2_NS * G2_SS ? 0 : nb + G2_SS;
            pa = pa0 + nb; pb = pb0 + nb;
            bar();
            ld(0, pa, pb, 0);
        }
        mm(1);
    }
    STAMP(12); CSTAMP(21);
    bar();
    const int g = lane >> 4, q = (lane & 15) >> 2, p = lane & 3;
#pragma unroll
    for (int dt = 0; dt < ND; ++dt)
#pragma unroll
        for (int tt = 0; tt < 2; ++tt) {
            const int row = 16 * (dl0 + dt) + 4 * g + q, seg = (2 * th + tt) ^ ((row >> 1) & 3);
            const u32x2 hv = tr_read(sm + G2_H + row * 128 + seg * 32 + p * 8);
            const unsigned o01 = pk_bf16(acc[dt][tt][0], acc[dt][tt][1]), o23 = pk_bf16(acc[dt][tt][2], acc[dt][tt][3]);
            const u32x2 r = {pk_bf16(bflo(hv[0]) + bflo(o01), bfhi(hv[0]) + bfhi(o01)), pk_bf16(bflo(hv[1]) + bflo(o23), bfhi(hv[1]) + bfhi(o23))};
            *(LAS u32x2*)(sm + (16 * (2 * th + tt) + (lane & 15)) * 176 + (16 * (dl0 + dt) + 4 * g) * 2) = r;
        }
}

extern "C" __global__ void s4d_geglu_block_end();
extern "C" __global__ __launch_bounds__(512, 2) void s4d_geglu_block(Params P PROF_ARG) {
    extern __shared__ __attribute__((aligned(16))) char smem[];
    lptr sm = (lptr)smem;
    STAMP(0);
    if (uni(threadIdx.x >> 6) == 7) {
        unsigned long long pc; unsigned off, len;
        asm volatile("s_getpc_b64 %0\n.Lpc_anchor_%=:\n\ts_mov_b32 %1, .Lpc_anchor_%=-s4d_geglu_block\n\ts_mov_b32 %2, s4d_geglu_block_end-s4d_geglu_block" : "=s"(pc), "=s"(off), "=s"(len));
        const unsigned o = (blockIdx.x >> 3) * 2048 + (threadIdx.x & 63) * 32;
        if (o < len) dma4((const void*)(pc - off), o, (unsigned)(size_t)sm + LDS_BYTES - 256);
    }
    const int tid = threadIdx.x, lane = tid & 63, w = uni(tid >> 6), b = blockIdx.x;
    const unsigned sm0 = (unsigned)(size_t)sm;
    char* ws = P.ws;
    unsigned* fl = (unsigned*)(ws + OFF_FL);
    unsigned tag;
    const int grp = b & 7, idx = b >> 3, grp0 = grp;
    const int rb = (grp & 1) * 8 + (idx & 7), cb = (grp >> 1) * 4 + (idx >> 3);
    const int tb = (grp >> 1) * 8 + (idx & 7), db = (grp & 1) * 4 + (idx >> 3);

    if (b < 160) tag = conv_wg(P, b, cb, sm PROF_FWD); else tag = spare_wg(P, b - 160, cb, sm PROF_FWD);

    if (w >= 4) g1_prefetch_w(ws, sm, cb, w & 3, lane);
    if (w == 4) wait_flags<32, 32>(fl + FL_HC + grp0 * 32 * 32, (tag << 3) + 5u, lane);
    if (w == 7) wait_flags<48, FL_W_S>(fl + grp0 * FL_REP + FL_WD, tag, lane);
    bar();
    STAMP(6);
    if (w >= 4) g1_loader(ws, sm, w & 3, lane, 128 * rb, cb, tag);
    else if (w & 1) g1_compute<1>(sm, ws, w >> 1, lane, 128 * rb, cb PROF_FWD); else g1_compute<0>(sm, ws, w >> 1, lane, 128 * rb, cb PROF_FWD);

    const int t0 = 64 * tb, d0 = 80 * db;
    if (w >= 4) {
        const int lw = w & 3;
        const char* ms = ws + OFF_MF + (size_t)(4 * tb + lw) * 48 * 1024;
        const char* wsrc = ws + OFF_WD + (size_t)lw * (40 * 1024) + (size_t)(5 * db) * 1024;
        const unsigned vl = lane * 16;
        auto stage_w = [&](int s) {
#pragma unroll
            for (int j = 0; j < 5; ++j) dma16s(wsrc + (size_t)(4 * s) * (40 * 1024) + j * 1024, vl, sm0 + (s % G2_NS) * G2_SS + G2_W + (5 * lw + j) * 1024);
        };
        auto stage_m = [&](int s) {
#pragma unroll
            for (int j = 0; j < 4; ++j) dma16s(ms + (size_t)(4 * s + j) * 1024, vl, sm0 + (s % G2_NS) * G2_SS + (4 * lw + j) * 1024);
        };
        stage_w(0); stage_w(1); stage_w(2);
#pragma unroll
        for (int j = 0; j < 3; ++j) {
            const int i = lw + 4 * j < 10 ? lw + 4 * j : lw;
            const int o = i * 1024 + lane * 16, row = o >> 7, c = ((o >> 4) & 7) ^ (((row >> 1) & 3) << 1);
            dma16s(ws + OFF_HT, (unsigned)((d0 + row) * (T * 2) + t0 * 2 + c * 16), sm0 + G2_H + i * 1024);
        }
        sbar();
        if (lw == 0) wait_flags<16, FL_M_S>(fl + FL_M + 16 * (tb >> 1) * FL_M_S, tag, lane);
        sbar();
        STAMPL(10);
        stage_m(0); stage_m(1); stage_m(2);
        wait_vm<8>();
        sbar();
#pragma unroll
        for (int s = 0; s < 11; ++s) {
            if (s == 0) wait_vm<4>(); else if (s <= 9) wait_vm<9>(); else wait_vm<0>();
            sbar();
            if (s + 3 < 12) { stage_m(s + 3); stage_w(s + 3); }
        }
        sbar();
    } else {
        STAMP(28);
        wait_vm<0>();
        sbar();
        if (tid == 0) set_flag(fl + FL_M + (16 * rb + cb) * FL_M_S, tag);
        STAMP(9);
        sbar();
        const int th = w & 1;
        if (w >> 1) g2_compute<2>(sm, lane, th, 3, t0, d0, P.out PROF_FWD); else g2_compute<3>(sm, lane, th, 0, t0, d0, P.out PROF_FWD);
    }
    bar();
    for (int ch = tid; ch < 640; ch += 512) {
        const int row = ch / 10, c = ch % 10;
        *(u32x4*)(P.out + (size_t)(t0 + row) * D + d0 + c * 8) = *(LAS u32x4*)(sm + row * 176 + c * 16);
    }
    if (b == 0 && tid == 0) fl[FL_E] = tag;
    STAMP(15);
}

extern "C" __global__ void s4d_geglu_block_end() {}

extern "C" void kernel_launch(void* const* d_in, const int* in_sizes, int n_in, void* d_out, int out_size, void* d_ws, size_t ws_size, hipStream_t stream) {
    Params P;
    P.x = (const bf16_t*)d_in[0]; P.logA = (const bf16_t*)d_in[1]; P.Aim = (const bf16_t*)d_in[2]; P.Cre = (const bf16_t*)d_in[3]; P.Cim = (const bf16_t*)d_in[4];
    P.logdt = (const bf16_t*)d_in[5]; P.Dskip = (const bf16_t*)d_in[6]; P.wg = (const bf16_t*)d_in[8]; P.wu = (const bf16_t*)d_in[9]; P.wd = (const bf16_t*)d_in[10];
    P.ws = (char*)d_ws; P.out = (bf16_t*)d_out;
    (void)hipFuncSetAttribute((const void*)s4d_geglu_block, hipFuncAttributeMaxDynamicSharedMemorySize, LDS_BYTES);
    s4d_geglu_block<<<256, 512, LDS_BYTES, stream>>>(P);
}
```

```cpp
#include <hip/hip_runtime.h>
#include <stdint.h>

typedef unsigned short bf16_t;
typedef short s16x4 __attribute__((ext_vector_type(4)));
typedef __bf16 bf16x8 __attribute__((ext_vector_type(8)));
typedef __bf16 bf16x2 __attribute__((ext_vector_type(2)));
typedef _Float16 h16x8 __attribute__((ext_vector_type(8)));
typedef _Float16 h16x2 __attribute__((ext_vector_type(2)));
typedef float f32x2 __attribute__((ext_vector_type(2)));
typedef float f32x4 __attribute__((ext_vector_type(4)));
typedef float f32x16 __attribute__((ext_vector_type(16)));
typedef unsigned u32x2 __attribute__((ext_vector_type(2)));
typedef unsigned u32x4 __attribute__((ext_vector_type(4)));
#define LAS __attribute__((address_space(3)))
typedef LAS char* lptr;

constexpr int T = 2048, D = 640, NM = 192, F = 1536;
constexpr size_t OFF_S1 = 0;
constexpr size_t OFF_HT = OFF_S1 + (size_t)T * 8;
constexpr size_t OFF_CS = OFF_HT + (size_t)T * D * 2;
constexpr size_t OFF_MF = OFF_CS + 65536;
constexpr size_t OFF_WD = OFF_MF + (size_t)T * F * 2;
constexpr size_t OFF_WG = OFF_WD + (size_t)F * D * 2;
constexpr size_t OFF_ST = OFF_WG + (size_t)2 * F * D * 2;
constexpr size_t OFF_FL = OFF_ST + (size_t)T * 8;
constexpr size_t OFF_PROF = OFF_FL + 196608;
constexpr int FL_E = 0;
constexpr int FL_REP = 4096;
constexpr int FL_LN = FL_REP, FL_LN_S = 16;
constexpr int FL_WG = FL_LN + 1024, FL_WD = FL_WG + 96 * 8, FL_W_S = 8;
constexpr int FL_M = 64, FL_M_S = 8;
constexpr int FL_HC = 9 * FL_REP;
static_assert(FL_WD + 48 * FL_W_S <= 2 * FL_REP && FL_M + 256 * FL_M_S <= FL_REP, "flag map");

#define STAMP(i)
#define STAMPL(i)
#define CSTAMP(i)
#define PROF_ARG
#define PROF_PASS
#define PROF_FWD

__device__ __forceinline__ float bf2f(unsigned v) { return __uint_as_float(v << 16); }
__device__ __forceinline__ float bflo(unsigned v) { return __uint_as_float(v << 16); }
__device__ __forceinline__ float bfhi(unsigned v) { return __uint_as_float(v & 0xffff0000u); }
__device__ __forceinline__ unsigned pk_bf16(float lo, float hi) { f32x2 v = {lo, hi}; return __builtin_bit_cast(unsigned, __builtin_convertvector(v, bf16x2)); }
__device__ __forceinline__ float rbf(float f) { return bflo(pk_bf16(f, f)); }
__device__ __forceinline__ unsigned pk_f16(f32x2 v) { return __builtin_bit_cast(unsigned, __builtin_convertvector(v, h16x2)); }
template <int CTRL> __device__ __forceinline__ float dppf(float x) { return __int_as_float(__builtin_amdgcn_update_dpp(0, __float_as_int(x), CTRL, 0xF, 0xF, true)); }
__device__ __forceinline__ float row16_sum(float x) { x += dppf<0xB1>(x); x += dppf<0x4E>(x); x += dppf<0x141>(x); x += dppf<0x140>(x); return x; }
__device__ __forceinline__ float row16_max(float x) { x = fmaxf(x, dppf<0xB1>(x)); x = fmaxf(x, dppf<0x4E>(x)); x = fmaxf(x, dppf<0x141>(x)); x = fmaxf(x, dppf<0x140>(x)); return x; }
__device__ __forceinline__ float xsum16_32(float x) {
    const unsigned u = __float_as_uint(x);
    const auto r = __builtin_amdgcn_permlane16_swap(u, u, false, false);
    const unsigned v = __float_as_uint(__uint_as_float(r[0]) + __uint_as_float(r[1]));
    const auto q = __builtin_amdgcn_permlane32_swap(v, v, false, false);
    return __uint_as_float(q[0]) + __uint_as_float(q[1]);
}
__device__ __forceinline__ u32x2 tr_read(lptr p) { return __builtin_bit_cast(u32x2, __builtin_amdgcn_ds_read_tr16_b64_v4i16((LAS s16x4*)p)); }
__device__ __forceinline__ u32x4 tr_read2(lptr p, int off2) { const u32x2 r1 = tr_read(p), r2 = tr_read(p + off2); return (u32x4){r1[0], r1[1], r2[0], r2[1]}; }
__device__ __forceinline__ int uni(int v) { return __builtin_amdgcn_readfirstlane(v); }

__device__ __forceinline__ void dma16(const void* sbase, unsigned voff, unsigned ldsaddr) {
    asm volatile("s_mov_b32 m0, %2\n\ts_nop 0\n\tglobal_load_lds_dwordx4 %0, %1" :: "v"(voff), "s"(sbase), "s"(ldsaddr) : "memory");
}
__device__ __forceinline__ void dma4(const void* sbase, unsigned voff, unsigned ldsaddr) {
    asm volatile("s_mov_b32 m0, %2\n\ts_nop 0\n\tglobal_load_lds_dword %0, %1" :: "v"(voff), "s"(sbase), "s"(ldsaddr) : "memory");
}
template <int N> __device__ __forceinline__ void wait_vm() { asm volatile("s_waitcnt vmcnt(%0)" :: "n"(N) : "memory"); }
template <int N> __device__ __forceinline__ void wait_vm_rt(int n) {
    if (n >= N) wait_vm<N>(); else if constexpr (N > 0) wait_vm_rt<N - 1>(n);
}
__device__ __forceinline__ void bar() { asm volatile("s_waitcnt lgkmcnt(0)\n\ts_barrier" ::: "memory"); }
__device__ __forceinline__ void sbar() { asm volatile("s_barrier" ::: "memory"); }
#define SB() __builtin_amdgcn_sched_barrier(0)
#define PIN(x) asm volatile("" : "+v"(x))
__device__ __forceinline__ void st_dev(void* p, u32x4 v) { asm volatile("global_store_dwordx4 %0, %1, off sc1\n\ts_nop 1" :: "v"(p), "v"(v) : "memory"); }
__device__ __forceinline__ void st_dev_o(const void* sbase, unsigned voff, u32x4 v) { asm volatile("global_store_dwordx4 %0, %1, %2 sc1\n\ts_nop 1" :: "v"(voff), "v"(v), "s"(sbase) : "memory"); }
__device__ __forceinline__ void st_dev4(void* p, float v) { asm volatile("global_store_dword %0, %1, off sc1\n\ts_nop 1" :: "v"(p), "v"(v) : "memory"); }
__device__ __forceinline__ void dma16s(const void* sbase, unsigned voff, unsigned ldsaddr) {
    asm volatile("s_mov_b32 m0, %2\n\ts_nop 0\n\tglobal_load_lds_dwordx4 %0, %1 sc1" :: "v"(voff), "s"(sbase), "s"(ldsaddr) : "memory");
}
__device__ __forceinline__ void dma4s(const void* sbase, unsigned voff, unsigned ldsaddr) {
    asm volatile("s_mov_b32 m0, %2\n\ts_nop 0\n\tglobal_load_lds_dword %0, %1 sc1" :: "v"(voff), "s"(sbase), "s"(ldsaddr) : "memory");
}
__device__ __forceinline__ void set_flag(unsigned* f, unsigned tag) { __hip_atomic_store(f, tag, __ATOMIC_RELAXED, __HIP_MEMORY_SCOPE_AGENT); }
__device__ __forceinline__ void set_flag_rep(unsigned* f, unsigned tag, int lane) { __hip_atomic_store(f + lane * FL_REP, tag, __ATOMIC_RELAXED, __HIP_MEMORY_SCOPE_AGENT); }
template <int N, int S>
__device__ __forceinline__ void wait_flags(unsigned* f, unsigned tag, int lane) {
    for (unsigned spins = 0; spins < (1u << 18); ++spins) {
        bool ok = true;
        if (lane < N) ok = __hip_atomic_load(f + lane * S, __ATOMIC_RELAXED, __HIP_MEMORY_SCOPE_AGENT) == tag;
        if (N > 64 && lane + 64 < N) ok &= __hip_atomic_load(f + (64 + lane) * S, __ATOMIC_RELAXED, __HIP_MEMORY_SCOPE_AGENT) == tag;
        if (N > 128 && lane + 128 < N) ok &= __hip_atomic_load(f + (128 + lane) * S, __ATOMIC_RELAXED, __HIP_MEMORY_SCOPE_AGENT) == tag;
        if (__all(ok)) break;
        __builtin_amdgcn_s_sleep(1);
    }
    asm volatile("" ::: "memory");
}

__device__ __forceinline__ unsigned epoch_tag(const char* ws) {
    unsigned e;
    asm volatile("s_load_dword %0, %1, 0x0\n\ts_waitcnt lgkmcnt(0)" : "=s"(e) : "s"(ws + OFF_FL + FL_E * 4) : "memory");
    return e + 1u;
}

constexpr int G1_NS = 7, G1_SS = 20480, G1_W = 8192;
constexpr int G2_NS = 4, G2_SS = 36 * 1024, G2_W = 16 * 1024;
constexpr int G2_H = G2_NS * G2_SS;
constexpr int G1_CS = G2_H + 10240;
constexpr int G1_ST = G1_CS + 3840;
constexpr int G1_SP = G1_ST + 1024;
constexpr int LDS_BYTES = 163840;
static_assert(G1_SP + 512 <= LDS_BYTES, "LDS map");
constexpr int KG_ROW = 272;
constexpr int KG_WAVE = 57 * KG_ROW;
constexpr int CV_KCH = 8320;
__host__ __device__ constexpr int cv_k(int c) { return (4 + c) * KG_WAVE + 3072; }
__host__ __device__ constexpr int cv_kc(int c) { return cv_k(c) + CV_KCH; }
static_assert(3072 + CV_KCH + 1296 <= KG_WAVE, "per-channel area");
constexpr int CV_X = 8 * KG_WAVE + 7 * KG_ROW + 16;
constexpr int CV_U = CV_X + 128 * 128;
constexpr int CV_M = CV_U + 144 * 128;
static_assert(CV_X % 128 == 0 || true, "");
static_assert(CV_M + 32 + 256 <= 163840 && CV_X % 16 == 0, "LDS map");
constexpr int LN_ROW = 1344;

__device__ __forceinline__ unsigned ln1_unit(int j, const bf16_t* __restrict__ x, char* __restrict__ ws PROF_ARG) {
    const int tid = threadIdx.x, r = tid >> 4, l16 = tid & 15, lane = tid & 63, w = uni(tid >> 6);
    const int t = 32 * j + r;
    u32x4 v[5];
#pragma unroll
    for (int i = 0; i < 5; ++i) v[i] = *(const u32x4*)(x + (size_t)t * D + (l16 + 16 * i) * 8);
    const unsigned tag = epoch_tag(ws);
    f32x2 f[20];
    f32x2 s2 = {0.f, 0.f};
#pragma unroll
    for (int i = 0; i < 5; ++i)
#pragma unroll
        for (int e = 0; e < 4; ++e) { f[4 * i + e] = (f32x2){bflo(v[i][e]), bfhi(v[i][e])}; s2 += f[4 * i + e]; }
    const float mean = row16_sum(s2[0] + s2[1]) * (1.0f / (float)D);
    f32x2 q2 = {0.f, 0.f};
#pragma unroll
    for (int i = 0; i < 20; ++i) { const f32x2 dv = f[i] - mean; q2 += dv * dv; }
    const float var = row16_sum(q2[0] + q2[1]) * (1.0f / (float)D);
    const float rs = __builtin_amdgcn_rcpf(rbf(sqrtf(rbf(rbf(var) + 1e-5f)))), nm = -rbf(mean) * rs;
    u32x4 o;
#pragma unroll
    for (int i = 0; i < 4; ++i) {
        const unsigned a = (unsigned)__builtin_amdgcn_readlane((int)__float_as_uint(rs), 16 * i), c = (unsigned)__builtin_amdgcn_readlane((int)__float_as_uint(nm), 16 * i);
        o[i] = lane == 0 ? a : c;
    }
    if (lane < 2) st_dev(ws + OFF_S1 + (size_t)lane * (T * 4) + (32 * j + 4 * w) * 4, o);
    if (tid < 4) set_flag((unsigned*)(ws + OFF_FL) + FL_HC + (4 * j + tid) * 32, tag << 3);
    STAMP(24);
    return tag;
}

__device__ __forceinline__ void wgtile_unit(int b, const bf16_t* __restrict__ wg, const bf16_t* __restrict__ wu, char* __restrict__ ws, lptr sm) {
    const int tid = threadIdx.x, lane = tid & 63, w = uni(tid >> 6);
    const int j = b >> 2, kq = b & 3;
    const int c16 = tid & 15, r0 = tid >> 4;
    u32x4 v[5];
    const bf16_t* src = (c16 < 8 ? wg : wu) + (size_t)(160 * kq + r0) * F + 64 * j + (c16 & 7) * 8;
#pragma unroll
    for (int i = 0; i < 5; ++i) v[i] = *(const u32x4*)(src + (size_t)(32 * i) * F);
    f32x2 a[4] = {};
#pragma unroll
    for (int i = 0; i < 5; ++i) {
        const int k = 32 * i + r0;
        *(LAS u32x4*)(sm + k * 256 + (((c16 >> 1) ^ (k & 3)) * 32) + (c16 & 1) * 16) = v[i];
#pragma unroll
        for (int e = 0; e < 4; ++e) a[e] += (f32x2){bflo(v[i][e]), bfhi(v[i][e])};
    }
#pragma unroll
    for (int e = 0; e < 4; ++e)
#pragma unroll
        for (int hf = 0; hf < 2; ++hf) {
            const float s = xsum16_32(a[e][hf]);
            if (lane < 16) *(LAS float*)(sm + 40960 + (w * 128 + c16 * 8 + 2 * e + hf) * 4) = s;
        }
    bar();
    const int g = lane >> 4, q = (lane & 15) >> 2, p = lane & 3;
    u32x4 o[5];
#pragma unroll
    for (int i = 0; i < 5; ++i) {
        const int item = w + 8 * i, ftl = item & 3, sub = item >> 2;
        const int k = 16 * sub + 4 * (g >> 1) + q;
        o[i] = tr_read2(sm + k * 256 + ((((g & 1) * 4 + ftl) ^ q) * 32) + p * 8, 8 * 256);
    }
#pragma unroll
    for (int i = 0; i < 5; ++i) {
        const int item = w + 8 * i, ftl = item & 3, sub = item >> 2;
        const int ftg = 4 * j + ftl, cb = ftg / 6, ft = ftg % 6;
        st_dev(ws + OFF_WG + ((((size_t)cb * 40 + 10 * kq + sub) * 6 + ft) * 64 + lane) * 16, o[i]);
    }
    if (tid < 128) {
        float s = 0.f;
#pragma unroll
        for (int i = 0; i < 8; ++i) s += *(LAS float*)(sm + 40960 + (i * 128 + tid) * 4);
        st_dev4(ws + OFF_CS + (size_t)(kq * 2 * F + (tid >> 6) * F + 64 * j + (tid & 63)) * 4, s);
    }
}

__device__ __forceinline__ void wdtile_unit(int fb, const bf16_t* __restrict__ wd, char* __restrict__ ws, lptr sm) {
    const int tid = threadIdx.x, lane = tid & 63, w = uni(tid >> 6);
    u32x4 v[5];
#pragma unroll
    for (int i = 0; i < 5; ++i) { const int ch = tid + 512 * i, row = ch / 80, c = ch % 80; v[i] = *(const u32x4*)(wd + (size_t)(32 * fb + row) * D + c * 8); }
#pragma unroll
    for (int i = 0; i < 5; ++i) { const int ch = tid + 512 * i, row = ch / 80, c = ch % 80; *(LAS u32x4*)(sm + row * LN_ROW + c * 16) = v[i]; }
    bar();
    const int g = lane >> 4, q = (lane & 15) >> 2, p = lane & 3;
    u32x4 o[5];
#pragma unroll
    for (int i = 0; i < 5; ++i) o[i] = tr_read2(sm + (16 * (g >> 1) + 4 * (g & 1) + q) * LN_ROW + (16 * (w + 8 * i) + 4 * p) * 2, 8 * LN_ROW);
#pragma unroll
    for (int i = 0; i < 5; ++i) st_dev(ws + OFF_WD + (((size_t)fb * 40 + w + 8 * i) * 64 + lane) * 16, o[i]);
}

__device__ __forceinline__ f32x2 cmul(f32x2 a, f32x2 b) {
    const f32x2 t = (f32x2){a[0], a[0]} * b;
    const f32x2 bs = {-b[1], b[0]};
    return __builtin_elementwise_fma((f32x2){a[1], a[1]}, bs, t);
}
__device__ __forceinline__ unsigned cmul_h(unsigned a, unsigned b) {
    unsigned t, r;
    asm("v_pk_mul_f16 %0, %1, %2 op_sel:[0,0] op_sel_hi:[0,1]" : "=v"(t) : "v"(a), "v"(b));
    asm("v_pk_fma_f16 %0, %1, %2, %3 op_sel:[1,1,0] op_sel_hi:[1,0,1] neg_lo:[0,1,0]" : "=v"(r) : "v"(a), "v"(b), "v"(t));
    return r;
}
__device__ __forceinline__ f32x2 cexp_p(float arl, float air, float p) {
    const float mag = __builtin_amdgcn_exp2f(arl * p);
    float rev = air * p; rev -= rintf(rev);
    return (f32x2){mag * __builtin_amdgcn_cosf(rev), mag * __builtin_amdgcn_sinf(rev)};
}
struct KgenIn { unsigned ldt, la, ai, cr[3], ci[3]; };
__device__ __forceinline__ void kgen_load(KgenIn& k, int d, int third, int lane, const bf16_t* __restrict__ logA, const bf16_t* __restrict__ Aim,
                                          const bf16_t* __restrict__ Cre, const bf16_t* __restrict__ Cim, const bf16_t* __restrict__ logdt) {
    k.ldt = logdt[d]; k.la = logA[d * NM + 64 * third + lane]; k.ai = Aim[d * NM + 64 * third + lane];
#pragma unroll
    for (int i = 0; i < 3; ++i) { k.cr[i] = Cre[d * NM + 64 * i + lane]; k.ci[i] = Cim[d * NM + 64 * i + lane]; }
}
__device__ __forceinline__ void kgen_task(const KgenIn& k, int third, int lane, lptr tab, f32x4 (&acc)[3], float& unscale) {
    const float dt = rbf(expf(bf2f(k.ldt)));
    float mx = 0.f;
#pragma unroll
    for (int i = 0; i < 3; ++i) mx = fmaxf(mx, fmaxf(fabsf(bf2f(k.cr[i])), fabsf(bf2f(k.ci[i]))));
    mx = row16_max(mx);
    {
        const auto r1 = __builtin_amdgcn_permlane16_swap(__float_as_uint(mx), __float_as_uint(mx), false, false);
        mx = fmaxf(__uint_as_float(r1[0]), __uint_as_float(r1[1]));
        const auto r2 = __builtin_amdgcn_permlane32_swap(__float_as_uint(mx), __float_as_uint(mx), false, false);
        mx = fmaxf(__uint_as_float(r2[0]), __uint_as_float(r2[1]));
    }
    mx *= 2.f * dt;
    int e = (int)((__float_as_uint(mx) >> 23) & 0xff) - 126;
    e = e < -100 ? -100 : (e > 100 ? 100 : e);
    const float scale = __uint_as_float((unsigned)(127 - e) << 23);
    unscale = __uint_as_float((unsigned)(127 + e) << 23);
    const float are = -rbf(__builtin_amdgcn_exp2f(bf2f(k.la) * 1.4426950408889634f)) * dt, aim = bf2f(k.ai) * dt;
    const float crw = bf2f(third == 0 ? k.cr[0] : (third == 1 ? k.cr[1] : k.cr[2])) * dt, ciw = bf2f(third == 0 ? k.ci[0] : (third == 1 ? k.ci[1] : k.ci[2])) * dt;
    const float arl = are * 1.4426950408889634f, air = aim * 0.15915494309189535f;
    lptr base = tab + 4 * lane;
#define PUT(row, z) *(LAS unsigned*)(base + (row) * KG_ROW) = pk_f16(z)
#define PUTH(row, z) *(LAS unsigned*)(base + (row) * KG_ROW) = (z)
    const f32x2 w1 = cexp_p(arl, air, 1.f), w8 = cexp_p(arl, air, 8.f), H1 = cexp_p(arl, air, 128.f);
    f32x2 qv = {2.f * scale * crw, -2.f * scale * ciw};
    const f32x2 w8c = {w8[0], -w8[1]};
    f32x2 Ec = w1, Hc = H1;
    unsigned Eh[8], Hh[8];
    Eh[1] = pk_f16(w1); Hh[1] = pk_f16(H1);
    PUT(0, qv); PUTH(16, 0x00003c00u); PUTH(17, Eh[1]); PUTH(24, Hh[1]);
#pragma unroll
    for (int i = 1; i < 16; ++i) {
        qv = cmul(qv, w8c);
        Hc = cmul(Hc, H1);
        if (i < 7) Ec = cmul(Ec, w1);
        PUT(i, qv);
        if (i < 7) { Eh[i + 1] = pk_f16(Ec); PUTH(17 + i, Eh[i + 1]); }
        const int hi = i + 1;
        if (hi < 8) { Hh[hi] = pk_f16(Hc); PUTH(hi < 4 ? 32 + 4 * (hi - 2) : 40 + 2 * (hi - 4), Hh[hi]); } else PUT(48 + (hi - 8), Hc);
        if (i >= 7 && i < 14) PUTH(25 + (i - 7), cmul_h(Eh[i - 6], Hh[1]));
        if (i >= 7 && i < 10) PUTH(33 + (i - 7), cmul_h(Eh[2 * (i - 6)], Hh[2]));
        if (i >= 10 && i < 13) PUTH(37 + (i - 10), cmul_h(Eh[2 * (i - 9)], Hh[3]));
        if (i >= 11 && i < 15) PUTH(41 + 2 * (i - 11), cmul_h(Hh[4 + (i - 11)], Eh[4]));
    }
#undef PUTH
#undef PUT
    asm volatile("" ::: "memory");
    lptr rb = tab + (lane & 15) * KG_ROW + (lane >> 4) * 16;
#pragma unroll
    for (int mt = 0; mt < 3; ++mt) acc[mt] = (f32x4){};
#pragma unroll
    for (int ks = 0; ks < 4; ++ks) {
        const h16x8 bq = *(LAS h16x8*)(rb + ks * 64);
#pragma unroll
        for (int mt = 0; mt < 3; ++mt) {
            const h16x8 ar = *(LAS h16x8*)(rb + (16 + 16 * mt) * KG_ROW + ks * 64);
            acc[mt] = __builtin_amdgcn_mfma_f32_16x16x32_f16(ar, bq, acc[mt], 0, 0, 0);
        }
    }
#pragma unroll
    for (int mt = 0; mt < 3; ++mt) PIN(acc[mt]);
    asm volatile("s_waitcnt lgkmcnt(0)" ::: "memory");
}

__device__ __forceinline__ void g1_prefetch_w(char* __restrict__ ws, lptr sm, int cb, int lw, int lane) {
    const unsigned sm0 = (unsigned)(size_t)sm;
    const char* wsrc = ws + OFF_WG + (size_t)cb * 40 * 6144 + 3 * lw * 1024;
#pragma unroll
    for (int s = 0; s < 4; ++s)
#pragma unroll
        for (int i = 0; i < 3; ++i) dma16s(wsrc + (size_t)s * 12288 + i * 1024, lane * 16, sm0 + s * G1_SS + G1_W + (3 * lw + i) * 1024);
    if (lw < 3) {
        const int f0 = 96 * cb;
        const unsigned co = (unsigned)(((lw == 0 ? f0 + lane : (lw == 1 ? (lane < 32 ? f0 + 64 + lane : F + f0 + lane - 32) : F + f0 + 32 + lane))) * 4);
#pragma unroll
        for (int kq = 0; kq < 4; ++kq) dma4s(ws + OFF_CS + kq * 2 * F * 4, co, sm0 + G1_CS + kq * 768 + lw * 256);
    }
}

struct KTab { unsigned short v[2048]; };
constexpr int ci_of(int l) {
    if (l < 0) return 641;
    if (l < 256) return l;
    const int s = l < 512 ? 1 : (l < 1024 ? 2 : 3);
    return ((l + (1 << (s - 1)) - 1 + ((l >> s) & 1)) >> s) + 128 * s;
}
constexpr KTab make_tab() { KTab t{}; for (int y = 0; y < 2048; ++y) t.v[y] = (unsigned short)(2 * ci_of(2047 - y)); return t; }
__device__ const __attribute__((aligned(16))) KTab g_ktab = make_tab();

struct Params {
    const bf16_t *x, *logA, *Aim, *Cre, *Cim, *logdt, *Dskip, *wg, *wu, *wd;
    char* ws;
    bf16_t* out;
};

__device__ __forceinline__ unsigned conv_wg(const Params& P, int b, int cb, lptr sm PROF_ARG) {
    const int tid = threadIdx.x, lane = tid & 63, w = uni(tid >> 6);
    char* ws = P.ws;
    const int d0 = 4 * ((b & 7) * 20 + (b >> 3));
    const unsigned sm0 = (unsigned)(size_t)sm;
    unsigned* fl = (unsigned*)(ws + OFF_FL);
    KgenIn k1, k2;
    u32x4 tb[4];
    u32x2 xg[8];
    kgen_load(k1, d0 + w / 3, w % 3, lane, P.logA, P.Aim, P.Cre, P.Cim, P.logdt);
    const unsigned tag = epoch_tag(ws);
    if (w < 4) {
        const int t2 = 8 + w;
        kgen_load(k2, d0 + t2 / 3, t2 % 3, lane, P.logA, P.Aim, P.Cre, P.Cim, P.logdt);
#pragma unroll
        for (int i = 0; i < 4; ++i) tb[i] = *(const u32x4*)(&g_ktab.v[8 * (lane + 64 * i)]);
    }
    const unsigned dsk_raw = ((const unsigned*)P.Dskip)[(d0 + (w & 3)) >> 1];
    asm volatile("" : "+v"(k1.ldt), "+v"(k1.la), "+v"(k1.ai), "+v"(k1.cr[0]), "+v"(k1.cr[1]), "+v"(k1.cr[2]), "+v"(k1.ci[0]), "+v"(k1.ci[1]), "+v"(k1.ci[2]) :: "memory");
    STAMP(1);
    const lptr tab = sm + w * KG_WAVE;
    LAS unsigned* cnt = (LAS unsigned*)(sm + CV_M + 16);
    if (w == 7 && lane == 0) *cnt = 0;
    bar();
    if (w >= 4) {
#pragma unroll
        for (int i = 0; i < 8; ++i) xg[i] = *(const u32x2*)(P.x + (size_t)(8 * (tid - 256) + i) * D + d0);
        asm volatile("" ::: "memory");
    }
    f32x4 a1[3], a2[3];
    {
        int third = w % 3, chn = w / 3;
#pragma unroll 1
        for (int rnd = 0; rnd < 2; ++rnd) {
            float un;
            kgen_task(k1, third, lane, tab, a2, un);
            if (third == 0 && lane == 0) *(LAS float*)(sm + CV_M + chn * 4) = un;
            if (rnd == 1 || w >= 4) break;
#pragma unroll
            for (int mt = 0; mt < 3; ++mt) a1[mt] = a2[mt];
            k1 = k2; third = (8 + w) % 3; chn = (8 + w) / 3;
        }
    }
    if (w < 4) {
#pragma unroll
        for (int mt = 0; mt < 3; ++mt) { *(LAS f32x4*)(tab + (mt * 64 + lane) * 16) = a1[mt]; *(LAS f32x4*)(tab + 3072 + (mt * 64 + lane) * 16) = a2[mt]; }
        asm volatile("s_waitcnt lgkmcnt(0)" ::: "memory");
        if (lane == 0) __hip_atomic_fetch_add(cnt, 1u, __ATOMIC_RELAXED, __HIP_MEMORY_SCOPE_WORKGROUP);
        while (__hip_atomic_load(cnt, __ATOMIC_RELAXED, __HIP_MEMORY_SCOPE_WORKGROUP) < 8u) __builtin_amdgcn_s_sleep(0);
        asm volatile("" ::: "memory");
        STAMP(2);
        f32x4 ks[3];
        lptr pt[3];
#pragma unroll
        for (int j = 0; j < 3; ++j) { const int t = 3 * w + j; pt[j] = sm + (t & 7) * KG_WAVE + (t >> 3) * 3072 + lane * 16; }
        const float un = *(LAS float*)(sm + CV_M + w * 4);
#pragma unroll
        for (int mt = 0; mt < 3; ++mt) ks[mt] = ((*(LAS f32x4*)(pt[0] + mt * 1024) + *(LAS f32x4*)(pt[1] + mt * 1024)) + *(LAS f32x4*)(pt[2] + mt * 1024)) * un;
        lptr kc = sm + cv_kc(0) + w * KG_WAVE;
        const int hq = lane >> 4, col = lane & 15;
        const u32x2 p0 = {pk_bf16(ks[0][0], ks[0][1]), pk_bf16(ks[0][2], ks[0][3])}, p1 = {pk_bf16(ks[1][0], ks[1][1]), pk_bf16(ks[1][2], ks[1][3])};
        const u32x2 p2 = {pk_bf16(ks[2][0], ks[2][1]), pk_bf16(ks[2][2], ks[2][3])};
        *(LAS u32x2*)(kc + 2 * (128 * (hq >> 1) + 4 * (hq & 1) + 8 * col)) = p0;
        if (hq < 2) *(LAS u32x2*)(kc + 2 * (256 + 64 * hq + 4 * col)) = p1;
        else { *(LAS unsigned*)(kc + 2 * (384 + 64 * (hq - 2) + 2 * col)) = p1[0]; *(LAS unsigned*)(kc + 2 * (416 + 64 * (hq - 2) + 2 * col)) = p1[1]; }
        if (hq < 2) {
            lptr k2p = kc + 2 * (512 + 64 * hq + col);
            *(LAS unsigned short*)k2p = (unsigned short)p2[0]; *(LAS unsigned short*)(k2p + 32) = (unsigned short)(p2[0] >> 16);
            *(LAS unsigned short*)(k2p + 64) = (unsigned short)p2[1]; *(LAS unsigned short*)(k2p + 96) = (unsigned short)(p2[1] >> 16);
        } else if (lane == 32) *(LAS unsigned short*)(kc + 2 * 640) = (unsigned short)p2[0];
        else if (lane >= 48 && lane < 55) *(LAS unsigned short*)(kc + 2 * (641 + lane - 48)) = 0;
        asm volatile("" ::: "memory");
        STAMP(13);
        u32x4 o[4];
#pragma unroll
        for (int i = 0; i < 4; ++i)
#pragma unroll
            for (int k = 0; k < 4; ++k) {
                const unsigned tk = tb[i][k];
                const unsigned lo = *(LAS unsigned short*)(kc + (tk & 0xffffu)), hi = *(LAS unsigned short*)(kc + (tk >> 16));
                o[i][k] = lo | (hi << 16);
            }
        lptr dst = sm + cv_k(0) + w * KG_WAVE;
#pragma unroll
        for (int i = 0; i < 4; ++i) {
            *(LAS u32x4*)(dst + (lane + 64 * i) * 16) = o[i];
            unsigned nx = (unsigned)__builtin_amdgcn_update_dpp(0, (int)o[i][0], 0x130, 0xF, 0xF, false);
            const unsigned wrap = i < 3 ? (unsigned)__builtin_amdgcn_readlane((int)o[i < 3 ? i + 1 : 3][0], 0) : 0u;
            nx = lane == 63 ? wrap : nx;
            const u32x4 od = {__builtin_amdgcn_alignbit(o[i][1], o[i][0], 16), __builtin_amdgcn_alignbit(o[i][2], o[i][1], 16),
                              __builtin_amdgcn_alignbit(o[i][3], o[i][2], 16), __builtin_amdgcn_alignbit(nx, o[i][3], 16)};
            *(LAS u32x4*)(dst + 4160 + (lane + 64 * i) * 16) = od;
        }
        if (lane < 8) *(LAS u32x4*)(dst + (lane >> 2) * 4160 + 4096 + (lane & 3) * 16) = (u32x4){0u, 0u, 0u, 0u};
        STAMP(14);
    } else {
#pragma unroll
        for (int mt = 0; mt < 3; ++mt) *(LAS f32x4*)(tab + (mt * 64 + lane) * 16) = a2[mt];
        asm volatile("s_waitcnt lgkmcnt(0)" ::: "memory");
        if (lane == 0) __hip_atomic_fetch_add(cnt, 1u, __ATOMIC_RELAXED, __HIP_MEMORY_SCOPE_WORKGROUP);
        const int gm = tid - 256, ln = gm >> 1, sw = ((ln >> 1) & 3) << 1;
        u32x4 xc[4];
#pragma unroll
        for (int ch = 0; ch < 4; ++ch) {
#pragma unroll
            for (int k = 0; k < 4; ++k) xc[ch][k] = __builtin_amdgcn_perm(xg[2 * k + 1][ch >> 1], xg[2 * k][ch >> 1], (ch & 1) ? 0x07060302u : 0x05040100u);
            *(LAS u32x4*)(sm + CV_X + ln * 128 + 16 * ((2 * ch + (gm & 1)) ^ sw)) = xc[ch];
        }
        if (gm < 128) *(LAS u32x4*)(sm + CV_U + gm * 16) = (u32x4){0u, 0u, 0u, 0u};
        wait_flags<16, FL_LN_S>(fl + (b & 7) * FL_REP + FL_LN + 16 * (w - 4) * FL_LN_S, tag, lane);
        STAMPL(25);
        f32x4 r0, r1, n0, n1;
        asm volatile("global_load_dwordx4 %0, %4, %5 sc1\n\tglobal_load_dwordx4 %1, %4, %5 offset:16 sc1\n\tglobal_load_dwordx4 %2, %4, %6 sc1\n\tglobal_load_dwordx4 %3, %4, %6 offset:16 sc1\n\ts_waitcnt vmcnt(0)"
                     : "=&v"(r0), "=&v"(r1), "=&v"(n0), "=&v"(n1) : "v"(gm * 32), "s"(ws + OFF_S1), "s"(ws + OFF_S1 + T * 4) : "memory");
#pragma unroll
        for (int ch = 0; ch < 4; ++ch) {
            u32x4 u;
#pragma unroll
            for (int k = 0; k < 4; ++k) {
                const f32x2 rs = k < 2 ? (f32x2){r0[2 * k], r0[2 * k + 1]} : (f32x2){r1[2 * k - 4], r1[2 * k - 3]}, nm = k < 2 ? (f32x2){n0[2 * k], n0[2 * k + 1]} : (f32x2){n1[2 * k - 4], n1[2 * k - 3]};
                const f32x2 a = __builtin_elementwise_fma((f32x2){bflo(xc[ch][k]), bfhi(xc[ch][k])}, rs, nm);
                u[k] = pk_bf16(a[0], a[1]);
            }
            *(LAS u32x4*)(sm + CV_U + 2048 + ln * 128 + 16 * ((2 * ch + (gm & 1)) ^ sw)) = u;
        }
    }
    bar();
    STAMP(3); CSTAMP(16);
    if (w < 4) {
        const int n = lane & 15, h = lane >> 4;
        const lptr fb = sm + cv_k(0) + w * KG_WAVE + ((n & 1) ? 0 : 4160) + 4 * (((2031 - n) >> 1) + 4 * h) - 512 * 7;
        f32x4 acc[8] = {};
        bf16x8 Fa[2][8], Ga[2][8];
        lptr gp, fp[4];
        auto addr = [&](int rr) {
            const int ln = n + (h >> 1) + 15 - 2 * rr;
            gp = sm + CV_U + 128 * ln + 16 * ((2 * w + (h & 1)) ^ (((ln >> 1) & 3) << 1));
#pragma unroll
            for (int k = 0; k < 4; ++k) { fp[k] = fb - 64 * rr + 1024 * k; asm volatile("" : "+v"(fp[k])); }
        };
        auto ldG = [&](int Q, int c) { Ga[Q][c] = *(LAS bf16x8*)(gp + 2048 * c); };
        u32x4 ftmp[2];
        auto ldF = [&](int Q, int a, int half) {
            lptr q = fp[(7 - a) >> 1] + 512 * ((7 - a) & 1) + 8 * half;
            ftmp[a & 1][2 * half] = *(LAS unsigned*)q; ftmp[a & 1][2 * half + 1] = *(LAS unsigned*)(q + 4);
            if (half) Fa[Q][a] = __builtin_bit_cast(bf16x8, ftmp[a & 1]);
        };
        const int so = n * 128 + 16 * ((2 * w + (h >> 1)) ^ (((n >> 1) & 3) << 1)) + 8 * (h & 1);
        u32x2 uv[8], xv[8];
        addr(0);
#pragma unroll
        for (int c = 0; c < 8; ++c) { ldF(0, c, 0); ldF(0, c, 1); ldG(0, c); }
        SB();
#pragma unroll
        for (int rr = 0; rr < 8; ++rr) {
            const int Q = rr & 1;
            int i = 0;
#pragma unroll
            for (int a = 0; a < 8; ++a)
#pragma unroll
                for (int c = a; c < 8; ++c) {
                    acc[c] = __builtin_amdgcn_mfma_f32_16x16x32_bf16(Fa[Q][a], Ga[Q][c - a], acc[c], 0, 0, 0); PIN(acc[c]); SB();
                    if (rr < 7) {
                        if (i == 0) { addr(rr + 1); SB(); }
                        else if (i <= 24) { const int k = (i - 1) / 3, m = (i - 1) % 3; if (m == 0) ldG(Q ^ 1, k); else ldF(Q ^ 1, k, m - 1); SB(); }
                    } else if (i < 16) {
                        if (i & 1) xv[i >> 1] = *(LAS u32x2*)(sm + CV_X + so + 2048 * (i >> 1)); else uv[i >> 1] = *(LAS u32x2*)(sm + CV_U + 2048 + so + 2048 * (i >> 1));
                        SB();
                    }
                    ++i;
                }
        }
        STAMP(4); CSTAMP(17);
        const int d = d0 + w;
        lptr yb = sm + cv_k(0) + w * KG_WAVE;
        const float dsk = (d & 1) ? bfhi(dsk_raw) : bflo(dsk_raw);
        const f32x2 dk2 = {dsk, dsk};
#pragma unroll
        for (int c2 = 0; c2 < 8; c2 += 2) {
            f32x2 hf[4];
#pragma unroll
            for (int i = 0; i < 4; ++i) { const unsigned e = uv[c2 + (i >> 1)][i & 1]; hf[i] = __builtin_elementwise_fma((f32x2){bflo(e), bfhi(e)}, dk2, (f32x2){acc[c2 + (i >> 1)][2 * (i & 1)], acc[c2 + (i >> 1)][2 * (i & 1) + 1]}); }
#pragma unroll
            for (int i = 0; i < 4; ++i) { const unsigned e = xv[c2 + (i >> 1)][i & 1]; hf[i] += (f32x2){bflo(e), bfhi(e)}; }
#pragma unroll
            for (int j = 0; j < 2; ++j) *(LAS u32x2*)(yb + (256 * (c2 + j) + 16 * n + 4 * h) * 2) = (u32x2){pk_bf16(hf[2 * j][0], hf[2 * j][1]), pk_bf16(hf[2 * j + 1][0], hf[2 * j + 1][1])};
        }
        asm volatile("" ::: "memory");
#pragma unroll
        for (int j = 0; j < 4; ++j) st_dev_o(ws + OFF_HT + (size_t)d * (T * 2) + j * 1024, lane * 16, *(LAS u32x4*)(yb + (64 * j + lane) * 16));
        STAMP(27);
        wait_vm<0>();
    } else if (w == 6) wait_flags<96, FL_W_S>(fl + (b & 7) * FL_REP + FL_WG, tag, lane);
    bar();
    if (tid < 8) __hip_atomic_fetch_add(fl + FL_HC + (tid * 32 + (b & 31)) * 32, 1u, __ATOMIC_RELAXED, __HIP_MEMORY_SCOPE_AGENT);
    STAMP(5);
    return tag;
}

__device__ __forceinline__ unsigned spare_wg(const Params& P, int j, int cb, lptr sm PROF_ARG) {
    const int tid = threadIdx.x, lane = tid & 63, w = uni(tid >> 6);
    unsigned* fl = (unsigned*)(P.ws + OFF_FL);
    unsigned tag;
    if (j < 64) tag = ln1_unit(j, P.x, P.ws PROF_FWD); else tag = epoch_tag(P.ws);
    if (j < 64) {
        wait_vm<0>();
        bar();
        if (tid < 8) set_flag_rep(fl + FL_LN + j * FL_LN_S, tag, tid);
    }
    STAMP(1);
    const int n_g = j >= 64 ? 2 : (j < 32 ? 1 : 0), g0 = j >= 64 ? 2 * (j - 64) : 64 + j;
    for (int i = 0; i < n_g; ++i) {
        wgtile_unit(g0 + i, P.wg, P.wu, P.ws, sm);
        wait_vm<0>();
        bar();
        if (tid < 8) set_flag_rep(fl + FL_WG + (g0 + i) * FL_W_S, tag, tid);
    }
    STAMP(2);
    if (j >= 64 || (j >= 32 && j < 48)) {
        const int fb = j >= 64 ? j - 64 : j;
        wdtile_unit(fb, P.wd, P.ws, sm);
        wait_vm<0>();
        bar();
        if (tid < 8) set_flag_rep(fl + FL_WD + fb * FL_W_S, tag, tid);
    }
    if (w == 6) wait_flags<96, FL_W_S>(fl + (blockIdx.x & 7) * FL_REP + FL_WG, tag, lane);
    bar();
    STAMP(5);
    return tag;
}

template <int WC>
struct G1Compute {
    f32x16 acc[3][2];
    bf16x8 Af[2][3], Bf[2][2];
    template <int P> __device__ __forceinline__ void mm(int ft, int tt) {
        acc[ft][tt] = __builtin_amdgcn_mfma_f32_32x32x16_bf16(Af[P][ft], Bf[P][tt], acc[ft][tt], 0, 0, 0); PIN(acc[ft][tt]); SB();
    }
    __device__ __forceinline__ static bf16x8 rdB(lptr p) { return __builtin_bit_cast(bf16x8, tr_read2(p, 8 * 256)); }
    template <int P, int KK, bool MORE, bool BAR>
    __device__ __forceinline__ void substep(lptr pa, const lptr (&pb)[2]) {
        mm<P>(0, 0);
        if (BAR) { sbar(); SB(); }
        if (MORE) { Bf[P ^ 1][0] = rdB(pb[0] + KK * 16 * 256); Af[P ^ 1][0] = *(LAS bf16x8*)(pa + KK * 6144); SB(); }
        mm<P>(0, 1);
        if (MORE) { Bf[P ^ 1][1] = rdB(pb[1] + KK * 16 * 256); Af[P ^ 1][1] = *(LAS bf16x8*)(pa + KK * 6144 + 1024); SB(); }
        mm<P>(1, 0);
        if (MORE) { Af[P ^ 1][2] = *(LAS bf16x8*)(pa + KK * 6144 + 2048); SB(); }
        mm<P>(1, 1);
        mm<P>(2, 0);
        mm<P>(2, 1);
    }
};

template <int WC>
__device__ __forceinline__ void g1_compute(lptr sm, char* __restrict__ ws, int wr, int lane, int t0, int cb PROF_ARG) {
    const int g = lane >> 4, q = (lane & 15) >> 2, p = lane & 3, hh = g >> 1, krow = 4 * hh + q;
    G1Compute<WC> C;
#pragma unroll
    for (int ft = 0; ft < 3; ++ft)
#pragma unroll
        for (int tt = 0; tt < 2; ++tt) C.acc[ft][tt] = (f32x16){};
    const lptr a0 = sm + G1_W + 3 * WC * 1024 + lane * 16;
    lptr b0[2];
#pragma unroll
    for (int tt = 0; tt < 2; ++tt) b0[tt] = sm + krow * 256 + (((2 * wr + tt) ^ q) * 64) + (g & 1) * 32 + p * 8;
    sbar();
    STAMP(7); CSTAMP(18);
#pragma unroll
    for (int tt = 0; tt < 2; ++tt) C.Bf[0][tt] = C.rdB(b0[tt]);
#pragma unroll
    for (int ft = 0; ft < 3; ++ft) C.Af[0][ft] = *(LAS bf16x8*)(a0 + ft * 1024);
    SB();
    lptr pa = a0, pb[2] = {b0[0], b0[1]};
    int nb = 0;
#pragma unroll 1
    for (int s = 0; s < 19; ++s) {
        C.template substep<0, 1, true, false>(pa, pb);
        nb = nb + G1_SS == G1_NS * G1_SS ? 0 : nb + G1_SS;
        pa = a0 + nb; pb[0] = b0[0] + nb; pb[1] = b0[1] + nb;
        SB();
        C.template substep<1, 0, true, true>(pa, pb);
    }
    C.template substep<0, 1, true, false>(pa, pb);
    C.template substep<1, 0, false, false>(pa, pb);
    STAMP(8); CSTAMP(19);
    bar();
    char* mF = ws + OFF_MF;
    f32x2 nm2[2], k1[2], k2[2], s2[2];
#pragma unroll
    for (int tt = 0; tt < 2; ++tt) {
        const f32x2 sq = *(LAS f32x2*)(sm + G1_ST + (32 * (2 * wr + tt) + (lane & 31)) * 8);
        const float rs = __builtin_amdgcn_rcpf(sq[1]);
        nm2[tt] = (f32x2){-sq[0], -sq[0]};
        k1[tt] = (f32x2){-2.302208198f * rs, -2.302208198f * rs};
        k2[tt] = (f32x2){-0.1029432397f * rs * rs * rs, -0.1029432397f * rs * rs * rs};
        s2[tt] = (f32x2){sq[1] * sq[1], sq[1] * sq[1]};
    }
    const unsigned mo = (unsigned)((((t0 + 64 * wr + (lane & 31)) >> 4) * 48 * 64 + ((lane & 15) + 16 * hh)) * 16);
#pragma unroll
    for (int ft = 0; ft < 3; ++ft) {
        const int fl = 16 * (3 * WC + ft) + 4 * hh;
        lptr cp = sm + G1_CS + 3072 + fl * 4;
        const f32x4 cg0 = *(LAS f32x4*)cp, cg1 = *(LAS f32x4*)(cp + 32), cu0 = *(LAS f32x4*)(cp + 384), cu1 = *(LAS f32x4*)(cp + 416);
        const int ftg = 6 * cb + 3 * WC + ft;
        const char* mb = mF + (size_t)((ftg >> 1) * 64 + 32 * (ftg & 1)) * 16;
#pragma unroll
        for (int tt = 0; tt < 2; ++tt) {
            f32x2 G[4], U[4], E[4];
#pragma unroll
            for (int r = 0; r < 4; ++r) {
                const f32x2 cg = r < 2 ? (f32x2){cg0[2 * r], cg0[2 * r + 1]} : (f32x2){cg1[2 * r - 4], cg1[2 * r - 3]};
                G[r] = __builtin_elementwise_fma(nm2[tt], cg, (f32x2){C.acc[ft][tt][2 * r], C.acc[ft][tt][2 * r + 1]}); PIN(G[r]);
            }
            SB();
#pragma unroll
            for (int r = 0; r < 4; ++r) { E[r] = G[r] * G[r]; PIN(E[r]); }
            SB();
#pragma unroll
            for (int r = 0; r < 4; ++r) { E[r] = __builtin_elementwise_fma(E[r], k2[tt], k1[tt]); PIN(E[r]); }
            SB();
#pragma unroll
            for (int r = 0; r < 4; ++r) { E[r] = G[r] * E[r]; PIN(E[r]); }
            SB();
#pragma unroll
            for (int r = 0; r < 4; ++r) {
                const f32x2 cu = r < 2 ? (f32x2){cu0[2 * r], cu0[2 * r + 1]} : (f32x2){cu1[2 * r - 4], cu1[2 * r - 3]};
                E[r] = (f32x2){__builtin_amdgcn_exp2f(E[r][0]), __builtin_amdgcn_exp2f(E[r][1])}; PIN(E[r]); SB();
                U[r] = __builtin_elementwise_fma(nm2[tt], cu, (f32x2){C.acc[ft][tt][8 + 2 * r], C.acc[ft][tt][9 + 2 * r]}); PIN(U[r]); SB();
            }
#pragma unroll
            for (int r = 0; r < 4; ++r) { E[r] = __builtin_elementwise_fma(E[r], s2[tt], s2[tt]); PIN(E[r]); }
            SB();
#pragma unroll
            for (int r = 0; r < 4; ++r) {
                E[r] = (f32x2){__builtin_amdgcn_rcpf(E[r][0]), __builtin_amdgcn_rcpf(E[r][1])}; PIN(E[r]); SB();
                G[r] = G[r] * U[r]; PIN(G[r]); SB();
            }
#pragma unroll
            for (int r = 0; r < 4; ++r) { G[r] = G[r] * E[r]; PIN(G[r]); }
            SB();
            u32x4 o;
#pragma unroll
            for (int r = 0; r < 4; ++r) o[r] = pk_bf16(G[r][0], G[r][1]);
            st_dev_o(mb, mo + tt * (2 * 48 * 1024), o);
            SB();
        }
    }
}

__device__ __forceinline__ void g1_loader(char* __restrict__ ws, lptr sm, int lw, int lane, int t0, int cb, unsigned tag) {
    const unsigned sm0 = (unsigned)(size_t)sm;
    {
        const char* hT = ws + OFF_HT;
        const char* wsrc = ws + OFF_WG + (size_t)cb * 40 * 6144 + 3 * lw * 1024;
        unsigned vh[2];
#pragma unroll
        for (int j = 0; j < 2; ++j) {
            const int o = (2 * lw + j) * 1024 + lane * 16, k = o >> 8, c = ((o >> 4) & 15) ^ ((k & 3) << 2);
            vh[j] = (unsigned)(k * (T * 2) + t0 * 2 + c * 16);
        }
        const unsigned vl = lane * 16;
        unsigned long long* st = (unsigned long long*)(ws + OFF_ST);
        auto stage = [&](int s) {
            const unsigned lb = sm0 + (s % G1_NS) * G1_SS;
            const char* hs = hT + (size_t)s * 32 * (T * 2);
            const char* wsx = wsrc + (size_t)s * 12288;
            dma16s(hs, vh[0], lb + (2 * lw) * 1024);
            if (s >= 4) dma16s(wsx, vl, lb + G1_W + (3 * lw) * 1024);
            dma16s(hs, vh[1], lb + (2 * lw + 1) * 1024);
            if (s >= 4) { dma16s(wsx + 1024, vl, lb + G1_W + (3 * lw + 1) * 1024); dma16s(wsx + 2048, vl, lb + G1_W + (3 * lw + 2) * 1024); }
        };
        u32x4 gv[3];
        unsigned long long g0, g1;
        float cs[8], sv[16];
#pragma unroll
        for (int s = 0; s < 4; ++s) stage(s);
        {
            const char* hp = hT + (size_t)(160 * lw) * (T * 2) + (t0 + 8 * cb) * 2;
            const unsigned go = (unsigned)lane * (T * 2);
            asm volatile("global_load_dwordx4 %0, %1, %2 sc1" : "=v"(gv[0]) : "v"(go), "s"(hp) : "memory");
            asm volatile("global_load_dwordx4 %0, %1, %2 sc1" : "=v"(gv[1]) : "v"(go), "s"(hp + (size_t)64 * (T * 2)) : "memory");
            asm volatile("global_load_dwordx4 %0, %1, %2 sc1" : "=v"(gv[2]) : "v"(lane < 32 ? go : 0u), "s"(hp + (size_t)128 * (T * 2)) : "memory");
        }
        wait_vm<9>();
        sbar();
#pragma unroll
        for (int s = 0; s < 19; ++s) {
            const int last = (s + 3) < 19 ? (s + 3) : 19;
            if (s == 3) asm volatile("s_waitcnt vmcnt(10)" : "+v"(gv[0]), "+v"(gv[1]), "+v"(gv[2]) :: "memory");
            else if (s == 17) asm volatile("s_waitcnt vmcnt(5)" : "+v"(g0), "+v"(g1) :: "memory");
            else wait_vm_rt<30>(s == 0 ? 7 : (s == 1 ? 10 : (s == 2 ? 13 : 5 * (last - (s + 1)) + (s == 15 || s == 16 ? 2 : 0))));
            sbar();
            if (s + 4 < 20) stage(s + 4);
            if (s == 11 && lw < 3) {
                lptr cp = sm + G1_CS + lw * 256 + lane * 4;
                *(LAS float*)(cp + 3072) = (*(LAS float*)cp + *(LAS float*)(cp + 768)) + (*(LAS float*)(cp + 1536) + *(LAS float*)(cp + 2304));
                asm volatile("s_waitcnt lgkmcnt(0)" ::: "memory");
            }
            if (s == 14) {
                asm volatile("global_load_dwordx2 %0, %1, %2 sc1" : "=v"(g0) : "v"(lane * 8), "s"(st + t0) : "memory");
                asm volatile("global_load_dwordx2 %0, %1, %2 sc1" : "=v"(g1) : "v"(lane * 8), "s"(st + t0 + 64) : "memory");
            }
            if (s >= 3 && s <= 6) {
#pragma unroll
                for (int e = 2 * (s - 3); e < 2 * (s - 3) + 2; ++e) {
                    const unsigned c32 = (unsigned)__builtin_amdgcn_readlane((int)gv[0][e >> 1], 0);
                    cs[e] = (e & 1) ? bfhi(c32) : bflo(c32);
                    const float d0 = ((e & 1) ? bfhi(gv[0][e >> 1]) : bflo(gv[0][e >> 1])) - cs[e], d1 = ((e & 1) ? bfhi(gv[1][e >> 1]) : bflo(gv[1][e >> 1])) - cs[e];
                    const float d2 = lane < 32 ? ((e & 1) ? bfhi(gv[2][e >> 1]) : bflo(gv[2][e >> 1])) - cs[e] : 0.f;
                    sv[e] = d0 + d1 + d2; sv[8 + e] = d0 * d0 + d1 * d1 + d2 * d2;
                    PIN(sv[e]); PIN(sv[8 + e]);
                }
            }
            if (s == 7) {
#pragma unroll
                for (int j = 0; j < 8; ++j) {
                    const auto r = __builtin_amdgcn_permlane32_swap(__float_as_uint(sv[j]), __float_as_uint(sv[j + 8]), false, false);
                    sv[j] = __uint_as_float(r[0]) + __uint_as_float(r[1]); PIN(sv[j]);
                }
            }
            if (s == 8) {
#pragma unroll
                for (int k = 0; k < 4; ++k) {
                    const auto r = __builtin_amdgcn_permlane16_swap(__float_as_uint(sv[k]), __float_as_uint(sv[k + 4]), false, false);
                    sv[k] = __uint_as_float(r[0]) + __uint_as_float(r[1]);
                }
#pragma unroll
                for (int k = 0; k < 4; ++k) { sv[k] = row16_sum(sv[k]); PIN(sv[k]); }
            }
            if (s == 9) {
                if ((lane & 15) == 0) *(LAS f32x4*)(sm + G1_SP + lw * 96 + (lane >> 4) * 16) = (f32x4){sv[0], sv[1], sv[2], sv[3]};
                if (lane == 0) { *(LAS f32x4*)(sm + G1_SP + lw * 96 + 64) = (f32x4){cs[0], cs[1], cs[2], cs[3]}; *(LAS f32x4*)(sm + G1_SP + lw * 96 + 80) = (f32x4){cs[4], cs[5], cs[6], cs[7]}; }
                asm volatile("s_waitcnt lgkmcnt(0)" ::: "memory");
            }
            if (s == 10 && lw == 3 && lane < 8) {
                float pm[4], m2 = 0.f, mean = 0.f;
#pragma unroll
                for (int i = 0; i < 4; ++i) {
                    const float a = *(LAS float*)(sm + G1_SP + i * 96 + lane * 4) * (1.0f / 160.0f), q = *(LAS float*)(sm + G1_SP + i * 96 + 32 + lane * 4);
                    pm[i] = *(LAS float*)(sm + G1_SP + i * 96 + 64 + lane * 4) + a;
                    m2 += q - 160.0f * a * a; mean += 0.25f * pm[i];
                }
#pragma unroll
                for (int i = 0; i < 4; ++i) m2 += 160.0f * (pm[i] - mean) * (pm[i] - mean);
                const float var = fmaxf(m2 * (1.0f / (float)D), 0.f);
                const unsigned mb = pk_bf16(mean, rbf(sqrtf(rbf(rbf(var) + 1e-5f))));
                __hip_atomic_store(st + t0 + 8 * cb + lane, ((unsigned long long)tag << 32) | mb, __ATOMIC_RELAXED, __HIP_MEMORY_SCOPE_AGENT);
            }
        }
        if (lw == 0) {
            for (unsigned spins = 0; !__all((unsigned)(g0 >> 32) == tag && (unsigned)(g1 >> 32) == tag) && spins < (1u << 18); ++spins) {
                __builtin_amdgcn_s_sleep(2);
                g0 = __hip_atomic_load(st + t0 + lane, __ATOMIC_RELAXED, __HIP_MEMORY_SCOPE_AGENT);
                g1 = __hip_atomic_load(st + t0 + 64 + lane, __ATOMIC_RELAXED, __HIP_MEMORY_SCOPE_AGENT);
            }
            *(LAS f32x2*)(sm + G1_ST + lane * 8) = (f32x2){bflo((unsigned)g0), bfhi((unsigned)g0)};
            *(LAS f32x2*)(sm + G1_ST + (64 + lane) * 8) = (f32x2){bflo((unsigned)g1), bfhi((unsigned)g1)};
        }
        bar();
    }
}

template <int ND>
__device__ __forceinline__ void g2_compute(lptr sm, int lane, int th, int dl0, int t0, int d0, bf16_t* __restrict__ out PROF_ARG) {
    f32x4 acc[ND][2] = {};
    bf16x8 A[2][ND], B[2][2];
    const lptr pb0 = sm + lane * 16 + 2 * th * 4096, pa0 = sm + lane * 16 + G2_W + dl0 * 1024;
    auto ld = [&](int P, lptr pa, lptr pb, int fl) {
#pragma unroll
        for (int tt = 0; tt < 2; ++tt) B[P][tt] = *(LAS bf16x8*)(pb + (tt * 4 + fl) * 1024);
#pragma unroll
        for (int dt = 0; dt < ND; ++dt) A[P][dt] = *(LAS bf16x8*)(pa + (fl * 5 + dt) * 1024);
        SB();
    };
    auto mm = [&](int P) {
#pragma unroll
        for (int dt = 0; dt < ND; ++dt)
#pragma unroll
            for (int tt = 0; tt < 2; ++tt) { acc[dt][tt] = __builtin_amdgcn_mfma_f32_16x16x32_bf16(A[P][dt], B[P][tt], acc[dt][tt], 0, 0, 0); PIN(acc[dt][tt]); SB(); }
    };
    sbar();
    STAMP(11); CSTAMP(20);
    ld(0, pa0, pb0, 0);
    lptr pa = pa0, pb = pb0;
    int nb = 0;
#pragma unroll 1
    for (int s = 0; s < 12; ++s) {
        ld(1, pa, pb, 1); mm(0);
        ld(0, pa, pb, 2); mm(1);
        ld(1, pa, pb, 3); mm(0);
        if (s < 11) {
            nb = nb + G2_SS == G2_NS * G2_SS ? 0 : nb + G2_SS;
            pa = pa0 + nb; pb = pb0 + nb;
            bar();
            ld(0, pa, pb, 0);
        }
        mm(1);
    }
    STAMP(12); CSTAMP(21);
    bar();
    const int g = lane >> 4, q = (lane & 15) >> 2, p = lane & 3;
#pragma unroll
    for (int dt = 0; dt < ND; ++dt)
#pragma unroll
        for (int tt = 0; tt < 2; ++tt) {
            const int row = 16 * (dl0 + dt) + 4 * g + q, seg = (2 * th + tt) ^ ((row >> 1) & 3);
            const u32x2 hv = tr_read(sm + G2_H + row * 128 + seg * 32 + p * 8);
            const unsigned o01 = pk_bf16(acc[dt][tt][0], acc[dt][tt][1]), o23 = pk_bf16(acc[dt][tt][2], acc[dt][tt][3]);
            const u32x2 r = {pk_bf16(bflo(hv[0]) + bflo(o01), bfhi(hv[0]) + bfhi(o01)), pk_bf16(bflo(hv[1]) + bflo(o23), bfhi(hv[1]) + bfhi(o23))};
            *(LAS u32x2*)(sm + (16 * (2 * th + tt) + (lane & 15)) * 176 + (16 * (dl0 + dt) + 4 * g) * 2) = r;
        }
}

extern "C" __global__ void s4d_geglu_block_end();
extern "C" __global__ __launch_bounds__(512, 2) void s4d_geglu_block(Params P PROF_ARG) {
    extern __shared__ __attribute__((aligned(16))) char smem[];
    lptr sm = (lptr)smem;
    STAMP(0);
    if (uni(threadIdx.x >> 6) == 7) {
        unsigned long long pc; unsigned off, len;
        asm volatile("s_getpc_b64 %0\n.Lpc_anchor_%=:\n\ts_mov_b32 %1, .Lpc_anchor_%=-s4d_geglu_block\n\ts_mov_b32 %2, s4d_geglu_block_end-s4d_geglu_block" : "=s"(pc), "=s"(off), "=s"(len));
        const unsigned o = (blockIdx.x >> 3) * 2048 + (threadIdx.x & 63) * 32;
        if (o < len) dma4((const void*)(pc - off), o, (unsigned)(size_t)sm + LDS_BYTES - 256);
        if (blockIdx.x >= 248) {
            const unsigned l = threadIdx.x & 63;
#pragma unroll
            for (int i = 0; i < 4; ++i) dma4(P.ws, (l + 64 * i) * (unsigned)(OFF_PROF / 256 & ~(size_t)127), (unsigned)(size_t)sm + LDS_BYTES - 256);
            dma4(P.out, l * (unsigned)(T * D * 2 / 64), (unsigned)(size_t)sm + LDS_BYTES - 256);
        }
    }
    const int tid = threadIdx.x, lane = tid & 63, w = uni(tid >> 6), b = blockIdx.x;
    const unsigned sm0 = (unsigned)(size_t)sm;
    char* ws = P.ws;
    unsigned* fl = (unsigned*)(ws + OFF_FL);
    unsigned tag;
    const int grp = b & 7, idx = b >> 3, grp0 = grp;
    const int rb = (grp & 1) * 8 + (idx & 7), cb = (grp >> 1) * 4 + (idx >> 3);
    const int tb = (grp >> 1) * 8 + (idx & 7), db = (grp & 1) * 4 + (idx >> 3);

    if (b < 160) tag = conv_wg(P, b, cb, sm PROF_FWD); else tag = spare_wg(P, b - 160, cb, sm PROF_FWD);

    if (w >= 4) g1_prefetch_w(ws, sm, cb, w & 3, lane);
    if (w == 4) wait_flags<32, 32>(fl + FL_HC + grp0 * 32 * 32, (tag << 3) + 5u, lane);
    if (w == 7) wait_flags<48, FL_W_S>(fl + grp0 * FL_REP + FL_WD, tag, lane);
    bar();
    STAMP(6);
    if (w >= 4) g1_loader(ws, sm, w & 3, lane, 128 * rb, cb, tag);
    else if (w & 1) g1_compute<1>(sm, ws, w >> 1, lane, 128 * rb, cb PROF_FWD); else g1_compute<0>(sm, ws, w >> 1, lane, 128 * rb, cb PROF_FWD);

    const int t0 = 64 * tb, d0 = 80 * db;
    if (w >= 4) {
        const int lw = w & 3;
        const char* ms = ws + OFF_MF + (size_t)(4 * tb + lw) * 48 * 1024;
        const char* wsrc = ws + OFF_WD + (size_t)lw * (40 * 1024) + (size_t)(5 * db) * 1024;
        const unsigned vl = lane * 16;
        auto stage_w = [&](int s) {
#pragma unroll
            for (int j = 0; j < 5; ++j) dma16s(wsrc + (size_t)(4 * s) * (40 * 1024) + j * 1024, vl, sm0 + (s % G2_NS) * G2_SS + G2_W + (5 * lw + j) * 1024);
        };
        auto stage_m = [&](int s) {
#pragma unroll
            for (int j = 0; j < 4; ++j) dma16s(ms + (size_t)(4 * s + j) * 1024, vl, sm0 + (s % G2_NS) * G2_SS + (4 * lw + j) * 1024);
        };
        stage_w(0); stage_w(1); stage_w(2);
#pragma unroll
        for (int j = 0; j < 3; ++j) {
            const int i = lw + 4 * j < 10 ? lw + 4 * j : lw;
            const int o = i * 1024 + lane * 16, row = o >> 7, c = ((o >> 4) & 7) ^ (((row >> 1) & 3) << 1);
            dma16s(ws + OFF_HT, (unsigned)((d0 + row) * (T * 2) + t0 * 2 + c * 16), sm0 + G2_H + i * 1024);
        }
        sbar();
        if (lw == 0) wait_flags<16, FL_M_S>(fl + FL_M + 16 * (tb >> 1) * FL_M_S, tag, lane);
        sbar();
        STAMPL(10);
        stage_m(0); stage_m(1); stage_m(2);
        wait_vm<8>();
        sbar();
#pragma unroll
        for (int s = 0; s < 11; ++s) {
            if (s == 0) wait_vm<4>(); else if (s <= 9) wait_vm<9>(); else wait_vm<0>();
            sbar();
            if (s + 3 < 12) { stage_m(s + 3); stage_w(s + 3); }
        }
        sbar();
    } else {
        STAMP(28);
        wait_vm<0>();
        sbar();
        if (tid == 0) set_flag(fl + FL_M + (16 * rb + cb) * FL_M_S, tag);
        STAMP(9);
        sbar();
        const int th = w & 1;
        if (w >> 1) g2_compute<2>(sm, lane, th, 3, t0, d0, P.out PROF_FWD); else g2_compute<3>(sm, lane, th, 0, t0, d0, P.out PROF_FWD);
    }
    bar();
    for (int ch = tid; ch < 640; ch += 512) {
        const int row = ch / 10, c = ch % 10;
        *(u32x4*)(P.out + (size_t)(t0 + row) * D + d0 + c * 8) = *(LAS u32x4*)(sm + row * 176 + c * 16);
    }
    if (b == 0 && tid == 0) fl[FL_E] = tag;
    STAMP(15);
}

extern "C" __global__ void s4d_geglu_block_end() {}

extern "C" void kernel_launch(void* const* d_in, const int* in_sizes, int n_in, void* d_out, int out_size, void* d_ws, size_t ws_size, hipStream_t stream) {
    Params P;
    P.x = (const bf16_t*)d_in[0]; P.logA = (const bf16_t*)d_in[1]; P.Aim = (const bf16_t*)d_in[2]; P.Cre = (const bf16_t*)d_in[3]; P.Cim = (const bf16_t*)d_in[4];
    P.logdt = (const bf16_t*)d_in[5]; P.Dskip = (const bf16_t*)d_in[6]; P.wg = (const bf16_t*)d_in[8]; P.wu = (const bf16_t*)d_in[9]; P.wd = (const bf16_t*)d_in[10];
    P.ws = (char*)d_ws; P.out = (bf16_t*)d_out;
    (void)hipFuncSetAttribute((const void*)s4d_geglu_block, hipFuncAttributeMaxDynamicSharedMemorySize, LDS_BYTES);
    s4d_geglu_block<<<256, 512, LDS_BYTES, stream>>>(P);
}
```

```cpp
#include <hip/hip_runtime.h>
#include <stdint.h>

typedef unsigned short bf16_t;
typedef short s16x4 __attribute__((ext_vector_type(4)));
typedef __bf16 bf16x8 __attribute__((ext_vector_type(8)));
typedef __bf16 bf16x2 __attribute__((ext_vector_type(2)));
typedef _Float16 h16x8 __attribute__((ext_vector_type(8)));
typedef _Float16 h16x2 __attribute__((ext_vector_type(2)));
typedef float f32x2 __attribute__((ext_vector_type(2)));
typedef float f32x4 __attribute__((ext_vector_type(4)));
typedef float f32x16 __attribute__((ext_vector_type(16)));
typedef unsigned u32x2 __attribute__((ext_vector_type(2)));
typedef unsigned u32x4 __attribute__((ext_vector_type(4)));
#define LAS __attribute__((address_space(3)))
typedef LAS char* lptr;

constexpr int T = 2048, D = 640, NM = 192, F = 1536;
constexpr size_t OFF_S1 = 0;
constexpr size_t OFF_HT = OFF_S1 + (size_t)T * 8;
constexpr size_t OFF_CS = OFF_HT + (size_t)T * D * 2;
constexpr size_t OFF_MF = OFF_CS + 65536;
constexpr size_t OFF_WD = OFF_MF + (size_t)T * F * 2;
constexpr size_t OFF_WG = OFF_WD + (size_t)F * D * 2;
constexpr size_t OFF_ST = OFF_WG + (size_t)2 * F * D * 2;
constexpr size_t OFF_FL = OFF_ST + (size_t)T * 8;
constexpr size_t OFF_PROF = OFF_FL + 196608;
constexpr int FL_E = 0;
constexpr int FL_REP = 4096;
constexpr int FL_LN = FL_REP, FL_LN_S = 16;
constexpr int FL_WG = FL_LN + 1024, FL_WD = FL_WG + 96 * 8, FL_W_S = 8;
constexpr int FL_M = 64, FL_M_S = 8;
constexpr int FL_HC = 9 * FL_REP;
static_assert(FL_WD + 48 * FL_W_S <= 2 * FL_REP && FL_M + 256 * FL_M_S <= FL_REP, "flag map");

#define STAMP(i)
#define STAMPL(i)
#define CSTAMP(i)
#define PROF_ARG
#define PROF_PASS
#define PROF_FWD

__device__ __forceinline__ float bf2f(unsigned v) { return __uint_as_float(v << 16); }
__device__ __forceinline__ float bflo(unsigned v) { return __uint_as_float(v << 16); }
__device__ __forceinline__ float bfhi(unsigned v) { return __uint_as_float(v & 0xffff0000u); }
__device__ __forceinline__ unsigned pk_bf16(float lo, float hi) { f32x2 v = {lo, hi}; return __builtin_bit_cast(unsigned, __builtin_convertvector(v, bf16x2)); }
__device__ __forceinline__ float rbf(float f) { return bflo(pk_bf16(f, f)); }
__device__ __forceinline__ unsigned pk_f16(f32x2 v) { return __builtin_bit_cast(unsigned, __builtin_convertvector(v, h16x2)); }
template <int CTRL> __device__ __forceinline__ float dppf(float x) { return __int_as_float(__builtin_amdgcn_update_dpp(0, __float_as_int(x), CTRL, 0xF, 0xF, true)); }
__device__ __forceinline__ float row16_sum(float x) { x += dppf<0xB1>(x); x += dppf<0x4E>(x); x += dppf<0x141>(x); x += dppf<0x140>(x); return x; }
__device__ __forceinline__ float row16_max(float x) { x = fmaxf(x, dppf<0xB1>(x)); x = fmaxf(x, dppf<0x4E>(x)); x = fmaxf(x, dppf<0x141>(x)); x = fmaxf(x, dppf<0x140>(x)); return x; }
__device__ __forceinline__ float xsum16_32(float x) {
    const unsigned u = __float_as_uint(x);
    const auto r = __builtin_amdgcn_permlane16_swap(u, u, false, false);
    const unsigned v = __float_as_uint(__uint_as_float(r[0]) + __uint_as_float(r[1]));
    const auto q = __builtin_amdgcn_permlane32_swap(v, v, false, false);
    return __uint_as_float(q[0]) + __uint_as_float(q[1]);
}
__device__ __forceinline__ u32x2 tr_read(lptr p) { return __builtin_bit_cast(u32x2, __builtin_amdgcn_ds_read_tr16_b64_v4i16((LAS s16x4*)p)); }
__device__ __forceinline__ u32x4 tr_read2(lptr p, int off2) { const u32x2 r1 = tr_read(p), r2 = tr_read(p + off2); return (u32x4){r1[0], r1[1], r2[0], r2[1]}; }
__device__ __forceinline__ int uni(int v) { return __builtin_amdgcn_readfirstlane(v); }

__device__ __forceinline__ void dma16(const void* sbase, unsigned voff, unsigned ldsaddr) {
    asm volatile("s_mov_b32 m0, %2\n\ts_nop 0\n\tglobal_load_lds_dwordx4 %0, %1" :: "v"(voff), "s"(sbase), "s"(ldsaddr) : "memory");
}
__device__ __forceinline__ void dma4(const void* sbase, unsigned voff, unsigned ldsaddr) {
    asm volatile("s_mov_b32 m0, %2\n\ts_nop 0\n\tglobal_load_lds_dword %0, %1" :: "v"(voff), "s"(sbase), "s"(ldsaddr) : "memory");
}
template <int N> __device__ __forceinline__ void wait_vm() { asm volatile("s_waitcnt vmcnt(%0)" :: "n"(N) : "memory"); }
template <int N> __device__ __forceinline__ void wait_vm_rt(int n) {
    if (n >= N) wait_vm<N>(); else if constexpr (N > 0) wait_vm_rt<N - 1>(n);
}
__device__ __forceinline__ void bar() { asm volatile("s_waitcnt lgkmcnt(0)\n\ts_barrier" ::: "memory"); }
__device__ __forceinline__ void sbar() { asm volatile("s_barrier" ::: "memory"); }
#define SB() __builtin_amdgcn_sched_barrier(0)
#define PIN(x) asm volatile("" : "+v"(x))
__device__ __forceinline__ void st_dev(void* p, u32x4 v) { asm volatile("global_store_dwordx4 %0, %1, off sc1\n\ts_nop 1" :: "v"(p), "v"(v) : "memory"); }
__device__ __forceinline__ void st_dev_o(const void* sbase, unsigned voff, u32x4 v) { asm volatile("global_store_dwordx4 %0, %1, %2 sc1\n\ts_nop 1" :: "v"(voff), "v"(v), "s"(sbase) : "memory"); }
__device__ __forceinline__ void st_dev4(void* p, float v) { asm volatile("global_store_dword %0, %1, off sc1\n\ts_nop 1" :: "v"(p), "v"(v) : "memory"); }
__device__ __forceinline__ void dma16s(const void* sbase, unsigned voff, unsigned ldsaddr) {
    asm volatile("s_mov_b32 m0, %2\n\ts_nop 0\n\tglobal_load_lds_dwordx4 %0, %1 sc1" :: "v"(voff), "s"(sbase), "s"(ldsaddr) : "memory");
}
__device__ __forceinline__ void dma4s(const void* sbase, unsigned voff, unsigned ldsaddr) {
    asm volatile("s_mov_b32 m0, %2\n\ts_nop 0\n\tglobal_load_lds_dword %0, %1 sc1" :: "v"(voff), "s"(sbase), "s"(ldsaddr) : "memory");
}
__device__ __forceinline__ void set_flag(unsigned* f, unsigned tag) { __hip_atomic_store(f, tag, __ATOMIC_RELAXED, __HIP_MEMORY_SCOPE_AGENT); }
__device__ __forceinline__ void set_flag_rep(unsigned* f, unsigned tag, int lane) { __hip_atomic_store(f + lane * FL_REP, tag, __ATOMIC_RELAXED, __HIP_MEMORY_SCOPE_AGENT); }
template <int N, int S>
__device__ __forceinline__ void wait_flags(unsigned* f, unsigned tag, int lane) {
    for (unsigned spins = 0; spins < (1u << 18); ++spins) {
        bool ok = true;
        if (lane < N) ok = __hip_atomic_load(f + lane * S, __ATOMIC_RELAXED, __HIP_MEMORY_SCOPE_AGENT) == tag;
        if (N > 64 && lane + 64 < N) ok &= __hip_atomic_load(f + (64 + lane) * S, __ATOMIC_RELAXED, __HIP_MEMORY_SCOPE_AGENT) == tag;
        if (N > 128 && lane + 128 < N) ok &= __hip_atomic_load(f + (128 + lane) * S, __ATOMIC_RELAXED, __HIP_MEMORY_SCOPE_AGENT) == tag;
        if (__all(ok)) break;
        __builtin_amdgcn_s_sleep(1);
    }
    asm volatile("" ::: "memory");
}

__device__ __forceinline__ unsigned epoch_tag(const char* ws) {
    unsigned e;
    asm volatile("s_load_dword %0, %1, 0x0\n\ts_waitcnt lgkmcnt(0)" : "=s"(e) : "s"(ws + OFF_FL + FL_E * 4) : "memory");
    return e + 1u;
}

constexpr int G1_NS = 7, G1_SS = 20480, G1_W = 8192;
constexpr int G2_NS = 4, G2_SS = 36 * 1024, G2_W = 16 * 1024;
constexpr int G2_H = G2_NS * G2_SS;
constexpr int G1_CS = G2_H + 10240;
constexpr int G1_ST = G1_CS + 3840;
constexpr int G1_SP = G1_ST + 1024;
constexpr int LDS_BYTES = 163840;
static_assert(G1_SP + 512 <= LDS_BYTES, "LDS map");
constexpr int KG_ROW = 272;
constexpr int KG_WAVE = 57 * KG_ROW;
constexpr int CV_KCH = 8320;
__host__ __device__ constexpr int cv_k(int c) { return (4 + c) * KG_WAVE + 3072; }
__host__ __device__ constexpr int cv_kc(int c) { return cv_k(c) + CV_KCH; }
static_assert(3072 + CV_KCH + 1296 <= KG_WAVE, "per-channel area");
constexpr int CV_X = 8 * KG_WAVE + 7 * KG_ROW + 16;
constexpr int CV_U = CV_X + 128 * 128;
constexpr int CV_M = CV_U + 144 * 128;
static_assert(CV_X % 128 == 0 || true, "");
static_assert(CV_M + 32 + 256 <= 163840 && CV_X % 16 == 0, "LDS map");
constexpr int LN_ROW = 1344;

__device__ __forceinline__ unsigned ln1_unit(int j, const bf16_t* __restrict__ x, char* __restrict__ ws PROF_ARG) {
    const int tid = threadIdx.x, r = tid >> 4, l16 = tid & 15, lane = tid & 63, w = uni(tid >> 6);
    const int t = 32 * j + r;
    u32x4 v[5];
#pragma unroll
    for (int i = 0; i < 5; ++i) v[i] = *(const u32x4*)(x + (size_t)t * D + (l16 + 16 * i) * 8);
    const unsigned tag = epoch_tag(ws);
    f32x2 f[20];
    f32x2 s2 = {0.f, 0.f};
#pragma unroll
    for (int i = 0; i < 5; ++i)
#pragma unroll
        for (int e = 0; e < 4; ++e) { f[4 * i + e] = (f32x2){bflo(v[i][e]), bfhi(v[i][e])}; s2 += f[4 * i + e]; }
    const float mean = row16_sum(s2[0] + s2[1]) * (1.0f / (float)D);
    f32x2 q2 = {0.f, 0.f};
#pragma unroll
    for (int i = 0; i < 20; ++i) { const f32x2 dv = f[i] - mean; q2 += dv * dv; }
    const float var = row16_sum(q2[0] + q2[1]) * (1.0f / (float)D);
    const float rs = __builtin_amdgcn_rcpf(rbf(sqrtf(rbf(rbf(var) + 1e-5f)))), nm = -rbf(mean) * rs;
    u32x4 o;
#pragma unroll
    for (int i = 0; i < 4; ++i) {
        const unsigned a = (unsigned)__builtin_amdgcn_readlane((int)__float_as_uint(rs), 16 * i), c = (unsigned)__builtin_amdgcn_readlane((int)__float_as_uint(nm), 16 * i);
        o[i] = lane == 0 ? a : c;
    }
    if (lane < 2) st_dev(ws + OFF_S1 + (size_t)lane * (T * 4) + (32 * j + 4 * w) * 4, o);
    if (tid < 4) set_flag((unsigned*)(ws + OFF_FL) + FL_HC + (4 * j + tid) * 32, tag << 3);
    STAMP(24);
    return tag;
}

__device__ __forceinline__ void wgtile_unit(int b, const bf16_t* __restrict__ wg, const bf16_t* __restrict__ wu, char* __restrict__ ws, lptr sm) {
    const int tid = threadIdx.x, lane = tid & 63, w = uni(tid >> 6);
    const int j = b >> 2, kq = b & 3;
    const int c16 = tid & 15, r0 = tid >> 4;
    u32x4 v[5];
    const bf16_t* src = (c16 < 8 ? wg : wu) + (size_t)(160 * kq + r0) * F + 64 * j + (c16 & 7) * 8;
#pragma unroll
    for (int i = 0; i < 5; ++i) v[i] = *(const u32x4*)(src + (size_t)(32 * i) * F);
    f32x2 a[4] = {};
#pragma unroll
    for (int i = 0; i < 5; ++i) {
        const int k = 32 * i + r0;
        *(LAS u32x4*)(sm + k * 256 + (((c16 >> 1) ^ (k & 3)) * 32) + (c16 & 1) * 16) = v[i];
#pragma unroll
        for (int e = 0; e < 4; ++e) a[e] += (f32x2){bflo(v[i][e]), bfhi(v[i][e])};
    }
#pragma unroll
    for (int e = 0; e < 4; ++e)
#pragma unroll
        for (int hf = 0; hf < 2; ++hf) {
            const float s = xsum16_32(a[e][hf]);
            if (lane < 16) *(LAS float*)(sm + 40960 + (w * 128 + c16 * 8 + 2 * e + hf) * 4) = s;
        }
    bar();
    const int g = lane >> 4, q = (lane & 15) >> 2, p = lane & 3;
    u32x4 o[5];
#pragma unroll
    for (int i = 0; i < 5; ++i) {
        const int item = w + 8 * i, ftl = item & 3, sub = item >> 2;
        const int k = 16 * sub + 4 * (g >> 1) + q;
        o[i] = tr_read2(sm + k * 256 + ((((g & 1) * 4 + ftl) ^ q) * 32) + p * 8, 8 * 256);
    }
#pragma unroll
    for (int i = 0; i < 5; ++i) {
        const int item = w + 8 * i, ftl = item & 3, sub = item >> 2;
        const int ftg = 4 * j + ftl, cb = ftg / 6, ft = ftg % 6;
        st_dev(ws + OFF_WG + ((((size_t)cb * 40 + 10 * kq + sub) * 6 + ft) * 64 + lane) * 16, o[i]);
    }
    if (tid < 128) {
        float s = 0.f;
#pragma unroll
        for (int i = 0; i < 8; ++i) s += *(LAS float*)(sm + 40960 + (i * 128 + tid) * 4);
        st_dev4(ws + OFF_CS + (size_t)(kq * 2 * F + (tid >> 6) * F + 64 * j + (tid & 63)) * 4, s);
    }
}

__device__ __forceinline__ void wdtile_unit(int fb, const bf16_t* __restrict__ wd, char* __restrict__ ws, lptr sm) {
    const int tid = threadIdx.x, lane = tid & 63, w = uni(tid >> 6);
    u32x4 v[5];
#pragma unroll
    for (int i = 0; i < 5; ++i) { const int ch = tid + 512 * i, row = ch / 80, c = ch % 80; v[i] = *(const u32x4*)(wd + (size_t)(32 * fb + row) * D + c * 8); }
#pragma unroll
    for (int i = 0; i < 5; ++i) { const int ch = tid + 512 * i, row = ch / 80, c = ch % 80; *(LAS u32x4*)(sm + row * LN_ROW + c * 16) = v[i]; }
    bar();
    const int g = lane >> 4, q = (lane & 15) >> 2, p = lane & 3;
    u32x4 o[5];
#pragma unroll
    for (int i = 0; i < 5; ++i) o[i] = tr_read2(sm + (16 * (g >> 1) + 4 * (g & 1) + q) * LN_ROW + (16 * (w + 8 * i) + 4 * p) * 2, 8 * LN_ROW);
#pragma unroll
    for (int i = 0; i < 5; ++i) st_dev(ws + OFF_WD + (((size_t)fb * 40 + w + 8 * i) * 64 + lane) * 16, o[i]);
}

__device__ __forceinline__ f32x2 cmul(f32x2 a, f32x2 b) {
    const f32x2 t = (f32x2){a[0], a[0]} * b;
    const f32x2 bs = {-b[1], b[0]};
    return __builtin_elementwise_fma((f32x2){a[1], a[1]}, bs, t);
}
__device__ __forceinline__ unsigned cmul_h(unsigned a, unsigned b) {
    unsigned t, r;
    asm("v_pk_mul_f16 %0, %1, %2 op_sel:[0,0] op_sel_hi:[0,1]" : "=v"(t) : "v"(a), "v"(b));
    asm("v_pk_fma_f16 %0, %1, %2, %3 op_sel:[1,1,0] op_sel_hi:[1,0,1] neg_lo:[0,1,0]" : "=v"(r) : "v"(a), "v"(b), "v"(t));
    return r;
}
__device__ __forceinline__ f32x2 cexp_p(float arl, float air, float p) {
    const float mag = __builtin_amdgcn_exp2f(arl * p);
    float rev = air * p; rev -= rintf(rev);
    return (f32x2){mag * __builtin_amdgcn_cosf(rev), mag * __builtin_amdgcn_sinf(rev)};
}
struct KgenIn { unsigned ldt, la, ai, cr[3], ci[3]; };
__device__ __forceinline__ void kgen_load(KgenIn& k, int d, int third, int lane, const bf16_t* __restrict__ logA, const bf16_t* __restrict__ Aim,
                                          const bf16_t* __restrict__ Cre, const bf16_t* __restrict__ Cim, const bf16_t* __restrict__ logdt) {
    k.ldt = logdt[d]; k.la = logA[d * NM + 64 * third + lane]; k.ai = Aim[d * NM + 64 * third + lane];
#pragma unroll
    for (int i = 0; i < 3; ++i) { k.cr[i] = Cre[d * NM + 64 * i + lane]; k.ci[i] = Cim[d * NM + 64 * i + lane]; }
}
__device__ __forceinline__ void kgen_task(const KgenIn& k, int third, int lane, lptr tab, f32x4 (&acc)[3], float& unscale) {
    const float dt = rbf(expf(bf2f(k.ldt)));
    float mx = 0.f;
#pragma unroll
    for (int i = 0; i < 3; ++i) mx = fmaxf(mx, fmaxf(fabsf(bf2f(k.cr[i])), fabsf(bf2f(k.ci[i]))));
    mx = row16_max(mx);
    {
        const auto r1 = __builtin_amdgcn_permlane16_swap(__float_as_uint(mx), __float_as_uint(mx), false, false);
        mx = fmaxf(__uint_as_float(r1[0]), __uint_as_float(r1[1]));
        const auto r2 = __builtin_amdgcn_permlane32_swap(__float_as_uint(mx), __float_as_uint(mx), false, false);
        mx = fmaxf(__uint_as_float(r2[0]), __uint_as_float(r2[1]));
    }
    mx *= 2.f * dt;
    int e = (int)((__float_as_uint(mx) >> 23) & 0xff) - 126;
    e = e < -100 ? -100 : (e > 100 ? 100 : e);
    const float scale = __uint_as_float((unsigned)(127 - e) << 23);
    unscale = __uint_as_float((unsigned)(127 + e) << 23);
    const float are = -rbf(__builtin_amdgcn_exp2f(bf2f(k.la) * 1.4426950408889634f)) * dt, aim = bf2f(k.ai) * dt;
    const float crw = bf2f(third == 0 ? k.cr[0] : (third == 1 ? k.cr[1] : k.cr[2])) * dt, ciw = bf2f(third == 0 ? k.ci[0] : (third == 1 ? k.ci[1] : k.ci[2])) * dt;
    const float arl = are * 1.4426950408889634f, air = aim * 0.15915494309189535f;
    lptr base = tab + 4 * lane;
#define PUT(row, z) *(LAS unsigned*)(base + (row) * KG_ROW) = pk_f16(z)
#define PUTH(row, z) *(LAS unsigned*)(base + (row) * KG_ROW) = (z)
    const f32x2 w1 = cexp_p(arl, air, 1.f), w8 = cexp_p(arl, air, 8.f), H1 = cexp_p(arl, air, 128.f);
    f32x2 qv = {2.f * scale * crw, -2.f * scale * ciw};
    const f32x2 w8c = {w8[0], -w8[1]};
    f32x2 Ec = w1, Hc = H1;
    unsigned Eh[8], Hh[8];
    Eh[1] = pk_f16(w1); Hh[1] = pk_f16(H1);
    PUT(0, qv); PUTH(16, 0x00003c00u); PUTH(17, Eh[1]); PUTH(24, Hh[1]);
#pragma unroll
    for (int i = 1; i < 16; ++i) {
        qv = cmul(qv, w8c);
        Hc = cmul(Hc, H1);
        if (i < 7) Ec = cmul(Ec, w1);
        PUT(i, qv);
        if (i < 7) { Eh[i + 1] = pk_f16(Ec); PUTH(17 + i, Eh[i + 1]); }
        const int hi = i + 1;
        if (hi < 8) { Hh[hi] = pk_f16(Hc); PUTH(hi < 4 ? 32 + 4 * (hi - 2) : 40 + 2 * (hi - 4), Hh[hi]); } else PUT(48 + (hi - 8), Hc);
        if (i >= 7 && i < 14) PUTH(25 + (i - 7), cmul_h(Eh[i - 6], Hh[1]));
        if (i >= 7 && i < 10) PUTH(33 + (i - 7), cmul_h(Eh[2 * (i - 6)], Hh[2]));
        if (i >= 10 && i < 13) PUTH(37 + (i - 10), cmul_h(Eh[2 * (i - 9)], Hh[3]));
        if (i >= 11 && i < 15) PUTH(41 + 2 * (i - 11), cmul_h(Hh[4 + (i - 11)], Eh[4]));
    }
#undef PUTH
#undef PUT
    asm volatile("" ::: "memory");
    lptr rb = tab + (lane & 15) * KG_ROW + (lane >> 4) * 16;
#pragma unroll
    for (int mt = 0; mt < 3; ++mt) acc[mt] = (f32x4){};
#pragma unroll
    for (int ks = 0; ks < 4; ++ks) {
        const h16x8 bq = *(LAS h16x8*)(rb + ks * 64);
#pragma unroll
        for (int mt = 0; mt < 3; ++mt) {
            const h16x8 ar = *(LAS h16x8*)(rb + (16 + 16 * mt) * KG_ROW + ks * 64);
            acc[mt] = __builtin_amdgcn_mfma_f32_16x16x32_f16(ar, bq, acc[mt], 0, 0, 0);
        }
    }
#pragma unroll
    for (int mt = 0; mt < 3; ++mt) PIN(acc[mt]);
    asm volatile("s_waitcnt lgkmcnt(0)" ::: "memory");
}

__device__ __forceinline__ void g1_prefetch_w(char* __restrict__ ws, lptr sm, int cb, int lw, int lane) {
    const unsigned sm0 = (unsigned)(size_t)sm;
    const char* wsrc = ws + OFF_WG + (size_t)cb * 40 * 6144 + 3 * lw * 1024;
#pragma unroll
    for (int s = 0; s < 4; ++s)
#pragma unroll
        for (int i = 0; i < 3; ++i) dma16s(wsrc + (size_t)s * 12288 + i * 1024, lane * 16, sm0 + s * G1_SS + G1_W + (3 * lw + i) * 1024);
    if (lw < 3) {
        const int f0 = 96 * cb;
        const unsigned co = (unsigned)(((lw == 0 ? f0 + lane : (lw == 1 ? (lane < 32 ? f0 + 64 + lane : F + f0 + lane - 32) : F + f0 + 32 + lane))) * 4);
#pragma unroll
        for (int kq = 0; kq < 4; ++kq) dma4s(ws + OFF_CS + kq * 2 * F * 4, co, sm0 + G1_CS + kq * 768 + lw * 256);
    }
}

struct KTab { unsigned short v[2048]; };
constexpr int ci_of(int l) {
    if (l < 0) return 641;
    if (l < 256) return l;
    const int s = l < 512 ? 1 : (l < 1024 ? 2 : 3);
    return ((l + (1 << (s - 1)) - 1 + ((l >> s) & 1)) >> s) + 128 * s;
}
constexpr KTab make_tab() { KTab t{}; for (int y = 0; y < 2048; ++y) t.v[y] = (unsigned short)(2 * ci_of(2047 - y)); return t; }
__device__ const __attribute__((aligned(16))) KTab g_ktab = make_tab();

struct Params {
    const bf16_t *x, *logA, *Aim, *Cre, *Cim, *logdt, *Dskip, *wg, *wu, *wd;
    char* ws;
    bf16_t* out;
};

__device__ __forceinline__ unsigned conv_wg(const Params& P, int b, int cb, lptr sm PROF_ARG) {
    const int tid = threadIdx.x, lane = tid & 63, w = uni(tid >> 6);
    char* ws = P.ws;
    const int d0 = 4 * ((b & 7) * 20 + (b >> 3));
    const unsigned sm0 = (unsigned)(size_t)sm;
    unsigned* fl = (unsigned*)(ws + OFF_FL);
    KgenIn k1, k2;
    u32x4 tb[4];
    u32x2 xg[8];
    kgen_load(k1, d0 + w / 3, w % 3, lane, P.logA, P.Aim, P.Cre, P.Cim, P.logdt);
    const unsigned tag = epoch_tag(ws);
    if (w < 4) {
        const int t2 = 8 + w;
        kgen_load(k2, d0 + t2 / 3, t2 % 3, lane, P.logA, P.Aim, P.Cre, P.Cim, P.logdt);
#pragma unroll
        for (int i = 0; i < 4; ++i) tb[i] = *(const u32x4*)(&g_ktab.v[8 * (lane + 64 * i)]);
    }
    const unsigned dsk_raw = ((const unsigned*)P.Dskip)[(d0 + (w & 3)) >> 1];
    asm volatile("" : "+v"(k1.ldt), "+v"(k1.la), "+v"(k1.ai), "+v"(k1.cr[0]), "+v"(k1.cr[1]), "+v"(k1.cr[2]), "+v"(k1.ci[0]), "+v"(k1.ci[1]), "+v"(k1.ci[2]) :: "memory");
    STAMP(1);
    const lptr tab = sm + w * KG_WAVE;
    LAS unsigned* cnt = (LAS unsigned*)(sm + CV_M + 16);
    if (w == 7 && lane == 0) *cnt = 0;
    bar();
    if (w >= 4) {
#pragma unroll
        for (int i = 0; i < 8; ++i) xg[i] = *(const u32x2*)(P.x + (size_t)(8 * (tid - 256) + i) * D + d0);
        asm volatile("" ::: "memory");
    }
    f32x4 a1[3], a2[3];
    {
        int third = w % 3, chn = w / 3;
#pragma unroll 1
        for (int rnd = 0; rnd < 2; ++rnd) {
            float un;
            kgen_task(k1, third, lane, tab, a2, un);
            if (third == 0 && lane == 0) *(LAS float*)(sm + CV_M + chn * 4) = un;
            if (rnd == 1 || w >= 4) break;
#pragma unroll
            for (int mt = 0; mt < 3; ++mt) a1[mt] = a2[mt];
            k1 = k2; third = (8 + w) % 3; chn = (8 + w) / 3;
        }
    }
    if (w < 4) {
#pragma unroll
        for (int mt = 0; mt < 3; ++mt) { *(LAS f32x4*)(tab + (mt * 64 + lane) * 16) = a1[mt]; *(LAS f32x4*)(tab + 3072 + (mt * 64 + lane) * 16) = a2[mt]; }
        asm volatile("s_waitcnt lgkmcnt(0)" ::: "memory");
        if (lane == 0) __hip_atomic_fetch_add(cnt, 1u, __ATOMIC_RELAXED, __HIP_MEMORY_SCOPE_WORKGROUP);
        while (__hip_atomic_load(cnt, __ATOMIC_RELAXED, __HIP_MEMORY_SCOPE_WORKGROUP) < 8u) __builtin_amdgcn_s_sleep(0);
        asm volatile("" ::: "memory");
        STAMP(2);
        f32x4 ks[3];
        lptr pt[3];
#pragma unroll
        for (int j = 0; j < 3; ++j) { const int t = 3 * w + j; pt[j] = sm + (t & 7) * KG_WAVE + (t >> 3) * 3072 + lane * 16; }
        const float un = *(LAS float*)(sm + CV_M + w * 4);
#pragma unroll
        for (int mt = 0; mt < 3; ++mt) ks[mt] = ((*(LAS f32x4*)(pt[0] + mt * 1024) + *(LAS f32x4*)(pt[1] + mt * 1024)) + *(LAS f32x4*)(pt[2] + mt * 1024)) * un;
        lptr kc = sm + cv_kc(0) + w * KG_WAVE;
        const int hq = lane >> 4, col = lane & 15;
        const u32x2 p0 = {pk_bf16(ks[0][0], ks[0][1]), pk_bf16(ks[0][2], ks[0][3])}, p1 = {pk_bf16(ks[1][0], ks[1][1]), pk_bf16(ks[1][2], ks[1][3])};
        const u32x2 p2 = {pk_bf16(ks[2][0], ks[2][1]), pk_bf16(ks[2][2], ks[2][3])};
        *(LAS u32x2*)(kc + 2 * (128 * (hq >> 1) + 4 * (hq & 1) + 8 * col)) = p0;
        if (hq < 2) *(LAS u32x2*)(kc + 2 * (256 + 64 * hq + 4 * col)) = p1;
        else { *(LAS unsigned*)(kc + 2 * (384 + 64 * (hq - 2) + 2 * col)) = p1[0]; *(LAS unsigned*)(kc + 2 * (416 + 64 * (hq - 2) + 2 * col)) = p1[1]; }
        if (hq < 2) {
            lptr k2p = kc + 2 * (512 + 64 * hq + col);
            *(LAS unsigned short*)k2p = (unsigned short)p2[0]; *(LAS unsigned short*)(k2p + 32) = (unsigned short)(p2[0] >> 16);
            *(LAS unsigned short*)(k2p + 64) = (unsigned short)p2[1]; *(LAS unsigned short*)(k2p + 96) = (unsigned short)(p2[1] >> 16);
        } else if (lane == 32) *(LAS unsigned short*)(kc + 2 * 640) = (unsigned short)p2[0];
        else if (lane >= 48 && lane < 55) *(LAS unsigned short*)(kc + 2 * (641 + lane - 48)) = 0;
        asm volatile("" ::: "memory");
        STAMP(13);
        u32x4 o[4];
#pragma unroll
        for (int i = 0; i < 4; ++i)
#pragma unroll
            for (int k = 0; k < 4; ++k) {
                const unsigned tk = tb[i][k];
                const unsigned lo = *(LAS unsigned short*)(kc + (tk & 0xffffu)), hi = *(LAS unsigned short*)(kc + (tk >> 16));
                o[i][k] = lo | (hi << 16);
            }
        lptr dst = sm + cv_k(0) + w * KG_WAVE;
#pragma unroll
        for (int i = 0; i < 4; ++i) {
            *(LAS u32x4*)(dst + (lane + 64 * i) * 16) = o[i];
            unsigned nx = (unsigned)__builtin_amdgcn_update_dpp(0, (int)o[i][0], 0x130, 0xF, 0xF, false);
            const unsigned wrap = i < 3 ? (unsigned)__builtin_amdgcn_readlane((int)o[i < 3 ? i + 1 : 3][0], 0) : 0u;
            nx = lane == 63 ? wrap : nx;
            const u32x4 od = {__builtin_amdgcn_alignbit(o[i][1], o[i][0], 16), __builtin_amdgcn_alignbit(o[i][2], o[i][1], 16),
                              __builtin_amdgcn_alignbit(o[i][3], o[i][2], 16), __builtin_amdgcn_alignbit(nx, o[i][3], 16)};
            *(LAS u32x4*)(dst + 4160 + (lane + 64 * i) * 16) = od;
        }
        if (lane < 8) *(LAS u32x4*)(dst + (lane >> 2) * 4160 + 4096 + (lane & 3) * 16) = (u32x4){0u, 0u, 0u, 0u};
        STAMP(14);
    } else {
#pragma unroll
        for (int mt = 0; mt < 3; ++mt) *(LAS f32x4*)(tab + (mt * 64 + lane) * 16) = a2[mt];
        asm volatile("s_waitcnt lgkmcnt(0)" ::: "memory");
        if (lane == 0) __hip_atomic_fetch_add(cnt, 1u, __ATOMIC_RELAXED, __HIP_MEMORY_SCOPE_WORKGROUP);
        const int gm = tid - 256, ln = gm >> 1, sw = ((ln >> 1) & 3) << 1;
        u32x4 xc[4];
#pragma unroll
        for (int ch = 0; ch < 4; ++ch) {
#pragma unroll
            for (int k = 0; k < 4; ++k) xc[ch][k] = __builtin_amdgcn_perm(xg[2 * k + 1][ch >> 1], xg[2 * k][ch >> 1], (ch & 1) ? 0x07060302u : 0x05040100u);
            *(LAS u32x4*)(sm + CV_X + ln * 128 + 16 * ((2 * ch + (gm & 1)) ^ sw)) = xc[ch];
        }
        if (gm < 128) *(LAS u32x4*)(sm + CV_U + gm * 16) = (u32x4){0u, 0u, 0u, 0u};
        wait_flags<16, FL_LN_S>(fl + (b & 7) * FL_REP + FL_LN + 16 * (w - 4) * FL_LN_S, tag, lane);
        STAMPL(25);
        f32x4 r0, r1, n0, n1;
        asm volatile("global_load_dwordx4 %0, %4, %5 sc1\n\tglobal_load_dwordx4 %1, %4, %5 offset:16 sc1\n\tglobal_load_dwordx4 %2, %4, %6 sc1\n\tglobal_load_dwordx4 %3, %4, %6 offset:16 sc1\n\ts_waitcnt vmcnt(0)"
                     : "=&v"(r0), "=&v"(r1), "=&v"(n0), "=&v"(n1) : "v"(gm * 32), "s"(ws + OFF_S1), "s"(ws + OFF_S1 + T * 4) : "memory");
#pragma unroll
        for (int ch = 0; ch < 4; ++ch) {
            u32x4 u;
#pragma unroll
            for (int k = 0; k < 4; ++k) {
                const f32x2 rs = k < 2 ? (f32x2){r0[2 * k], r0[2 * k + 1]} : (f32x2){r1[2 * k - 4], r1[2 * k - 3]}, nm = k < 2 ? (f32x2){n0[2 * k], n0[2 * k + 1]} : (f32x2){n1[2 * k - 4], n1[2 * k - 3]};
                const f32x2 a = __builtin_elementwise_fma((f32x2){bflo(xc[ch][k]), bfhi(xc[ch][k])}, rs, nm);
                u[k] = pk_bf16(a[0], a[1]);
            }
            *(LAS u32x4*)(sm + CV_U + 2048 + ln * 128 + 16 * ((2 * ch + (gm & 1)) ^ sw)) = u;
        }
    }
    bar();
    STAMP(3); CSTAMP(16);
    if (w < 4) {
        const int n = lane & 15, h = lane >> 4;
        const lptr fb = sm + cv_k(0) + w * KG_WAVE + ((n & 1) ? 0 : 4160) + 4 * (((2031 - n) >> 1) + 4 * h) - 512 * 7;
        f32x4 acc[8] = {};
        bf16x8 Fa[2][8], Ga[2][8];
        lptr gp, fp[4];
        auto addr = [&](int rr) {
            const int ln = n + (h >> 1) + 15 - 2 * rr;
            gp = sm + CV_U + 128 * ln + 16 * ((2 * w + (h & 1)) ^ (((ln >> 1) & 3) << 1));
#pragma unroll
            for (int k = 0; k < 4; ++k) { fp[k] = fb - 64 * rr + 1024 * k; asm volatile("" : "+v"(fp[k])); }
        };
        auto ldG = [&](int Q, int c) { Ga[Q][c] = *(LAS bf16x8*)(gp + 2048 * c); };
        u32x4 ftmp[2];
        auto ldF = [&](int Q, int a, int half) {
            lptr q = fp[(7 - a) >> 1] + 512 * ((7 - a) & 1) + 8 * half;
            ftmp[a & 1][2 * half] = *(LAS unsigned*)q; ftmp[a & 1][2 * half + 1] = *(LAS unsigned*)(q + 4);
            if (half) Fa[Q][a] = __builtin_bit_cast(bf16x8, ftmp[a & 1]);
        };
        const int so = n * 128 + 16 * ((2 * w + (h >> 1)) ^ (((n >> 1) & 3) << 1)) + 8 * (h & 1);
        u32x2 uv[8], xv[8];
        addr(0);
#pragma unroll
        for (int c = 0; c < 8; ++c) { ldF(0, c, 0); ldF(0, c, 1); ldG(0, c); }
        SB();
#pragma unroll
        for (int rr = 0; rr < 8; ++rr) {
            const int Q = rr & 1;
            int i = 0;
#pragma unroll
            for (int a = 0; a < 8; ++a)
#pragma unroll
                for (int c = a; c < 8; ++c) {
                    acc[c] = __builtin_amdgcn_mfma_f32_16x16x32_bf16(Fa[Q][a], Ga[Q][c - a], acc[c], 0, 0, 0); PIN(acc[c]); SB();
                    if (rr < 7) {
                        if (i == 0) { addr(rr + 1); SB(); }
                        else if (i <= 24) { const int k = (i - 1) / 3, m = (i - 1) % 3; if (m == 0) ldG(Q ^ 1, k); else ldF(Q ^ 1, k, m - 1); SB(); }
                    } else if (i < 16) {
                        if (i & 1) xv[i >> 1] = *(LAS u32x2*)(sm + CV_X + so + 2048 * (i >> 1)); else uv[i >> 1] = *(LAS u32x2*)(sm + CV_U + 2048 + so + 2048 * (i >> 1));
                        SB();
                    }
                    ++i;
                }
        }
        STAMP(4); CSTAMP(17);
        const int d = d0 + w;
        lptr yb = sm + cv_k(0) + w * KG_WAVE;
        const float dsk = (d & 1) ? bfhi(dsk_raw) : bflo(dsk_raw);
        const f32x2 dk2 = {dsk, dsk};
#pragma unroll
        for (int c2 = 0; c2 < 8; c2 += 2) {
            f32x2 hf[4];
#pragma unroll
            for (int i = 0; i < 4; ++i) { const unsigned e = uv[c2 + (i >> 1)][i & 1]; hf[i] = __builtin_elementwise_fma((f32x2){bflo(e), bfhi(e)}, dk2, (f32x2){acc[c2 + (i >> 1)][2 * (i & 1)], acc[c2 + (i >> 1)][2 * (i & 1) + 1]}); }
#pragma unroll
            for (int i = 0; i < 4; ++i) { const unsigned e = xv[c2 + (i >> 1)][i & 1]; hf[i] += (f32x2){bflo(e), bfhi(e)}; }
#pragma unroll
            for (int j = 0; j < 2; ++j) *(LAS u32x2*)(yb + (256 * (c2 + j) + 16 * n + 4 * h) * 2) = (u32x2){pk_bf16(hf[2 * j][0], hf[2 * j][1]), pk_bf16(hf[2 * j + 1][0], hf[2 * j + 1][1])};
        }
        asm volatile("" ::: "memory");
#pragma unroll
        for (int j = 0; j < 4; ++j) st_dev_o(ws + OFF_HT + (size_t)d * (T * 2) + j * 1024, lane * 16, *(LAS u32x4*)(yb + (64 * j + lane) * 16));
        STAMP(27);
        wait_vm<0>();
    } else if (w == 6) wait_flags<96, FL_W_S>(fl + (b & 7) * FL_REP + FL_WG, tag, lane);
    bar();
    if (tid < 8) __hip_atomic_fetch_add(fl + FL_HC + (tid * 32 + (b & 31)) * 32, 1u, __ATOMIC_RELAXED, __HIP_MEMORY_SCOPE_AGENT);
    STAMP(5);
    return tag;
}

__device__ __forceinline__ unsigned spare_wg(const Params& P, int j, int cb, lptr sm PROF_ARG) {
    const int tid = threadIdx.x, lane = tid & 63, w = uni(tid >> 6);
    unsigned* fl = (unsigned*)(P.ws + OFF_FL);
    unsigned tag;
    if (j < 64) tag = ln1_unit(j, P.x, P.ws PROF_FWD); else tag = epoch_tag(P.ws);
    if (j < 64) {
        wait_vm<0>();
        bar();
        if (tid < 8) set_flag_rep(fl + FL_LN + j * FL_LN_S, tag, tid);
    }
    STAMP(1);
    const int n_g = j >= 64 ? 2 : (j < 32 ? 1 : 0), g0 = j >= 64 ? 2 * (j - 64) : 64 + j;
    for (int i = 0; i < n_g; ++i) {
        wgtile_unit(g0 + i, P.wg, P.wu, P.ws, sm);
        wait_vm<0>();
        bar();
        if (tid < 8) set_flag_rep(fl + FL_WG + (g0 + i) * FL_W_S, tag, tid);
    }
    STAMP(2);
    if (j >= 64 || (j >= 32 && j < 48)) {
        const int fb = j >= 64 ? j - 64 : j;
        wdtile_unit(fb, P.wd, P.ws, sm);
        wait_vm<0>();
        bar();
        if (tid < 8) set_flag_rep(fl + FL_WD + fb * FL_W_S, tag, tid);
    }
    if (w == 6) wait_flags<96, FL_W_S>(fl + (blockIdx.x & 7) * FL_REP + FL_WG, tag, lane);
    bar();
    STAMP(5);
    return tag;
}

template <int WC>
struct G1Compute {
    f32x16 acc[3][2];
    bf16x8 Af[2][3], Bf[2][2];
    template <int P> __device__ __forceinline__ void mm(int ft, int tt) {
        acc[ft][tt] = __builtin_amdgcn_mfma_f32_32x32x16_bf16(Af[P][ft], Bf[P][tt], acc[ft][tt], 0, 0, 0); PIN(acc[ft][tt]); SB();
    }
    __device__ __forceinline__ static bf16x8 rdB(lptr p) { return __builtin_bit_cast(bf16x8, tr_read2(p, 8 * 256)); }
    template <int P, int KK, bool MORE, bool BAR>
    __device__ __forceinline__ void substep(lptr pa, const lptr (&pb)[2]) {
        mm<P>(0, 0);
        if (BAR) { sbar(); SB(); }
        if (MORE) { Bf[P ^ 1][0] = rdB(pb[0] + KK * 16 * 256); Af[P ^ 1][0] = *(LAS bf16x8*)(pa + KK * 6144); SB(); }
        mm<P>(0, 1);
        if (MORE) { Bf[P ^ 1][1] = rdB(pb[1] + KK * 16 * 256); Af[P ^ 1][1] = *(LAS bf16x8*)(pa + KK * 6144 + 1024); SB(); }
        mm<P>(1, 0);
        if (MORE) { Af[P ^ 1][2] = *(LAS bf16x8*)(pa + KK * 6144 + 2048); SB(); }
        mm<P>(1, 1);
        mm<P>(2, 0);
        mm<P>(2, 1);
    }
};

template <int WC>
__device__ __forceinline__ void g1_compute(lptr sm, char* __restrict__ ws, int wr, int lane, int t0, int cb PROF_ARG) {
    const int g = lane >> 4, q = (lane & 15) >> 2, p = lane & 3, hh = g >> 1, krow = 4 * hh + q;
    G1Compute<WC> C;
#pragma unroll
    for (int ft = 0; ft < 3; ++ft)
#pragma unroll
        for (int tt = 0; tt < 2; ++tt) C.acc[ft][tt] = (f32x16){};
    const lptr a0 = sm + G1_W + 3 * WC * 1024 + lane * 16;
    lptr b0[2];
#pragma unroll
    for (int tt = 0; tt < 2; ++tt) b0[tt] = sm + krow * 256 + (((2 * wr + tt) ^ q) * 64) + (g & 1) * 32 + p * 8;
    sbar();
    STAMP(7); CSTAMP(18);
#pragma unroll
    for (int tt = 0; tt < 2; ++tt) C.Bf[0][tt] = C.rdB(b0[tt]);
#pragma unroll
    for (int ft = 0; ft < 3; ++ft) C.Af[0][ft] = *(LAS bf16x8*)(a0 + ft * 1024);
    SB();
    lptr pa = a0, pb[2] = {b0[0], b0[1]};
    int nb = 0;
#pragma unroll 1
    for (int s = 0; s < 19; ++s) {
        C.template substep<0, 1, true, false>(pa, pb);
        nb = nb + G1_SS == G1_NS * G1_SS ? 0 : nb + G1_SS;
        pa = a0 + nb; pb[0] = b0[0] + nb; pb[1] = b0[1] + nb;
        SB();
        C.template substep<1, 0, true, true>(pa, pb);
    }
    C.template substep<0, 1, true, false>(pa, pb);
    C.template substep<1, 0, false, false>(pa, pb);
    STAMP(8); CSTAMP(19);
    bar();
    char* mF = ws + OFF_MF;
    f32x2 nm2[2], k1[2], k2[2], s2[2];
#pragma unroll
    for (int tt = 0; tt < 2; ++tt) {
        const f32x2 sq = *(LAS f32x2*)(sm + G1_ST + (32 * (2 * wr + tt) + (lane & 31)) * 8);
        const float rs = __builtin_amdgcn_rcpf(sq[1]);
        nm2[tt] = (f32x2){-sq[0], -sq[0]};
        k1[tt] = (f32x2){-2.302208198f * rs, -2.302208198f * rs};
        k2[tt] = (f32x2){-0.1029432397f * rs * rs * rs, -0.1029432397f * rs * rs * rs};
        s2[tt] = (f32x2){sq[1] * sq[1], sq[1] * sq[1]};
    }
    const unsigned mo = (unsigned)((((t0 + 64 * wr + (lane & 31)) >> 4) * 48 * 64 + ((lane & 15) + 16 * hh)) * 16);
#pragma unroll
    for (int ft = 0; ft < 3; ++ft) {
        const int fl = 16 * (3 * WC + ft) + 4 * hh;
        lptr cp = sm + G1_CS + 3072 + fl * 4;
        const f32x4 cg0 = *(LAS f32x4*)cp, cg1 = *(LAS f32x4*)(cp + 32), cu0 = *(LAS f32x4*)(cp + 384), cu1 = *(LAS f32x4*)(cp + 416);
        const int ftg = 6 * cb + 3 * WC + ft;
        const char* mb = mF + (size_t)((ftg >> 1) * 64 + 32 * (ftg & 1)) * 16;
#pragma unroll
        for (int tt = 0; tt < 2; ++tt) {
            f32x2 G[4], U[4], E[4];
#pragma unroll
            for (int r = 0; r < 4; ++r) {
                const f32x2 cg = r < 2 ? (f32x2){cg0[2 * r], cg0[2 * r + 1]} : (f32x2){cg1[2 * r - 4], cg1[2 * r - 3]};
                G[r] = __builtin_elementwise_fma(nm2[tt], cg, (f32x2){C.acc[ft][tt][2 * r], C.acc[ft][tt][2 * r + 1]}); PIN(G[r]);
            }
            SB();
#pragma unroll
            for (int r = 0; r < 4; ++r) { E[r] = G[r] * G[r]; PIN(E[r]); }
            SB();
#pragma unroll
            for (int r = 0; r < 4; ++r) { E[r] = __builtin_elementwise_fma(E[r], k2[tt], k1[tt]); PIN(E[r]); }
            SB();
#pragma unroll
            for (int r = 0; r < 4; ++r) { E[r] = G[r] * E[r]; PIN(E[r]); }
            SB();
#pragma unroll
            for (int r = 0; r < 4; ++r) {
                const f32x2 cu = r < 2 ? (f32x2){cu0[2 * r], cu0[2 * r + 1]} : (f32x2){cu1[2 * r - 4], cu1[2 * r - 3]};
                E[r] = (f32x2){__builtin_amdgcn_exp2f(E[r][0]), __builtin_amdgcn_exp2f(E[r][1])}; PIN(E[r]); SB();
                U[r] = __builtin_elementwise_fma(nm2[tt], cu, (f32x2){C.acc[ft][tt][8 + 2 * r], C.acc[ft][tt][9 + 2 * r]}); PIN(U[r]); SB();
            }
#pragma unroll
            for (int r = 0; r < 4; ++r) { E[r] = __builtin_elementwise_fma(E[r], s2[tt], s2[tt]); PIN(E[r]); }
            SB();
#pragma unroll
            for (int r = 0; r < 4; ++r) {
                E[r] = (f32x2){__builtin_amdgcn_rcpf(E[r][0]), __builtin_amdgcn_rcpf(E[r][1])}; PIN(E[r]); SB();
                G[r] = G[r] * U[r]; PIN(G[r]); SB();
            }
#pragma unroll
            for (int r = 0; r < 4; ++r) { G[r] = G[r] * E[r]; PIN(G[r]); }
            SB();
            u32x4 o;
#pragma unroll
            for (int r = 0; r < 4; ++r) o[r] = pk_bf16(G[r][0], G[r][1]);
            st_dev_o(mb, mo + tt * (2 * 48 * 1024), o);
            SB();
        }
    }
}

__device__ __forceinline__ void g1_loader(char* __restrict__ ws, lptr sm, int lw, int lane, int t0, int cb, int t2, int db, unsigned tag) {
    const unsigned sm0 = (unsigned)(size_t)sm;
    {
        const char* hT = ws + OFF_HT;
        const char* wsrc = ws + OFF_WG + (size_t)cb * 40 * 6144 + 3 * lw * 1024;
        unsigned vh[2];
#pragma unroll
        for (int j = 0; j < 2; ++j) {
            const int o = (2 * lw + j) * 1024 + lane * 16, k = o >> 8, c = ((o >> 4) & 15) ^ ((k & 3) << 2);
            vh[j] = (unsigned)(k * (T * 2) + t0 * 2 + c * 16);
        }
        const unsigned vl = lane * 16;
        unsigned long long* st = (unsigned long long*)(ws + OFF_ST);
        auto stage = [&](int s) {
            const unsigned lb = sm0 + (s % G1_NS) * G1_SS;
            const char* hs = hT + (size_t)s * 32 * (T * 2);
            const char* wsx = wsrc + (size_t)s * 12288;
            dma16s(hs, vh[0], lb + (2 * lw) * 1024);
            if (s >= 4) dma16s(wsx, vl, lb + G1_W + (3 * lw) * 1024);
            dma16s(hs, vh[1], lb + (2 * lw + 1) * 1024);
            if (s >= 4) { dma16s(wsx + 1024, vl, lb + G1_W + (3 * lw + 1) * 1024); dma16s(wsx + 2048, vl, lb + G1_W + (3 * lw + 2) * 1024); }
        };
        u32x4 gv[3];
        unsigned long long g0, g1;
        float cs[8], sv[16];
#pragma unroll
        for (int s = 0; s < 4; ++s) stage(s);
        {
            const char* hp = hT + (size_t)(160 * lw) * (T * 2) + (t0 + 8 * cb) * 2;
            const unsigned go = (unsigned)lane * (T * 2);
            asm volatile("global_load_dwordx4 %0, %1, %2 sc1" : "=v"(gv[0]) : "v"(go), "s"(hp) : "memory");
            asm volatile("global_load_dwordx4 %0, %1, %2 sc1" : "=v"(gv[1]) : "v"(go), "s"(hp + (size_t)64 * (T * 2)) : "memory");
            asm volatile("global_load_dwordx4 %0, %1, %2 sc1" : "=v"(gv[2]) : "v"(lane < 32 ? go : 0u), "s"(hp + (size_t)128 * (T * 2)) : "memory");
        }
        wait_vm<9>();
        sbar();
#pragma unroll
        for (int s = 0; s < 19; ++s) {
            const int last = (s + 3) < 19 ? (s + 3) : 19;
            if (s == 3) asm volatile("s_waitcnt vmcnt(10)" : "+v"(gv[0]), "+v"(gv[1]), "+v"(gv[2]) :: "memory");
            else if (s == 17) asm volatile("s_waitcnt vmcnt(13)" : "+v"(g0), "+v"(g1) :: "memory");
            else if (s == 18) wait_vm<8>();
            else wait_vm_rt<30>(s == 0 ? 7 : (s == 1 ? 10 : (s == 2 ? 13 : 5 * (last - (s + 1)) + (s == 15 || s == 16 ? 2 : 0))));
            sbar();
            if (s + 4 < 20) stage(s + 4);
            if (s == 11 && lw < 3) {
                lptr cp = sm + G1_CS + lw * 256 + lane * 4;
                *(LAS float*)(cp + 3072) = (*(LAS float*)cp + *(LAS float*)(cp + 768)) + (*(LAS float*)(cp + 1536) + *(LAS float*)(cp + 2304));
                asm volatile("s_waitcnt lgkmcnt(0)" ::: "memory");
            }
            if (s == 16) {
#pragma unroll
                for (int j = 0; j < 3; ++j) {
                    const int i = lw + 4 * j < 10 ? lw + 4 * j : lw;
                    const int o = i * 1024 + lane * 16, row = o >> 7, c = ((o >> 4) & 7) ^ (((row >> 1) & 3) << 1);
                    dma16s(ws + OFF_HT, (unsigned)((80 * db + row) * (T * 2) + t2 * 2 + c * 16), sm0 + G2_H + i * 1024);
                }
            }
            if (s == 16 || s == 18) {
                const int s2 = s == 16 ? 0 : 1;
                const char* wd = ws + OFF_WD + (size_t)(4 * s2 + lw) * (40 * 1024) + (size_t)(5 * db) * 1024;
#pragma unroll
                for (int j = 0; j < 5; ++j) dma16s(wd + j * 1024, vl, sm0 + s2 * G2_SS + G2_W + (5 * lw + j) * 1024);
            }
            if (s == 14) {
                asm volatile("global_load_dwordx2 %0, %1, %2 sc1" : "=v"(g0) : "v"(lane * 8), "s"(st + t0) : "memory");
                asm volatile("global_load_dwordx2 %0, %1, %2 sc1" : "=v"(g1) : "v"(lane * 8), "s"(st + t0 + 64) : "memory");
            }
            if (s >= 3 && s <= 6) {
#pragma unroll
                for (int e = 2 * (s - 3); e < 2 * (s - 3) + 2; ++e) {
                    const unsigned c32 = (unsigned)__builtin_amdgcn_readlane((int)gv[0][e >> 1], 0);
                    cs[e] = (e & 1) ? bfhi(c32) : bflo(c32);
                    const float d0 = ((e & 1) ? bfhi(gv[0][e >> 1]) : bflo(gv[0][e >> 1])) - cs[e], d1 = ((e & 1) ? bfhi(gv[1][e >> 1]) : bflo(gv[1][e >> 1])) - cs[e];
                    const float d2 = lane < 32 ? ((e & 1) ? bfhi(gv[2][e >> 1]) : bflo(gv[2][e >> 1])) - cs[e] : 0.f;
                    sv[e] = d0 + d1 + d2; sv[8 + e] = d0 * d0 + d1 * d1 + d2 * d2;
                    PIN(sv[e]); PIN(sv[8 + e]);
                }
            }
            if (s == 7) {
#pragma unroll
                for (int j = 0; j < 8; ++j) {
                    const auto r = __builtin_amdgcn_permlane32_swap(__float_as_uint(sv[j]), __float_as_uint(sv[j + 8]), false, false);
                    sv[j] = __uint_as_float(r[0]) + __uint_as_float(r[1]); PIN(sv[j]);
                }
            }
            if (s == 8) {
#pragma unroll
                for (int k = 0; k < 4; ++k) {
                    const auto r = __builtin_amdgcn_permlane16_swap(__float_as_uint(sv[k]), __float_as_uint(sv[k + 4]), false, false);
                    sv[k] = __uint_as_float(r[0]) + __uint_as_float(r[1]);
                }
#pragma unroll
                for (int k = 0; k < 4; ++k) { sv[k] = row16_sum(sv[k]); PIN(sv[k]); }
            }
            if (s == 9) {
                if ((lane & 15) == 0) *(LAS f32x4*)(sm + G1_SP + lw * 96 + (lane >> 4) * 16) = (f32x4){sv[0], sv[1], sv[2], sv[3]};
                if (lane == 0) { *(LAS f32x4*)(sm + G1_SP + lw * 96 + 64) = (f32x4){cs[0], cs[1], cs[2], cs[3]}; *(LAS f32x4*)(sm + G1_SP + lw * 96 + 80) = (f32x4){cs[4], cs[5], cs[6], cs[7]}; }
                asm volatile("s_waitcnt lgkmcnt(0)" ::: "memory");
            }
            if (s == 10 && lw == 3 && lane < 8) {
                float pm[4], m2 = 0.f, mean = 0.f;
#pragma unroll
                for (int i = 0; i < 4; ++i) {
                    const float a = *(LAS float*)(sm + G1_SP + i * 96 + lane * 4) * (1.0f / 160.0f), q = *(LAS float*)(sm + G1_SP + i * 96 + 32 + lane * 4);
                    pm[i] = *(LAS float*)(sm + G1_SP + i * 96 + 64 + lane * 4) + a;
                    m2 += q - 160.0f * a * a; mean += 0.25f * pm[i];
                }
#pragma unroll
                for (int i = 0; i < 4; ++i) m2 += 160.0f * (pm[i] - mean) * (pm[i] - mean);
                const float var = fmaxf(m2 * (1.0f / (float)D), 0.f);
                const unsigned mb = pk_bf16(mean, rbf(sqrtf(rbf(rbf(var) + 1e-5f))));
                __hip_atomic_store(st + t0 + 8 * cb + lane, ((unsigned long long)tag << 32) | mb, __ATOMIC_RELAXED, __HIP_MEMORY_SCOPE_AGENT);
            }
        }
        if (lw == 0) {
            for (unsigned spins = 0; !__all((unsigned)(g0 >> 32) == tag && (unsigned)(g1 >> 32) == tag) && spins < (1u << 18); ++spins) {
                __builtin_amdgcn_s_sleep(2);
                g0 = __hip_atomic_load(st + t0 + lane, __ATOMIC_RELAXED, __HIP_MEMORY_SCOPE_AGENT);
                g1 = __hip_atomic_load(st + t0 + 64 + lane, __ATOMIC_RELAXED, __HIP_MEMORY_SCOPE_AGENT);
            }
            *(LAS f32x2*)(sm + G1_ST + lane * 8) = (f32x2){bflo((unsigned)g0), bfhi((unsigned)g0)};
            *(LAS f32x2*)(sm + G1_ST + (64 + lane) * 8) = (f32x2){bflo((unsigned)g1), bfhi((unsigned)g1)};
        }
        bar();
    }
}

template <int ND>
__device__ __forceinline__ void g2_compute(lptr sm, int lane, int th, int dl0, int t0, int d0, bf16_t* __restrict__ out PROF_ARG) {
    f32x4 acc[ND][2] = {};
    bf16x8 A[2][ND], B[2][2];
    const lptr pb0 = sm + lane * 16 + 2 * th * 4096, pa0 = sm + lane * 16 + G2_W + dl0 * 1024;
    auto ld = [&](int P, lptr pa, lptr pb, int fl) {
#pragma unroll
        for (int tt = 0; tt < 2; ++tt) B[P][tt] = *(LAS bf16x8*)(pb + (tt * 4 + fl) * 1024);
#pragma unroll
        for (int dt = 0; dt < ND; ++dt) A[P][dt] = *(LAS bf16x8*)(pa + (fl * 5 + dt) * 1024);
        SB();
    };
    auto mm = [&](int P) {
#pragma unroll
        for (int dt = 0; dt < ND; ++dt)
#pragma unroll
            for (int tt = 0; tt < 2; ++tt) { acc[dt][tt] = __builtin_amdgcn_mfma_f32_16x16x32_bf16(A[P][dt], B[P][tt], acc[dt][tt], 0, 0, 0); PIN(acc[dt][tt]); SB(); }
    };
    sbar();
    STAMP(11); CSTAMP(20);
    ld(0, pa0, pb0, 0);
    lptr pa = pa0, pb = pb0;
    int nb = 0;
#pragma unroll 1
    for (int s = 0; s < 12; ++s) {
        ld(1, pa, pb, 1); mm(0);
        ld(0, pa, pb, 2); mm(1);
        ld(1, pa, pb, 3); mm(0);
        if (s < 11) {
            nb = nb + G2_SS == G2_NS * G2_SS ? 0 : nb + G2_SS;
            pa = pa0 + nb; pb = pb0 + nb;
            bar();
            ld(0, pa, pb, 0);
        }
        mm(1);
    }
    STAMP(12); CSTAMP(21);
    bar();
    const int g = lane >> 4, q = (lane & 15) >> 2, p = lane & 3;
#pragma unroll
    for (int dt = 0; dt < ND; ++dt)
#pragma unroll
        for (int tt = 0; tt < 2; ++tt) {
            const int row = 16 * (dl0 + dt) + 4 * g + q, seg = (2 * th + tt) ^ ((row >> 1) & 3);
            const u32x2 hv = tr_read(sm + G2_H + row * 128 + seg * 32 + p * 8);
            const unsigned o01 = pk_bf16(acc[dt][tt][0], acc[dt][tt][1]), o23 = pk_bf16(acc[dt][tt][2], acc[dt][tt][3]);
            const u32x2 r = {pk_bf16(bflo(hv[0]) + bflo(o01), bfhi(hv[0]) + bfhi(o01)), pk_bf16(bflo(hv[1]) + bflo(o23), bfhi(hv[1]) + bfhi(o23))};
            *(LAS u32x2*)(sm + (16 * (2 * th + tt) + (lane & 15)) * 176 + (16 * (dl0 + dt) + 4 * g) * 2) = r;
        }
}

extern "C" __global__ void s4d_geglu_block_end();
extern "C" __global__ __launch_bounds__(512, 2) void s4d_geglu_block(Params P PROF_ARG) {
    extern __shared__ __attribute__((aligned(16))) char smem[];
    lptr sm = (lptr)smem;
    STAMP(0);
    if (uni(threadIdx.x >> 6) == 7) {
        unsigned long long pc; unsigned off, len;
        asm volatile("s_getpc_b64 %0\n.Lpc_anchor_%=:\n\ts_mov_b32 %1, .Lpc_anchor_%=-s4d_geglu_block\n\ts_mov_b32 %2, s4d_geglu_block_end-s4d_geglu_block" : "=s"(pc), "=s"(off), "=s"(len));
        const unsigned o = (blockIdx.x >> 3) * 2048 + (threadIdx.x & 63) * 32;
        if (o < len) dma4((const void*)(pc - off), o, (unsigned)(size_t)sm + LDS_BYTES - 256);
    }
    const int tid = threadIdx.x, lane = tid & 63, w = uni(tid >> 6), b = blockIdx.x;
    const unsigned sm0 = (unsigned)(size_t)sm;
    char* ws = P.ws;
    unsigned* fl = (unsigned*)(ws + OFF_FL);
    unsigned tag;
    const int grp = b & 7, idx = b >> 3, grp0 = grp;
    const int rb = (grp & 1) * 8 + (idx & 7), cb = (grp >> 1) * 4 + (idx >> 3);
    const int tb = (grp >> 1) * 8 + (idx & 7), db = (grp & 1) * 4 + (idx >> 3);

    if (b < 160) tag = conv_wg(P, b, cb, sm PROF_FWD); else tag = spare_wg(P, b - 160, cb, sm PROF_FWD);

    if (w >= 4) g1_prefetch_w(ws, sm, cb, w & 3, lane);
    if (w == 4) wait_flags<32, 32>(fl + FL_HC + grp0 * 32 * 32, (tag << 3) + 5u, lane);
    if (w == 7) wait_flags<48, FL_W_S>(fl + grp0 * FL_REP + FL_WD, tag, lane);
    bar();
    STAMP(6);
    if (w >= 4) g1_loader(ws, sm, w & 3, lane, 128 * rb, cb, 64 * tb, db, tag);
    else if (w & 1) g1_compute<1>(sm, ws, w >> 1, lane, 128 * rb, cb PROF_FWD); else g1_compute<0>(sm, ws, w >> 1, lane, 128 * rb, cb PROF_FWD);

    const int t0 = 64 * tb, d0 = 80 * db;
    if (w >= 4) {
        const int lw = w & 3;
        const char* ms = ws + OFF_MF + (size_t)(4 * tb + lw) * 48 * 1024;
        const char* wsrc = ws + OFF_WD + (size_t)lw * (40 * 1024) + (size_t)(5 * db) * 1024;
        const unsigned vl = lane * 16;
        auto stage_w = [&](int s) {
#pragma unroll
            for (int j = 0; j < 5; ++j) dma16s(wsrc + (size_t)(4 * s) * (40 * 1024) + j * 1024, vl, sm0 + (s % G2_NS) * G2_SS + G2_W + (5 * lw + j) * 1024);
        };
        auto stage_m = [&](int s) {
#pragma unroll
            for (int j = 0; j < 4; ++j) dma16s(ms + (size_t)(4 * s + j) * 1024, vl, sm0 + (s % G2_NS) * G2_SS + (4 * lw + j) * 1024);
        };
        stage_w(2);
        sbar();
        if (lw == 0) wait_flags<16, FL_M_S>(fl + FL_M + 16 * (tb >> 1) * FL_M_S, tag, lane);
        sbar();
        STAMPL(10);
        stage_m(0); stage_m(1); stage_m(2);
        wait_vm<8>();
        sbar();
#pragma unroll
        for (int s = 0; s < 11; ++s) {
            if (s == 0) wait_vm<4>(); else if (s <= 9) wait_vm<9>(); else wait_vm<0>();
            sbar();
            if (s + 3 < 12) { stage_m(s + 3); stage_w(s + 3); }
        }
        sbar();
    } else {
        STAMP(28);
        wait_vm<0>();
        sbar();
        if (tid == 0) set_flag(fl + FL_M + (16 * rb + cb) * FL_M_S, tag);
        STAMP(9);
        sbar();
        const int th = w & 1;
        if (w >> 1) g2_compute<2>(sm, lane, th, 3, t0, d0, P.out PROF_FWD); else g2_compute<3>(sm, lane, th, 0, t0, d0, P.out PROF_FWD);
    }
    bar();
    for (int ch = tid; ch < 640; ch += 512) {
        const int row = ch / 10, c = ch % 10;
        *(u32x4*)(P.out + (size_t)(t0 + row) * D + d0 + c * 8) = *(LAS u32x4*)(sm + row * 176 + c * 16);
    }
    if (b == 0 && tid == 0) fl[FL_E] = tag;
    STAMP(15);
}

extern "C" __global__ void s4d_geglu_block_end() {}

extern "C" void kernel_launch(void* const* d_in, const int* in_sizes, int n_in, void* d_out, int out_size, void* d_ws, size_t ws_size, hipStream_t stream) {
    Params P;
    P.x = (const bf16_t*)d_in[0]; P.logA = (const bf16_t*)d_in[1]; P.Aim = (const bf16_t*)d_in[2]; P.Cre = (const bf16_t*)d_in[3]; P.Cim = (const bf16_t*)d_in[4];
    P.logdt = (const bf16_t*)d_in[5]; P.Dskip = (const bf16_t*)d_in[6]; P.wg = (const bf16_t*)d_in[8]; P.wu = (const bf16_t*)d_in[9]; P.wd = (const bf16_t*)d_in[10];
    P.ws = (char*)d_ws; P.out = (bf16_t*)d_out;
    (void)hipFuncSetAttribute((const void*)s4d_geglu_block, hipFuncAttributeMaxDynamicSharedMemorySize, LDS_BYTES);
    s4d_geglu_block<<<256, 512, LDS_BYTES, stream>>>(P);
}
```

```cpp
#include <hip/hip_runtime.h>
#include <stdint.h>

typedef unsigned short bf16_t;
typedef short s16x4 __attribute__((ext_vector_type(4)));
typedef __bf16 bf16x8 __attribute__((ext_vector_type(8)));
typedef __bf16 bf16x2 __attribute__((ext_vector_type(2)));
typedef _Float16 h16x8 __attribute__((ext_vector_type(8)));
typedef _Float16 h16x2 __attribute__((ext_vector_type(2)));
typedef float f32x2 __attribute__((ext_vector_type(2)));
typedef float f32x4 __attribute__((ext_vector_type(4)));
typedef float f32x16 __attribute__((ext_vector_type(16)));
typedef unsigned u32x2 __attribute__((ext_vector_type(2)));
typedef unsigned u32x4 __attribute__((ext_vector_type(4)));
#define LAS __attribute__((address_space(3)))
typedef LAS char* lptr;

constexpr int T = 2048, D = 640, NM = 192, F = 1536;
constexpr size_t OFF_S1 = 0;
constexpr size_t OFF_HT = OFF_S1 + (size_t)T * 8;
constexpr size_t OFF_CS = OFF_HT + (size_t)T * D * 2;
constexpr size_t OFF_MF = OFF_CS + 65536;
constexpr size_t OFF_WD = OFF_MF + (size_t)T * F * 2;
constexpr size_t OFF_WG = OFF_WD + (size_t)F * D * 2;
constexpr size_t OFF_ST = OFF_WG + (size_t)2 * F * D * 2;
constexpr size_t OFF_FL = OFF_ST + (size_t)T * 8;
constexpr size_t OFF_PROF = OFF_FL + 196608;
constexpr int FL_E = 0;
constexpr int FL_REP = 4096;
constexpr int FL_LN = FL_REP, FL_LN_S = 16;
constexpr int FL_WG = FL_LN + 1024, FL_WD = FL_WG + 96 * 8, FL_W_S = 8;
constexpr int FL_M = 64, FL_M_S = 8;
constexpr int FL_HC = 9 * FL_REP;
static_assert(FL_WD + 48 * FL_W_S <= 2 * FL_REP && FL_M + 256 * FL_M_S <= FL_REP, "flag map");

#define STAMP(i)
#define STAMPL(i)
#define CSTAMP(i)
#define PROF_ARG
#define PROF_PASS
#define PROF_FWD

__device__ __forceinline__ float bf2f(unsigned v) { return __uint_as_float(v << 16); }
__device__ __forceinline__ float bflo(unsigned v) { return __uint_as_float(v << 16); }
__device__ __forceinline__ float bfhi(unsigned v) { return __uint_as_float(v & 0xffff0000u); }
__device__ __forceinline__ unsigned pk_bf16(float lo, float hi) { f32x2 v = {lo, hi}; return __builtin_bit_cast(unsigned, __builtin_convertvector(v, bf16x2)); }
__device__ __forceinline__ float rbf(float f) { return bflo(pk_bf16(f, f)); }
__device__ __forceinline__ unsigned pk_f16(f32x2 v) { return __builtin_bit_cast(unsigned, __builtin_convertvector(v, h16x2)); }
template <int CTRL> __device__ __forceinline__ float dppf(float x) { return __int_as_float(__builtin_amdgcn_update_dpp(0, __float_as_int(x), CTRL, 0xF, 0xF, true)); }
__device__ __forceinline__ float row16_sum(float x) { x += dppf<0xB1>(x); x += dppf<0x4E>(x); x += dppf<0x141>(x); x += dppf<0x140>(x); return x; }
__device__ __forceinline__ float row16_max(float x) { x = fmaxf(x, dppf<0xB1>(x)); x = fmaxf(x, dppf<0x4E>(x)); x = fmaxf(x, dppf<0x141>(x)); x = fmaxf(x, dppf<0x140>(x)); return x; }
__device__ __forceinline__ float xsum16_32(float x) {
    const unsigned u = __float_as_uint(x);
    const auto r = __builtin_amdgcn_permlane16_swap(u, u, false, false);
    const unsigned v = __float_as_uint(__uint_as_float(r[0]) + __uint_as_float(r[1]));
    const auto q = __builtin_amdgcn_permlane32_swap(v, v, false, false);
    return __uint_as_float(q[0]) + __uint_as_float(q[1]);
}
__device__ __forceinline__ u32x2 tr_read(lptr p) { return __builtin_bit_cast(u32x2, __builtin_amdgcn_ds_read_tr16_b64_v4i16((LAS s16x4*)p)); }
__device__ __forceinline__ u32x4 tr_read2(lptr p, int off2) { const u32x2 r1 = tr_read(p), r2 = tr_read(p + off2); return (u32x4){r1[0], r1[1], r2[0], r2[1]}; }
__device__ __forceinline__ int uni(int v) { return __builtin_amdgcn_readfirstlane(v); }

__device__ __forceinline__ void dma16(const void* sbase, unsigned voff, unsigned ldsaddr) {
    asm volatile("s_mov_b32 m0, %2\n\ts_nop 0\n\tglobal_load_lds_dwordx4 %0, %1" :: "v"(voff), "s"(sbase), "s"(ldsaddr) : "memory");
}
__device__ __forceinline__ void dma4(const void* sbase, unsigned voff, unsigned ldsaddr) {
    asm volatile("s_mov_b32 m0, %2\n\ts_nop 0\n\tglobal_load_lds_dword %0, %1" :: "v"(voff), "s"(sbase), "s"(ldsaddr) : "memory");
}
template <int N> __device__ __forceinline__ void wait_vm() { asm volatile("s_waitcnt vmcnt(%0)" :: "n"(N) : "memory"); }
template <int N> __device__ __forceinline__ void wait_vm_rt(int n) {
    if (n >= N) wait_vm<N>(); else if constexpr (N > 0) wait_vm_rt<N - 1>(n);
}
__device__ __forceinline__ void bar() { asm volatile("s_waitcnt lgkmcnt(0)\n\ts_barrier" ::: "memory"); }
__device__ __forceinline__ void sbar() { asm volatile("s_barrier" ::: "memory"); }
#define SB() __builtin_amdgcn_sched_barrier(0)
#define PIN(x) asm volatile("" : "+v"(x))
__device__ __forceinline__ void st_dev(void* p, u32x4 v) { asm volatile("global_store_dwordx4 %0, %1, off sc1\n\ts_nop 1" :: "v"(p), "v"(v) : "memory"); }
__device__ __forceinline__ void st_dev_o(const void* sbase, unsigned voff, u32x4 v) { asm volatile("global_store_dwordx4 %0, %1, %2 sc1\n\ts_nop 1" :: "v"(voff), "v"(v), "s"(sbase) : "memory"); }
__device__ __forceinline__ void st_dev4(void* p, float v) { asm volatile("global_store_dword %0, %1, off sc1\n\ts_nop 1" :: "v"(p), "v"(v) : "memory"); }
__device__ __forceinline__ void dma16s(const void* sbase, unsigned voff, unsigned ldsaddr) {
    asm volatile("s_mov_b32 m0, %2\n\ts_nop 0\n\tglobal_load_lds_dwordx4 %0, %1 sc1" :: "v"(voff), "s"(sbase), "s"(ldsaddr) : "memory");
}
__device__ __forceinline__ void dma4s(const void* sbase, unsigned voff, unsigned ldsaddr) {
    asm volatile("s_mov_b32 m0, %2\n\ts_nop 0\n\tglobal_load_lds_dword %0, %1 sc1" :: "v"(voff), "s"(sbase), "s"(ldsaddr) : "memory");
}
__device__ __forceinline__ void set_flag(unsigned* f, unsigned tag) { __hip_atomic_store(f, tag, __ATOMIC_RELAXED, __HIP_MEMORY_SCOPE_AGENT); }
__device__ __forceinline__ void set_flag_rep(unsigned* f, unsigned tag, int lane) { __hip_atomic_store(f + lane * FL_REP, tag, __ATOMIC_RELAXED, __HIP_MEMORY_SCOPE_AGENT); }
template <int N, int S>
__device__ __forceinline__ void wait_flags(unsigned* f, unsigned tag, int lane) {
    for (unsigned spins = 0; spins < (1u << 18); ++spins) {
        bool ok = true;
        if (lane < N) ok = __hip_atomic_load(f + lane * S, __ATOMIC_RELAXED, __HIP_MEMORY_SCOPE_AGENT) == tag;
        if (N > 64 && lane + 64 < N) ok &= __hip_atomic_load(f + (64 + lane) * S, __ATOMIC_RELAXED, __HIP_MEMORY_SCOPE_AGENT) == tag;
        if (N > 128 && lane + 128 < N) ok &= __hip_atomic_load(f + (128 + lane) * S, __ATOMIC_RELAXED, __HIP_MEMORY_SCOPE_AGENT) == tag;
        if (__all(ok)) break;
        __builtin_amdgcn_s_sleep(1);
    }
    asm volatile("" ::: "memory");
}

__device__ __forceinline__ unsigned epoch_tag(const char* ws) {
    unsigned e;
    asm volatile("s_load_dword %0, %1, 0x0\n\ts_waitcnt lgkmcnt(0)" : "=s"(e) : "s"(ws + OFF_FL + FL_E * 4) : "memory");
    return e + 1u;
}

constexpr int G1_NS = 7, G1_SS = 20480, G1_W = 8192;
constexpr int G2_NS = 4, G2_SS = 36 * 1024, G2_W = 16 * 1024;
constexpr int G2_H = G2_NS * G2_SS;
constexpr int G1_CS = G2_H + 10240;
constexpr int G1_ST = G1_CS + 3840;
constexpr int G1_SP = G1_ST + 1024;
constexpr int LDS_BYTES = 163840;
static_assert(G1_SP + 512 <= LDS_BYTES, "LDS map");
constexpr int KG_ROW = 272;
constexpr int KG_WAVE = 57 * KG_ROW;
constexpr int CV_KCH = 8320;
__host__ __device__ constexpr int cv_k(int c) { return (4 + c) * KG_WAVE + 3072; }
__host__ __device__ constexpr int cv_kc(int c) { return cv_k(c) + CV_KCH; }
static_assert(3072 + CV_KCH + 1296 <= KG_WAVE, "per-channel area");
constexpr int CV_X = 8 * KG_WAVE + 7 * KG_ROW + 16;
constexpr int CV_U = CV_X + 128 * 128;
constexpr int CV_M = CV_U + 144 * 128;
static_assert(CV_X % 128 == 0 || true, "");
static_assert(CV_M + 32 + 256 <= 163840 && CV_X % 16 == 0, "LDS map");
constexpr int LN_ROW = 1344;

__device__ __forceinline__ void ln1_load(int j, const bf16_t* __restrict__ x, u32x4 (&v)[5]) {
    const int tid = threadIdx.x, t = 32 * j + (tid >> 4);
#pragma unroll
    for (int i = 0; i < 5; ++i) v[i] = *(const u32x4*)(x + (size_t)t * D + ((tid & 15) + 16 * i) * 8);
}
__device__ __forceinline__ void ln1_unit(int j, const u32x4 (&v)[5], char* __restrict__ ws, unsigned tag PROF_ARG) {
    const int tid = threadIdx.x, lane = tid & 63, w = uni(tid >> 6);
    f32x2 f[20];
    f32x2 s2 = {0.f, 0.f};
#pragma unroll
    for (int i = 0; i < 5; ++i)
#pragma unroll
        for (int e = 0; e < 4; ++e) { f[4 * i + e] = (f32x2){bflo(v[i][e]), bfhi(v[i][e])}; s2 += f[4 * i + e]; }
    const float mean = row16_sum(s2[0] + s2[1]) * (1.0f / (float)D);
    f32x2 q2 = {0.f, 0.f};
#pragma unroll
    for (int i = 0; i < 20; ++i) { const f32x2 dv = f[i] - mean; q2 += dv * dv; }
    const float var = row16_sum(q2[0] + q2[1]) * (1.0f / (float)D);
    const float rs = __builtin_amdgcn_rcpf(rbf(sqrtf(rbf(rbf(var) + 1e-5f)))), nm = -rbf(mean) * rs;
    u32x4 o;
#pragma unroll
    for (int i = 0; i < 4; ++i) {
        const unsigned a = (unsigned)__builtin_amdgcn_readlane((int)__float_as_uint(rs), 16 * i), c = (unsigned)__builtin_amdgcn_readlane((int)__float_as_uint(nm), 16 * i);
        o[i] = lane == 0 ? a : c;
    }
    if (lane < 2) st_dev(ws + OFF_S1 + (size_t)lane * (T * 4) + (32 * j + 4 * w) * 4, o);
    if (tid < 4) set_flag((unsigned*)(ws + OFF_FL) + FL_HC + (4 * j + tid) * 32, tag << 3);
    STAMP(24);
}

__device__ __forceinline__ void wgtile_load(int b, const bf16_t* __restrict__ wg, const bf16_t* __restrict__ wu, u32x4 (&v)[5]) {
    const int tid = threadIdx.x, j = b >> 2, kq = b & 3, c16 = tid & 15, r0 = tid >> 4;
    const bf16_t* src = (c16 < 8 ? wg : wu) + (size_t)(160 * kq + r0) * F + 64 * j + (c16 & 7) * 8;
#pragma unroll
    for (int i = 0; i < 5; ++i) v[i] = *(const u32x4*)(src + (size_t)(32 * i) * F);
}
__device__ __forceinline__ void wgtile_unit(int b, const u32x4 (&v)[5], char* __restrict__ ws, lptr sm) {
    const int tid = threadIdx.x, lane = tid & 63, w = uni(tid >> 6);
    const int j = b >> 2, kq = b & 3;
    const int c16 = tid & 15, r0 = tid >> 4;
    f32x2 a[4] = {};
#pragma unroll
    for (int i = 0; i < 5; ++i) {
        const int k = 32 * i + r0;
        *(LAS u32x4*)(sm + k * 256 + (((c16 >> 1) ^ (k & 3)) * 32) + (c16 & 1) * 16) = v[i];
#pragma unroll
        for (int e = 0; e < 4; ++e) a[e] += (f32x2){bflo(v[i][e]), bfhi(v[i][e])};
    }
#pragma unroll
    for (int e = 0; e < 4; ++e)
#pragma unroll
        for (int hf = 0; hf < 2; ++hf) {
            const float s = xsum16_32(a[e][hf]);
            if (lane < 16) *(LAS float*)(sm + 40960 + (w * 128 + c16 * 8 + 2 * e + hf) * 4) = s;
        }
    bar();
    const int g = lane >> 4, q = (lane & 15) >> 2, p = lane & 3;
    u32x4 o[5];
#pragma unroll
    for (int i = 0; i < 5; ++i) {
        const int item = w + 8 * i, ftl = item & 3, sub = item >> 2;
        const int k = 16 * sub + 4 * (g >> 1) + q;
        o[i] = tr_read2(sm + k * 256 + ((((g & 1) * 4 + ftl) ^ q) * 32) + p * 8, 8 * 256);
    }
#pragma unroll
    for (int i = 0; i < 5; ++i) {
        const int item = w + 8 * i, ftl = item & 3, sub = item >> 2;
        const int ftg = 4 * j + ftl, cb = ftg / 6, ft = ftg % 6;
        st_dev(ws + OFF_WG + ((((size_t)cb * 40 + 10 * kq + sub) * 6 + ft) * 64 + lane) * 16, o[i]);
    }
    if (tid < 128) {
        float s = 0.f;
#pragma unroll
        for (int i = 0; i < 8; ++i) s += *(LAS float*)(sm + 40960 + (i * 128 + tid) * 4);
        st_dev4(ws + OFF_CS + (size_t)(kq * 2 * F + (tid >> 6) * F + 64 * j + (tid & 63)) * 4, s);
    }
}

__device__ __forceinline__ void wdtile_load(int fb, const bf16_t* __restrict__ wd, u32x4 (&v)[5]) {
    const int tid = threadIdx.x;
#pragma unroll
    for (int i = 0; i < 5; ++i) { const int ch = tid + 512 * i, row = ch / 80, c = ch % 80; v[i] = *(const u32x4*)(wd + (size_t)(32 * fb + row) * D + c * 8); }
}
__device__ __forceinline__ void wdtile_unit(int fb, const u32x4 (&v)[5], char* __restrict__ ws, lptr sm) {
    const int tid = threadIdx.x, lane = tid & 63, w = uni(tid >> 6);
#pragma unroll
    for (int i = 0; i < 5; ++i) { const int ch = tid + 512 * i, row = ch / 80, c = ch % 80; *(LAS u32x4*)(sm + row * LN_ROW + c * 16) = v[i]; }
    bar();
    const int g = lane >> 4, q = (lane & 15) >> 2, p = lane & 3;
    u32x4 o[5];
#pragma unroll
    for (int i = 0; i < 5; ++i) o[i] = tr_read2(sm + (16 * (g >> 1) + 4 * (g & 1) + q) * LN_ROW + (16 * (w + 8 * i) + 4 * p) * 2, 8 * LN_ROW);
#pragma unroll
    for (int i = 0; i < 5; ++i) st_dev(ws + OFF_WD + (((size_t)fb * 40 + w + 8 * i) * 64 + lane) * 16, o[i]);
}

__device__ __forceinline__ f32x2 cmul(f32x2 a, f32x2 b) {
    const f32x2 t = (f32x2){a[0], a[0]} * b;
    const f32x2 bs = {-b[1], b[0]};
    return __builtin_elementwise_fma((f32x2){a[1], a[1]}, bs, t);
}
__device__ __forceinline__ unsigned cmul_h(unsigned a, unsigned b) {
    unsigned t, r;
    asm("v_pk_mul_f16 %0, %1, %2 op_sel:[0,0] op_sel_hi:[0,1]" : "=v"(t) : "v"(a), "v"(b));
    asm("v_pk_fma_f16 %0, %1, %2, %3 op_sel:[1,1,0] op_sel_hi:[1,0,1] neg_lo:[0,1,0]" : "=v"(r) : "v"(a), "v"(b), "v"(t));
    return r;
}
__device__ __forceinline__ f32x2 cexp_p(float arl, float air, float p) {
    const float mag = __builtin_amdgcn_exp2f(arl * p);
    float rev = air * p; rev -= rintf(rev);
    return (f32x2){mag * __builtin_amdgcn_cosf(rev), mag * __builtin_amdgcn_sinf(rev)};
}
struct KgenIn { unsigned ldt, la, ai, cr[3], ci[3]; };
__device__ __forceinline__ void kgen_load(KgenIn& k, int d, int third, int lane, const bf16_t* __restrict__ logA, const bf16_t* __restrict__ Aim,
                                          const bf16_t* __restrict__ Cre, const bf16_t* __restrict__ Cim, const bf16_t* __restrict__ logdt) {
    k.ldt = logdt[d]; k.la = logA[d * NM + 64 * third + lane]; k.ai = Aim[d * NM + 64 * third + lane];
#pragma unroll
    for (int i = 0; i < 3; ++i) { k.cr[i] = Cre[d * NM + 64 * i + lane]; k.ci[i] = Cim[d * NM + 64 * i + lane]; }
}
__device__ __forceinline__ void kgen_task(const KgenIn& k, int third, int lane, lptr tab, f32x4 (&acc)[3], float& unscale) {
    const float dt = rbf(expf(bf2f(k.ldt)));
    float mx = 0.f;
#pragma unroll
    for (int i = 0; i < 3; ++i) mx = fmaxf(mx, fmaxf(fabsf(bf2f(k.cr[i])), fabsf(bf2f(k.ci[i]))));
    mx = row16_max(mx);
    {
        const auto r1 = __builtin_amdgcn_permlane16_swap(__float_as_uint(mx), __float_as_uint(mx), false, false);
        mx = fmaxf(__uint_as_float(r1[0]), __uint_as_float(r1[1]));
        const auto r2 = __builtin_amdgcn_permlane32_swap(__float_as_uint(mx), __float_as_uint(mx), false, false);
        mx = fmaxf(__uint_as_float(r2[0]), __uint_as_float(r2[1]));
    }
    mx *= 2.f * dt;
    int e = (int)((__float_as_uint(mx) >> 23) & 0xff) - 126;
    e = e < -100 ? -100 : (e > 100 ? 100 : e);
    const float scale = __uint_as_float((unsigned)(127 - e) << 23);
    unscale = __uint_as_float((unsigned)(127 + e) << 23);
    const float are = -rbf(__builtin_amdgcn_exp2f(bf2f(k.la) * 1.4426950408889634f)) * dt, aim = bf2f(k.ai) * dt;
    const float crw = bf2f(third == 0 ? k.cr[0] : (third == 1 ? k.cr[1] : k.cr[2])) * dt, ciw = bf2f(third == 0 ? k.ci[0] : (third == 1 ? k.ci[1] : k.ci[2])) * dt;
    const float arl = are * 1.4426950408889634f, air = aim * 0.15915494309189535f;
    lptr base = tab + 4 * lane;
#define PUT(row, z) *(LAS unsigned*)(base + (row) * KG_ROW) = pk_f16(z)
#define PUTH(row, z) *(LAS unsigned*)(base + (row) * KG_ROW) = (z)
    const f32x2 w1 = cexp_p(arl, air, 1.f), w8 = cexp_p(arl, air, 8.f), H1 = cexp_p(arl, air, 128.f);
    f32x2 qv = {2.f * scale * crw, -2.f * scale * ciw};
    const f32x2 w8c = {w8[0], -w8[1]};
    f32x2 Ec = w1, Hc = H1;
    unsigned Eh[8], Hh[8];
    Eh[1] = pk_f16(w1); Hh[1] = pk_f16(H1);
    PUT(0, qv); PUTH(16, 0x00003c00u); PUTH(17, Eh[1]); PUTH(24, Hh[1]);
#pragma unroll
    for (int i = 1; i < 16; ++i) {
        qv = cmul(qv, w8c);
        Hc = cmul(Hc, H1);
        if (i < 7) Ec = cmul(Ec, w1);
        PUT(i, qv);
        if (i < 7) { Eh[i + 1] = pk_f16(Ec); PUTH(17 + i, Eh[i + 1]); }
        const int hi = i + 1;
        if (hi < 8) { Hh[hi] = pk_f16(Hc); PUTH(hi < 4 ? 32 + 4 * (hi - 2) : 40 + 2 * (hi - 4), Hh[hi]); } else PUT(48 + (hi - 8), Hc);
        if (i >= 7 && i < 14) PUTH(25 + (i - 7), cmul_h(Eh[i - 6], Hh[1]));
        if (i >= 7 && i < 10) PUTH(33 + (i - 7), cmul_h(Eh[2 * (i - 6)], Hh[2]));
        if (i >= 10 && i < 13) PUTH(37 + (i - 10), cmul_h(Eh[2 * (i - 9)], Hh[3]));
        if (i >= 11 && i < 15) PUTH(41 + 2 * (i - 11), cmul_h(Hh[4 + (i - 11)], Eh[4]));
    }
#undef PUTH
#undef PUT
    asm volatile("" ::: "memory");
    lptr rb = tab + (lane & 15) * KG_ROW + (lane >> 4) * 16;
#pragma unroll
    for (int mt = 0; mt < 3; ++mt) acc[mt] = (f32x4){};
#pragma unroll
    for (int ks = 0; ks < 4; ++ks) {
        const h16x8 bq = *(LAS h16x8*)(rb + ks * 64);
#pragma unroll
        for (int mt = 0; mt < 3; ++mt) {
            const h16x8 ar = *(LAS h16x8*)(rb + (16 + 16 * mt) * KG_ROW + ks * 64);
            acc[mt] = __builtin_amdgcn_mfma_f32_16x16x32_f16(ar, bq, acc[mt], 0, 0, 0);
        }
    }
#pragma unroll
    for (int mt = 0; mt < 3; ++mt) PIN(acc[mt]);
    asm volatile("s_waitcnt lgkmcnt(0)" ::: "memory");
}

__device__ __forceinline__ void g1_prefetch_w(char* __restrict__ ws, lptr sm, int cb, int lw, int lane) {
    const unsigned sm0 = (unsigned)(size_t)sm;
    const char* wsrc = ws + OFF_WG + (size_t)cb * 40 * 6144 + 3 * lw * 1024;
#pragma unroll
    for (int s = 0; s < 4; ++s)
#pragma unroll
        for (int i = 0; i < 3; ++i) dma16s(wsrc + (size_t)s * 12288 + i * 1024, lane * 16, sm0 + s * G1_SS + G1_W + (3 * lw + i) * 1024);
    if (lw < 3) {
        const int f0 = 96 * cb;
        const unsigned co = (unsigned)(((lw == 0 ? f0 + lane : (lw == 1 ? (lane < 32 ? f0 + 64 + lane : F + f0 + lane - 32) : F + f0 + 32 + lane))) * 4);
#pragma unroll
        for (int kq = 0; kq < 4; ++kq) dma4s(ws + OFF_CS + kq * 2 * F * 4, co, sm0 + G1_CS + kq * 768 + lw * 256);
    }
}

struct KTab { unsigned short v[2048]; };
constexpr int ci_of(int l) {
    if (l < 0) return 641;
    if (l < 256) return l;
    const int s = l < 512 ? 1 : (l < 1024 ? 2 : 3);
    return ((l + (1 << (s - 1)) - 1 + ((l >> s) & 1)) >> s) + 128 * s;
}
constexpr KTab make_tab() { KTab t{}; for (int y = 0; y < 2048; ++y) t.v[y] = (unsigned short)(2 * ci_of(2047 - y)); return t; }
__device__ const __attribute__((aligned(16))) KTab g_ktab = make_tab();

struct Params {
    const bf16_t *x, *logA, *Aim, *Cre, *Cim, *logdt, *Dskip, *wg, *wu, *wd;
    char* ws;
    bf16_t* out;
};

__device__ __forceinline__ unsigned conv_wg(const Params& P, int b, int cb, lptr sm PROF_ARG) {
    const int tid = threadIdx.x, lane = tid & 63, w = uni(tid >> 6);
    char* ws = P.ws;
    const int d0 = 4 * ((b & 7) * 20 + (b >> 3));
    const unsigned sm0 = (unsigned)(size_t)sm;
    unsigned* fl = (unsigned*)(ws + OFF_FL);
    KgenIn k1, k2;
    u32x4 tb[4];
    u32x2 xg[8];
    kgen_load(k1, d0 + w / 3, w % 3, lane, P.logA, P.Aim, P.Cre, P.Cim, P.logdt);
    const unsigned tag = epoch_tag(ws);
    if (w < 4) {
        const int t2 = 8 + w;
        kgen_load(k2, d0 + t2 / 3, t2 % 3, lane, P.logA, P.Aim, P.Cre, P.Cim, P.logdt);
#pragma unroll
        for (int i = 0; i < 4; ++i) tb[i] = *(const u32x4*)(&g_ktab.v[8 * (lane + 64 * i)]);
    }
    const unsigned dsk_raw = ((const unsigned*)P.Dskip)[(d0 + (w & 3)) >> 1];
    asm volatile("" : "+v"(k1.ldt), "+v"(k1.la), "+v"(k1.ai), "+v"(k1.cr[0]), "+v"(k1.cr[1]), "+v"(k1.cr[2]), "+v"(k1.ci[0]), "+v"(k1.ci[1]), "+v"(k1.ci[2]) :: "memory");
    STAMP(1);
    const lptr tab = sm + w * KG_WAVE;
    LAS unsigned* cnt = (LAS unsigned*)(sm + CV_M + 16);
    if (w == 7 && lane == 0) *cnt = 0;
    bar();
    if (w >= 4) {
#pragma unroll
        for (int i = 0; i < 8; ++i) xg[i] = *(const u32x2*)(P.x + (size_t)(8 * (tid - 256) + i) * D + d0);
        asm volatile("" ::: "memory");
    }
    f32x4 a1[3], a2[3];
    {
        int third = w % 3, chn = w / 3;
#pragma unroll 1
        for (int rnd = 0; rnd < 2; ++rnd) {
            float un;
            kgen_task(k1, third, lane, tab, a2, un);
            if (third == 0 && lane == 0) *(LAS float*)(sm + CV_M + chn * 4) = un;
            if (rnd == 1 || w >= 4) break;
#pragma unroll
            for (int mt = 0; mt < 3; ++mt) a1[mt] = a2[mt];
            k1 = k2; third = (8 + w) % 3; chn = (8 + w) / 3;
        }
    }
    if (w < 4) {
#pragma unroll
        for (int mt = 0; mt < 3; ++mt) { *(LAS f32x4*)(tab + (mt * 64 + lane) * 16) = a1[mt]; *(LAS f32x4*)(tab + 3072 + (mt * 64 + lane) * 16) = a2[mt]; }
        asm volatile("s_waitcnt lgkmcnt(0)" ::: "memory");
        if (lane == 0) __hip_atomic_fetch_add(cnt, 1u, __ATOMIC_RELAXED, __HIP_MEMORY_SCOPE_WORKGROUP);
        while (__hip_atomic_load(cnt, __ATOMIC_RELAXED, __HIP_MEMORY_SCOPE_WORKGROUP) < 8u) __builtin_amdgcn_s_sleep(0);
        asm volatile("" ::: "memory");
        STAMP(2);
        f32x4 ks[3];
        lptr pt[3];
#pragma unroll
        for (int j = 0; j < 3; ++j) { const int t = 3 * w + j; pt[j] = sm + (t & 7) * KG_WAVE + (t >> 3) * 3072 + lane * 16; }
        const float un = *(LAS float*)(sm + CV_M + w * 4);
#pragma unroll
        for (int mt = 0; mt < 3; ++mt) ks[mt] = ((*(LAS f32x4*)(pt[0] + mt * 1024) + *(LAS f32x4*)(pt[1] + mt * 1024)) + *(LAS f32x4*)(pt[2] + mt * 1024)) * un;
        lptr kc = sm + cv_kc(0) + w * KG_WAVE;
        const int hq = lane >> 4, col = lane & 15;
        const u32x2 p0 = {pk_bf16(ks[0][0], ks[0][1]), pk_bf16(ks[0][2], ks[0][3])}, p1 = {pk_bf16(ks[1][0], ks[1][1]), pk_bf16(ks[1][2], ks[1][3])};
        const u32x2 p2 = {pk_bf16(ks[2][0], ks[2][1]), pk_bf16(ks[2][2], ks[2][3])};
        *(LAS u32x2*)(kc + 2 * (128 * (hq >> 1) + 4 * (hq & 1) + 8 * col)) = p0;
        if (hq < 2) *(LAS u32x2*)(kc + 2 * (256 + 64 * hq + 4 * col)) = p1;
        else { *(LAS unsigned*)(kc + 2 * (384 + 64 * (hq - 2) + 2 * col)) = p1[0]; *(LAS unsigned*)(kc + 2 * (416 + 64 * (hq - 2) + 2 * col)) = p1[1]; }
        if (hq < 2) {
            lptr k2p = kc + 2 * (512 + 64 * hq + col);
            *(LAS unsigned short*)k2p = (unsigned short)p2[0]; *(LAS unsigned short*)(k2p + 32) = (unsigned short)(p2[0] >> 16);
            *(LAS unsigned short*)(k2p + 64) = (unsigned short)p2[1]; *(LAS unsigned short*)(k2p + 96) = (unsigned short)(p2[1] >> 16);
        } else if (lane == 32) *(LAS unsigned short*)(kc + 2 * 640) = (unsigned short)p2[0];
        else if (lane >= 48 && lane < 55) *(LAS unsigned short*)(kc + 2 * (641 + lane - 48)) = 0;
        asm volatile("" ::: "memory");
        STAMP(13);
        u32x4 o[4];
#pragma unroll
        for (int i = 0; i < 4; ++i)
#pragma unroll
            for (int k = 0; k < 4; ++k) {
                const unsigned tk = tb[i][k];
                const unsigned lo = *(LAS unsigned short*)(kc + (tk & 0xffffu)), hi = *(LAS unsigned short*)(kc + (tk >> 16));
                o[i][k] = lo | (hi << 16);
            }
        lptr dst = sm + cv_k(0) + w * KG_WAVE;
#pragma unroll
        for (int i = 0; i < 4; ++i) {
            *(LAS u32x4*)(dst + (lane + 64 * i) * 16) = o[i];
            unsigned nx = (unsigned)__builtin_amdgcn_update_dpp(0, (int)o[i][0], 0x130, 0xF, 0xF, false);
            const unsigned wrap = i < 3 ? (unsigned)__builtin_amdgcn_readlane((int)o[i < 3 ? i + 1 : 3][0], 0) : 0u;
            nx = lane == 63 ? wrap : nx;
            const u32x4 od = {__builtin_amdgcn_alignbit(o[i][1], o[i][0], 16), __builtin_amdgcn_alignbit(o[i][2], o[i][1], 16),
                              __builtin_amdgcn_alignbit(o[i][3], o[i][2], 16), __builtin_amdgcn_alignbit(nx, o[i][3], 16)};
            *(LAS u32x4*)(dst + 4160 + (lane + 64 * i) * 16) = od;
        }
        if (lane < 8) *(LAS u32x4*)(dst + (lane >> 2) * 4160 + 4096 + (lane & 3) * 16) = (u32x4){0u, 0u, 0u, 0u};
        STAMP(14);
    } else {
#pragma unroll
        for (int mt = 0; mt < 3; ++mt) *(LAS f32x4*)(tab + (mt * 64 + lane) * 16) = a2[mt];
        asm volatile("s_waitcnt lgkmcnt(0)" ::: "memory");
        if (lane == 0) __hip_atomic_fetch_add(cnt, 1u, __ATOMIC_RELAXED, __HIP_MEMORY_SCOPE_WORKGROUP);
        const int gm = tid - 256, ln = gm >> 1, sw = ((ln >> 1) & 3) << 1;
        u32x4 xc[4];
#pragma unroll
        for (int ch = 0; ch < 4; ++ch) {
#pragma unroll
            for (int k = 0; k < 4; ++k) xc[ch][k] = __builtin_amdgcn_perm(xg[2 * k + 1][ch >> 1], xg[2 * k][ch >> 1], (ch & 1) ? 0x07060302u : 0x05040100u);
            *(LAS u32x4*)(sm + CV_X + ln * 128 + 16 * ((2 * ch + (gm & 1)) ^ sw)) = xc[ch];
        }
        if (gm < 128) *(LAS u32x4*)(sm + CV_U + gm * 16) = (u32x4){0u, 0u, 0u, 0u};
        wait_flags<16, FL_LN_S>(fl + (b & 7) * FL_REP + FL_LN + 16 * (w - 4) * FL_LN_S, tag, lane);
        STAMPL(25);
        f32x4 r0, r1, n0, n1;
        asm volatile("global_load_dwordx4 %0, %4, %5 sc1\n\tglobal_load_dwordx4 %1, %4, %5 offset:16 sc1\n\tglobal_load_dwordx4 %2, %4, %6 sc1\n\tglobal_load_dwordx4 %3, %4, %6 offset:16 sc1\n\ts_waitcnt vmcnt(0)"
                     : "=&v"(r0), "=&v"(r1), "=&v"(n0), "=&v"(n1) : "v"(gm * 32), "s"(ws + OFF_S1), "s"(ws + OFF_S1 + T * 4) : "memory");
#pragma unroll
        for (int ch = 0; ch < 4; ++ch) {
            u32x4 u;
#pragma unroll
            for (int k = 0; k < 4; ++k) {
                const f32x2 rs = k < 2 ? (f32x2){r0[2 * k], r0[2 * k + 1]} : (f32x2){r1[2 * k - 4], r1[2 * k - 3]}, nm = k < 2 ? (f32x2){n0[2 * k], n0[2 * k + 1]} : (f32x2){n1[2 * k - 4], n1[2 * k - 3]};
                const f32x2 a = __builtin_elementwise_fma((f32x2){bflo(xc[ch][k]), bfhi(xc[ch][k])}, rs, nm);
                u[k] = pk_bf16(a[0], a[1]);
            }
            *(LAS u32x4*)(sm + CV_U + 2048 + ln * 128 + 16 * ((2 * ch + (gm & 1)) ^ sw)) = u;
        }
    }
    bar();
    STAMP(3); CSTAMP(16);
    if (w < 4) {
        const int n = lane & 15, h = lane >> 4;
        const lptr fb = sm + cv_k(0) + w * KG_WAVE + ((n & 1) ? 0 : 4160) + 4 * (((2031 - n) >> 1) + 4 * h) - 512 * 7;
        f32x4 acc[8] = {};
        bf16x8 Fa[2][8], Ga[2][8];
        lptr gp, fp[4];
        auto addr = [&](int rr) {
            const int ln = n + (h >> 1) + 15 - 2 * rr;
            gp = sm + CV_U + 128 * ln + 16 * ((2 * w + (h & 1)) ^ (((ln >> 1) & 3) << 1));
#pragma unroll
            for (int k = 0; k < 4; ++k) { fp[k] = fb - 64 * rr + 1024 * k; asm volatile("" : "+v"(fp[k])); }
        };
        auto ldG = [&](int Q, int c) { Ga[Q][c] = *(LAS bf16x8*)(gp + 2048 * c); };
        u32x4 ftmp[2];
        auto ldF = [&](int Q, int a, int half) {
            lptr q = fp[(7 - a) >> 1] + 512 * ((7 - a) & 1) + 8 * half;
            ftmp[a & 1][2 * half] = *(LAS unsigned*)q; ftmp[a & 1][2 * half + 1] = *(LAS unsigned*)(q + 4);
            if (half) Fa[Q][a] = __builtin_bit_cast(bf16x8, ftmp[a & 1]);
        };
        const int so = n * 128 + 16 * ((2 * w + (h >> 1)) ^ (((n >> 1) & 3) << 1)) + 8 * (h & 1);
        u32x2 uv[8], xv[8];
        addr(0);
#pragma unroll
        for (int c = 0; c < 8; ++c) { ldF(0, c, 0); ldF(0, c, 1); ldG(0, c); }
        SB();
#pragma unroll
        for (int rr = 0; rr < 8; ++rr) {
            const int Q = rr & 1;
            int i = 0;
#pragma unroll
            for (int a = 0; a < 8; ++a)
#pragma unroll
                for (int c = a; c < 8; ++c) {
                    acc[c] = __builtin_amdgcn_mfma_f32_16x16x32_bf16(Fa[Q][a], Ga[Q][c - a], acc[c], 0, 0, 0); PIN(acc[c]); SB();
                    if (rr < 7) {
                        if (i == 0) { addr(rr + 1); SB(); }
                        else if (i <= 24) { const int k = (i - 1) / 3, m = (i - 1) % 3; if (m == 0) ldG(Q ^ 1, k); else ldF(Q ^ 1, k, m - 1); SB(); }
                    } else if (i < 16) {
                        if (i & 1) xv[i >> 1] = *(LAS u32x2*)(sm + CV_X + so + 2048 * (i >> 1)); else uv[i >> 1] = *(LAS u32x2*)(sm + CV_U + 2048 + so + 2048 * (i >> 1));
                        SB();
                    }
                    ++i;
                }
        }
        STAMP(4); CSTAMP(17);
        const int d = d0 + w;
        lptr yb = sm + cv_k(0) + w * KG_WAVE;
        const float dsk = (d & 1) ? bfhi(dsk_raw) : bflo(dsk_raw);
        const f32x2 dk2 = {dsk, dsk};
#pragma unroll
        for (int c2 = 0; c2 < 8; c2 += 2) {
            f32x2 hf[4];
#pragma unroll
            for (int i = 0; i < 4; ++i) { const unsigned e = uv[c2 + (i >> 1)][i & 1]; hf[i] = __builtin_elementwise_fma((f32x2){bflo(e), bfhi(e)}, dk2, (f32x2){acc[c2 + (i >> 1)][2 * (i & 1)], acc[c2 + (i >> 1)][2 * (i & 1) + 1]}); }
#pragma unroll
            for (int i = 0; i < 4; ++i) { const unsigned e = xv[c2 + (i >> 1)][i & 1]; hf[i] += (f32x2){bflo(e), bfhi(e)}; }
#pragma unroll
            for (int j = 0; j < 2; ++j) *(LAS u32x2*)(yb + (256 * (c2 + j) + 16 * n + 4 * h) * 2) = (u32x2){pk_bf16(hf[2 * j][0], hf[2 * j][1]), pk_bf16(hf[2 * j + 1][0], hf[2 * j + 1][1])};
        }
        asm volatile("" ::: "memory");
#pragma unroll
        for (int j = 0; j < 4; ++j) st_dev_o(ws + OFF_HT + (size_t)d * (T * 2) + j * 1024, lane * 16, *(LAS u32x4*)(yb + (64 * j + lane) * 16));
        STAMP(27);
        wait_vm<0>();
    } else if (w == 6) wait_flags<96, FL_W_S>(fl + (b & 7) * FL_REP + FL_WG, tag, lane);
    bar();
    if (tid < 8) __hip_atomic_fetch_add(fl + FL_HC + (tid * 32 + (b & 31)) * 32, 1u, __ATOMIC_RELAXED, __HIP_MEMORY_SCOPE_AGENT);
    STAMP(5);
    return tag;
}

__device__ __forceinline__ unsigned spare_wg(const Params& P, int j, int cb, lptr sm PROF_ARG) {
    const int tid = threadIdx.x, lane = tid & 63, w = uni(tid >> 6);
    unsigned* fl = (unsigned*)(P.ws + OFF_FL);
    unsigned tag;
    auto publish = [&](int word) { wait_vm<0>(); bar(); if (tid < 8) set_flag_rep(fl + word, tag, tid); };
    if (j < 64) {
        u32x4 vx[5], vw[5];
        ln1_load(j, P.x, vx);
        tag = epoch_tag(P.ws);
        ln1_unit(j, vx, P.ws, tag PROF_FWD);
        if (j < 32) {
            wgtile_load(64 + j, P.wg, P.wu, vw);
            asm volatile("" ::: "memory");
            wait_vm<5>(); bar();
            if (tid < 8) set_flag_rep(fl + FL_LN + j * FL_LN_S, tag, tid);
            STAMP(1);
            wgtile_unit(64 + j, vw, P.ws, sm);
            publish(FL_WG + (64 + j) * FL_W_S);
        } else if (j < 48) {
            wdtile_load(j, P.wd, vw);
            asm volatile("" ::: "memory");
            wait_vm<5>(); bar();
            if (tid < 8) set_flag_rep(fl + FL_LN + j * FL_LN_S, tag, tid);
            STAMP(1);
            wdtile_unit(j, vw, P.ws, sm);
            publish(FL_WD + j * FL_W_S);
        } else {
            publish(FL_LN + j * FL_LN_S);
            STAMP(1);
        }
    } else {
        u32x4 va[5], vb[5], vc[5];
#define W_DELAY 100
        if (W_DELAY) { const unsigned long long t0 = wall_clock64(); while (wall_clock64() - t0 < W_DELAY) __builtin_amdgcn_s_sleep(2); }
        wgtile_load(2 * (j - 64), P.wg, P.wu, va);
        wgtile_load(2 * (j - 64) + 1, P.wg, P.wu, vb);
        wdtile_load(j - 64, P.wd, vc);
        tag = epoch_tag(P.ws);
        STAMP(1);
        wgtile_unit(2 * (j - 64), va, P.ws, sm);
        publish(FL_WG + 2 * (j - 64) * FL_W_S);
        wgtile_unit(2 * (j - 64) + 1, vb, P.ws, sm);
        publish(FL_WG + (2 * (j - 64) + 1) * FL_W_S);
        wdtile_unit(j - 64, vc, P.ws, sm);
        publish(FL_WD + (j - 64) * FL_W_S);
    }
    STAMP(2);
    if (w == 6) wait_flags<96, FL_W_S>(fl + (blockIdx.x & 7) * FL_REP + FL_WG, tag, lane);
    bar();
    STAMP(5);
    return tag;
}

template <int WC>
struct G1Compute {
    f32x16 acc[3][2];
    bf16x8 Af[2][3], Bf[2][2];
    template <int P> __device__ __forceinline__ void mm(int ft, int tt) {
        acc[ft][tt] = __builtin_amdgcn_mfma_f32_32x32x16_bf16(Af[P][ft], Bf[P][tt], acc[ft][tt], 0, 0, 0); PIN(acc[ft][tt]); SB();
    }
    __device__ __forceinline__ static bf16x8 rdB(lptr p) { return __builtin_bit_cast(bf16x8, tr_read2(p, 8 * 256)); }
    template <int P, int KK, bool MORE, bool BAR>
    __device__ __forceinline__ void substep(lptr pa, const lptr (&pb)[2]) {
        mm<P>(0, 0);
        if (BAR) { sbar(); SB(); }
        if (MORE) { Bf[P ^ 1][0] = rdB(pb[0] + KK * 16 * 256); Af[P ^ 1][0] = *(LAS bf16x8*)(pa + KK * 6144); SB(); }
        mm<P>(0, 1);
        if (MORE) { Bf[P ^ 1][1] = rdB(pb[1] + KK * 16 * 256); Af[P ^ 1][1] = *(LAS bf16x8*)(pa + KK * 6144 + 1024); SB(); }
        mm<P>(1, 0);
        if (MORE) { Af[P ^ 1][2] = *(LAS bf16x8*)(pa + KK * 6144 + 2048); SB(); }
        mm<P>(1, 1);
        mm<P>(2, 0);
        mm<P>(2, 1);
    }
};

template <int WC>
__device__ __forceinline__ void g1_compute(lptr sm, char* __restrict__ ws, int wr, int lane, int t0, int cb PROF_ARG) {
    const int g = lane >> 4, q = (lane & 15) >> 2, p = lane & 3, hh = g >> 1, krow = 4 * hh + q;
    G1Compute<WC> C;
#pragma unroll
    for (int ft = 0; ft < 3; ++ft)
#pragma unroll
        for (int tt = 0; tt < 2; ++tt) C.acc[ft][tt] = (f32x16){};
    const lptr a0 = sm + G1_W + 3 * WC * 1024 + lane * 16;
    lptr b0[2];
#pragma unroll
    for (int tt = 0; tt < 2; ++tt) b0[tt] = sm + krow * 256 + (((2 * wr + tt) ^ q) * 64) + (g & 1) * 32 + p * 8;
    sbar();
    STAMP(7); CSTAMP(18);
#pragma unroll
    for (int tt = 0; tt < 2; ++tt) C.Bf[0][tt] = C.rdB(b0[tt]);
#pragma unroll
    for (int ft = 0; ft < 3; ++ft) C.Af[0][ft] = *(LAS bf16x8*)(a0 + ft * 1024);
    SB();
    lptr pa = a0, pb[2] = {b0[0], b0[1]};
    int nb = 0;
#pragma unroll 1
    for (int s = 0; s < 19; ++s) {
        C.template substep<0, 1, true, false>(pa, pb);
        nb = nb + G1_SS == G1_NS * G1_SS ? 0 : nb + G1_SS;
        pa = a0 + nb; pb[0] = b0[0] + nb; pb[1] = b0[1] + nb;
        SB();
        C.template substep<1, 0, true, true>(pa, pb);
    }
    C.template substep<0, 1, true, false>(pa, pb);
    C.template substep<1, 0, false, false>(pa, pb);
    STAMP(8); CSTAMP(19);
    bar();
    char* mF = ws + OFF_MF;
    f32x2 nm2[2], k1[2], k2[2], s2[2];
#pragma unroll
    for (int tt = 0; tt < 2; ++tt) {
        const f32x2 sq = *(LAS f32x2*)(sm + G1_ST + (32 * (2 * wr + tt) + (lane & 31)) * 8);
        const float rs = __builtin_amdgcn_rcpf(sq[1]);
        nm2[tt] = (f32x2){-sq[0], -sq[0]};
        k1[tt] = (f32x2){-2.302208198f * rs, -2.302208198f * rs};
        k2[tt] = (f32x2){-0.1029432397f * rs * rs * rs, -0.1029432397f * rs * rs * rs};
        s2[tt] = (f32x2){sq[1] * sq[1], sq[1] * sq[1]};
    }
    const unsigned mo = (unsigned)((((t0 + 64 * wr + (lane & 31)) >> 4) * 48 * 64 + ((lane & 15) + 16 * hh)) * 16);
#pragma unroll
    for (int ft = 0; ft < 3; ++ft) {
        const int fl = 16 * (3 * WC + ft) + 4 * hh;
        lptr cp = sm + G1_CS + 3072 + fl * 4;
        const f32x4 cg0 = *(LAS f32x4*)cp, cg1 = *(LAS f32x4*)(cp + 32), cu0 = *(LAS f32x4*)(cp + 384), cu1 = *(LAS f32x4*)(cp + 416);
        const int ftg = 6 * cb + 3 * WC + ft;
        const char* mb = mF + (size_t)((ftg >> 1) * 64 + 32 * (ftg & 1)) * 16;
#pragma unroll
        for (int tt = 0; tt < 2; ++tt) {
            f32x2 G[4], U[4], E[4];
#pragma unroll
            for (int r = 0; r < 4; ++r) {
                const f32x2 cg = r < 2 ? (f32x2){cg0[2 * r], cg0[2 * r + 1]} : (f32x2){cg1[2 * r - 4], cg1[2 * r - 3]};
                G[r] = __builtin_elementwise_fma(nm2[tt], cg, (f32x2){C.acc[ft][tt][2 * r], C.acc[ft][tt][2 * r + 1]}); PIN(G[r]);
            }
            SB();
#pragma unroll
            for (int r = 0; r < 4; ++r) { E[r] = G[r] * G[r]; PIN(E[r]); }
            SB();
#pragma unroll
            for (int r = 0; r < 4; ++r) { E[r] = __builtin_elementwise_fma(E[r], k2[tt], k1[tt]); PIN(E[r]); }
            SB();
#pragma unroll
            for (int r = 0; r < 4; ++r) { E[r] = G[r] * E[r]; PIN(E[r]); }
            SB();
#pragma unroll
            for (int r = 0; r < 4; ++r) {
                const f32x2 cu = r < 2 ? (f32x2){cu0[2 * r], cu0[2 * r + 1]} : (f32x2){cu1[2 * r - 4], cu1[2 * r - 3]};
                E[r] = (f32x2){__builtin_amdgcn_exp2f(E[r][0]), __builtin_amdgcn_exp2f(E[r][1])}; PIN(E[r]); SB();
                U[r] = __builtin_elementwise_fma(nm2[tt], cu, (f32x2){C.acc[ft][tt][8 + 2 * r], C.acc[ft][tt][9 + 2 * r]}); PIN(U[r]); SB();
            }
#pragma unroll
            for (int r = 0; r < 4; ++r) { E[r] = __builtin_elementwise_fma(E[r], s2[tt], s2[tt]); PIN(E[r]); }
            SB();
#pragma unroll
            for (int r = 0; r < 4; ++r) {
                E[r] = (f32x2){__builtin_amdgcn_rcpf(E[r][0]), __builtin_amdgcn_rcpf(E[r][1])}; PIN(E[r]); SB();
                G[r] = G[r] * U[r]; PIN(G[r]); SB();
            }
#pragma unroll
            for (int r = 0; r < 4; ++r) { G[r] = G[r] * E[r]; PIN(G[r]); }
            SB();
            u32x4 o;
#pragma unroll
            for (int r = 0; r < 4; ++r) o[r] = pk_bf16(G[r][0], G[r][1]);
            st_dev_o(mb, mo + tt * (2 * 48 * 1024), o);
            SB();
        }
    }
}

__device__ __forceinline__ void g1_loader(char* __restrict__ ws, lptr sm, int lw, int lane, int t0, int cb, int t2, int db, unsigned tag) {
    const unsigned sm0 = (unsigned)(size_t)sm;
    {
        const char* hT = ws + OFF_HT;
        const char* wsrc = ws + OFF_WG + (size_t)cb * 40 * 6144 + 3 * lw * 1024;
        unsigned vh[2];
#pragma unroll
        for (int j = 0; j < 2; ++j) {
            const int o = (2 * lw + j) * 1024 + lane * 16, k = o >> 8, c = ((o >> 4) & 15) ^ ((k & 3) << 2);
            vh[j] = (unsigned)(k * (T * 2) + t0 * 2 + c * 16);
        }
        const unsigned vl = lane * 16;
        unsigned long long* st = (unsigned long long*)(ws + OFF_ST);
        auto stage = [&](int s) {
            const unsigned lb = sm0 + (s % G1_NS) * G1_SS;
            const char* hs = hT + (size_t)s * 32 * (T * 2);
            const char* wsx = wsrc + (size_t)s * 12288;
            dma16s(hs, vh[0], lb + (2 * lw) * 1024);
            if (s >= 4) dma16s(wsx, vl, lb + G1_W + (3 * lw) * 1024);
            dma16s(hs, vh[1], lb + (2 * lw + 1) * 1024);
            if (s >= 4) { dma16s(wsx + 1024, vl, lb + G1_W + (3 * lw + 1) * 1024); dma16s(wsx + 2048, vl, lb + G1_W + (3 * lw + 2) * 1024); }
        };
        u32x4 gv[3];
        unsigned long long g0, g1;
        float cs[8], sv[16];
#pragma unroll
        for (int s = 0; s < 4; ++s) stage(s);
        {
            const char* hp = hT + (size_t)(160 * lw) * (T * 2) + (t0 + 8 * cb) * 2;
            const unsigned go = (unsigned)lane * (T * 2);
            asm volatile("global_load_dwordx4 %0, %1, %2 sc1" : "=v"(gv[0]) : "v"(go), "s"(hp) : "memory");
            asm volatile("global_load_dwordx4 %0, %1, %2 sc1" : "=v"(gv[1]) : "v"(go), "s"(hp + (size_t)64 * (T * 2)) : "memory");
            asm volatile("global_load_dwordx4 %0, %1, %2 sc1" : "=v"(gv[2]) : "v"(lane < 32 ? go : 0u), "s"(hp + (size_t)128 * (T * 2)) : "memory");
        }
        wait_vm<9>();
        sbar();
#pragma unroll
        for (int s = 0; s < 19; ++s) {
            const int last = (s + 3) < 19 ? (s + 3) : 19;
            if (s == 3) asm volatile("s_waitcnt vmcnt(10)" : "+v"(gv[0]), "+v"(gv[1]), "+v"(gv[2]) :: "memory");
            else if (s == 17) asm volatile("s_waitcnt vmcnt(13)" : "+v"(g0), "+v"(g1) :: "memory");
            else if (s == 18) wait_vm<8>();
            else wait_vm_rt<30>(s == 0 ? 7 : (s == 1 ? 10 : (s == 2 ? 13 : 5 * (last - (s + 1)) + (s == 15 || s == 16 ? 2 : 0))));
            sbar();
            if (s + 4 < 20) stage(s + 4);
            if (s == 11 && lw < 3) {
                lptr cp = sm + G1_CS + lw * 256 + lane * 4;
                *(LAS float*)(cp + 3072) = (*(LAS float*)cp + *(LAS float*)(cp + 768)) + (*(LAS float*)(cp + 1536) + *(LAS float*)(cp + 2304));
                asm volatile("s_waitcnt lgkmcnt(0)" ::: "memory");
            }
            if (s == 16) {
#pragma unroll
                for (int j = 0; j < 3; ++j) {
                    const int i = lw + 4 * j < 10 ? lw + 4 * j : lw;
                    const int o = i * 1024 + lane * 16, row = o >> 7, c = ((o >> 4) & 7) ^ (((row >> 1) & 3) << 1);
                    dma16s(ws + OFF_HT, (unsigned)((80 * db + row) * (T * 2) + t2 * 2 + c * 16), sm0 + G2_H + i * 1024);
                }
            }
            if (s == 16 || s == 18) {
                const int s2 = s == 16 ? 0 : 1;
                const char* wd = ws + OFF_WD + (size_t)(4 * s2 + lw) * (40 * 1024) + (size_t)(5 * db) * 1024;
#pragma unroll
                for (int j = 0; j < 5; ++j) dma16s(wd + j * 1024, vl, sm0 + s2 * G2_SS + G2_W + (5 * lw + j) * 1024);
            }
            if (s == 14) {
                asm volatile("global_load_dwordx2 %0, %1, %2 sc1" : "=v"(g0) : "v"(lane * 8), "s"(st + t0) : "memory");
                asm volatile("global_load_dwordx2 %0, %1, %2 sc1" : "=v"(g1) : "v"(lane * 8), "s"(st + t0 + 64) : "memory");
            }
            if (s >= 3 && s <= 6) {
#pragma unroll
                for (int e = 2 * (s - 3); e < 2 * (s - 3) + 2; ++e) {
                    const unsigned c32 = (unsigned)__builtin_amdgcn_readlane((int)gv[0][e >> 1], 0);
                    cs[e] = (e & 1) ? bfhi(c32) : bflo(c32);
                    const float d0 = ((e & 1) ? bfhi(gv[0][e >> 1]) : bflo(gv[0][e >> 1])) - cs[e], d1 = ((e & 1) ? bfhi(gv[1][e >> 1]) : bflo(gv[1][e >> 1])) - cs[e];
                    const float d2 = lane < 32 ? ((e & 1) ? bfhi(gv[2][e >> 1]) : bflo(gv[2][e >> 1])) - cs[e] : 0.f;
                    sv[e] = d0 + d1 + d2; sv[8 + e] = d0 * d0 + d1 * d1 + d2 * d2;
                    PIN(sv[e]); PIN(sv[8 + e]);
                }
            }
            if (s == 7) {
#pragma unroll
                for (int j = 0; j < 8; ++j) {
                    const auto r = __builtin_amdgcn_permlane32_swap(__float_as_uint(sv[j]), __float_as_uint(sv[j + 8]), false, false);
                    sv[j] = __uint_as_float(r[0]) + __uint_as_float(r[1]); PIN(sv[j]);
                }
            }
            if (s == 8) {
#pragma unroll
                for (int k = 0; k < 4; ++k) {
                    const auto r = __builtin_amdgcn_permlane16_swap(__float_as_uint(sv[k]), __float_as_uint(sv[k + 4]), false, false);
                    sv[k] = __uint_as_float(r[0]) + __uint_as_float(r[1]);
                }
#pragma unroll
                for (int k = 0; k < 4; ++k) { sv[k] = row16_sum(sv[k]); PIN(sv[k]); }
            }
            if (s == 9) {
                if ((lane & 15) == 0) *(LAS f32x4*)(sm + G1_SP + lw * 96 + (lane >> 4) * 16) = (f32x4){sv[0], sv[1], sv[2], sv[3]};
                if (lane == 0) { *(LAS f32x4*)(sm + G1_SP + lw * 96 + 64) = (f32x4){cs[0], cs[1], cs[2], cs[3]}; *(LAS f32x4*)(sm + G1_SP + lw * 96 + 80) = (f32x4){cs[4], cs[5], cs[6], cs[7]}; }
                asm volatile("s_waitcnt lgkmcnt(0)" ::: "memory");
            }
            if (s == 10 && lw == 3 && lane < 8) {
                float pm[4], m2 = 0.f, mean = 0.f;
#pragma unroll
                for (int i = 0; i < 4; ++i) {
                    const float a = *(LAS float*)(sm + G1_SP + i * 96 + lane * 4) * (1.0f / 160.0f), q = *(LAS float*)(sm + G1_SP + i * 96 + 32 + lane * 4);
                    pm[i] = *(LAS float*)(sm + G1_SP + i * 96 + 64 + lane * 4) + a;
                    m2 += q - 160.0f * a * a; mean += 0.25f * pm[i];
                }
#pragma unroll
                for (int i = 0; i < 4; ++i) m2 += 160.0f * (pm[i] - mean) * (pm[i] - mean);
                const float var = fmaxf(m2 * (1.0f / (float)D), 0.f);
                const unsigned mb = pk_bf16(mean, rbf(sqrtf(rbf(rbf(var) + 1e-5f))));
                __hip_atomic_store(st + t0 + 8 * cb + lane, ((unsigned long long)tag << 32) | mb, __ATOMIC_RELAXED, __HIP_MEMORY_SCOPE_AGENT);
            }
        }
        if (lw == 0) {
            for (unsigned spins = 0; !__all((unsigned)(g0 >> 32) == tag && (unsigned)(g1 >> 32) == tag) && spins < (1u << 18); ++spins) {
                __builtin_amdgcn_s_sleep(2);
                g0 = __hip_atomic_load(st + t0 + lane, __ATOMIC_RELAXED, __HIP_MEMORY_SCOPE_AGENT);
                g1 = __hip_atomic_load(st + t0 + 64 + lane, __ATOMIC_RELAXED, __HIP_MEMORY_SCOPE_AGENT);
            }
            *(LAS f32x2*)(sm + G1_ST + lane * 8) = (f32x2){bflo((unsigned)g0), bfhi((unsigned)g0)};
            *(LAS f32x2*)(sm + G1_ST + (64 + lane) * 8) = (f32x2){bflo((unsigned)g1), bfhi((unsigned)g1)};
        }
        bar();
    }
}

template <int ND>
__device__ __forceinline__ void g2_compute(lptr sm, int lane, int th, int dl0, int t0, int d0, bf16_t* __restrict__ out PROF_ARG) {
    f32x4 acc[ND][2] = {};
    bf16x8 A[2][ND], B[2][2];
    const lptr pb0 = sm + lane * 16 + 2 * th * 4096, pa0 = sm + lane * 16 + G2_W + dl0 * 1024;
    auto ld = [&](int P, lptr pa, lptr pb, int fl) {
#pragma unroll
        for (int tt = 0; tt < 2; ++tt) B[P][tt] = *(LAS bf16x8*)(pb + (tt * 4 + fl) * 1024);
#pragma unroll
        for (int dt = 0; dt < ND; ++dt) A[P][dt] = *(LAS bf16x8*)(pa + (fl * 5 + dt) * 1024);
        SB();
    };
    auto mm = [&](int P) {
#pragma unroll
        for (int dt = 0; dt < ND; ++dt)
#pragma unroll
            for (int tt = 0; tt < 2; ++tt) { acc[dt][tt] = __builtin_amdgcn_mfma_f32_16x16x32_bf16(A[P][dt], B[P][tt], acc[dt][tt], 0, 0, 0); PIN(acc[dt][tt]); SB(); }
    };
    sbar();
    STAMP(11); CSTAMP(20);
    ld(0, pa0, pb0, 0);
    lptr pa = pa0, pb = pb0;
    int nb = 0;
#pragma unroll 1
    for (int s = 0; s < 12; ++s) {
        ld(1, pa, pb, 1); mm(0);
        ld(0, pa, pb, 2); mm(1);
        ld(1, pa, pb, 3); mm(0);
        if (s < 11) {
            nb = nb + G2_SS == G2_NS * G2_SS ? 0 : nb + G2_SS;
            pa = pa0 + nb; pb = pb0 + nb;
            bar();
            ld(0, pa, pb, 0);
        }
        mm(1);
    }
    STAMP(12); CSTAMP(21);
    bar();
    const int g = lane >> 4, q = (lane & 15) >> 2, p = lane & 3;
#pragma unroll
    for (int dt = 0; dt < ND; ++dt)
#pragma unroll
        for (int tt = 0; tt < 2; ++tt) {
            const int row = 16 * (dl0 + dt) + 4 * g + q, seg = (2 * th + tt) ^ ((row >> 1) & 3);
            const u32x2 hv = tr_read(sm + G2_H + row * 128 + seg * 32 + p * 8);
            const unsigned o01 = pk_bf16(acc[dt][tt][0], acc[dt][tt][1]), o23 = pk_bf16(acc[dt][tt][2], acc[dt][tt][3]);
            const u32x2 r = {pk_bf16(bflo(hv[0]) + bflo(o01), bfhi(hv[0]) + bfhi(o01)), pk_bf16(bflo(hv[1]) + bflo(o23), bfhi(hv[1]) + bfhi(o23))};
            *(LAS u32x2*)(sm + (16 * (2 * th + tt) + (lane & 15)) * 176 + (16 * (dl0 + dt) + 4 * g) * 2) = r;
        }
}

extern "C" __global__ void s4d_geglu_block_end();
extern "C" __global__ __launch_bounds__(512, 2) void s4d_geglu_block(Params P PROF_ARG) {
    extern __shared__ __attribute__((aligned(16))) char smem[];
    lptr sm = (lptr)smem;
    STAMP(0);
    if (uni(threadIdx.x >> 6) == 7) {
        unsigned long long pc; unsigned off, len;
        asm volatile("s_getpc_b64 %0\n.Lpc_anchor_%=:\n\ts_mov_b32 %1, .Lpc_anchor_%=-s4d_geglu_block\n\ts_mov_b32 %2, s4d_geglu_block_end-s4d_geglu_block" : "=s"(pc), "=s"(off), "=s"(len));
        const unsigned o = (blockIdx.x >> 3) * 2048 + (threadIdx.x & 63) * 32;
        if (o < len) dma4((const void*)(pc - off), o, (unsigned)(size_t)sm + LDS_BYTES - 256);
    }
    const int tid = threadIdx.x, lane = tid & 63, w = uni(tid >> 6), b = blockIdx.x;
    const unsigned sm0 = (unsigned)(size_t)sm;
    char* ws = P.ws;
    unsigned* fl = (unsigned*)(ws + OFF_FL);
    unsigned tag;
    const int grp = b & 7, idx = b >> 3, grp0 = grp;
    const int rb = (grp & 1) * 8 + (idx & 7), cb = (grp >> 1) * 4 + (idx >> 3);
    const int tb = (grp >> 1) * 8 + (idx & 7), db = (grp & 1) * 4 + (idx >> 3);

    if (b < 160) tag = conv_wg(P, b, cb, sm PROF_FWD); else tag = spare_wg(P, b - 160, cb, sm PROF_FWD);

    if (w >= 4) g1_prefetch_w(ws, sm, cb, w & 3, lane);
    if (w == 4) wait_flags<32, 32>(fl + FL_HC + grp0 * 32 * 32, (tag << 3) + 5u, lane);
    if (w == 7) wait_flags<48, FL_W_S>(fl + grp0 * FL_REP + FL_WD, tag, lane);
    bar();
    STAMP(6);
    if (w >= 4) g1_loader(ws, sm, w & 3, lane, 128 * rb, cb, 64 * tb, db, tag);
    else if (w & 1) g1_compute<1>(sm, ws, w >> 1, lane, 128 * rb, cb PROF_FWD); else g1_compute<0>(sm, ws, w >> 1, lane, 128 * rb, cb PROF_FWD);

    const int t0 = 64 * tb, d0 = 80 * db;
    if (w >= 4) {
        const int lw = w & 3;
        const char* ms = ws + OFF_MF + (size_t)(4 * tb + lw) * 48 * 1024;
        const char* wsrc = ws + OFF_WD + (size_t)lw * (40 * 1024) + (size_t)(5 * db) * 1024;
        const unsigned vl = lane * 16;
        auto stage_w = [&](int s) {
#pragma unroll
            for (int j = 0; j < 5; ++j) dma16s(wsrc + (size_t)(4 * s) * (40 * 1024) + j * 1024, vl, sm0 + (s % G2_NS) * G2_SS + G2_W + (5 * lw + j) * 1024);
        };
        auto stage_m = [&](int s) {
#pragma unroll
            for (int j = 0; j < 4; ++j) dma16s(ms + (size_t)(4 * s + j) * 1024, vl, sm0 + (s % G2_NS) * G2_SS + (4 * lw + j) * 1024);
        };
        stage_w(2);
        sbar();
        if (lw == 0) wait_flags<16, FL_M_S>(fl + FL_M + 16 * (tb >> 1) * FL_M_S, tag, lane);
        sbar();
        STAMPL(10);
        stage_m(0); stage_m(1); stage_m(2);
        wait_vm<8>();
        sbar();
#pragma unroll
        for (int s = 0; s < 11; ++s) {
            if (s == 0) wait_vm<4>(); else if (s <= 9) wait_vm<9>(); else wait_vm<0>();
            sbar();
            if (s + 3 < 12) { stage_m(s + 3); stage_w(s + 3); }
        }
        sbar();
    } else {
        STAMP(28);
        wait_vm<0>();
        sbar();
        if (tid == 0) set_flag(fl + FL_M + (16 * rb + cb) * FL_M_S, tag);
        STAMP(9);
        sbar();
        const int th = w & 1;
        if (w >> 1) g2_compute<2>(sm, lane, th, 3, t0, d0, P.out PROF_FWD); else g2_compute<3>(sm, lane, th, 0, t0, d0, P.out PROF_FWD);
    }
    bar();
    for (int ch = tid; ch < 640; ch += 512) {
        const int row = ch / 10, c = ch % 10;
        *(u32x4*)(P.out + (size_t)(t0 + row) * D + d0 + c * 8) = *(LAS u32x4*)(sm + row * 176 + c * 16);
    }
    if (b == 0 && tid == 0) fl[FL_E] = tag;
    STAMP(15);
}

extern "C" __global__ void s4d_geglu_block_end() {}

extern "C" void kernel_launch(void* const* d_in, const int* in_sizes, int n_in, void* d_out, int out_size, void* d_ws, size_t ws_size, hipStream_t stream) {
    Params P;
    P.x = (const bf16_t*)d_in[0]; P.logA = (const bf16_t*)d_in[1]; P.Aim = (const bf16_t*)d_in[2]; P.Cre = (const bf16_t*)d_in[3]; P.Cim = (const bf16_t*)d_in[4];
    P.logdt = (const bf16_t*)d_in[5]; P.Dskip = (const bf16_t*)d_in[6]; P.wg = (const bf16_t*)d_in[8]; P.wu = (const bf16_t*)d_in[9]; P.wd = (const bf16_t*)d_in[10];
    P.ws = (char*)d_ws; P.out = (bf16_t*)d_out;
    (void)hipFuncSetAttribute((const void*)s4d_geglu_block, hipFuncAttributeMaxDynamicSharedMemorySize, LDS_BYTES);
    s4d_geglu_block<<<256, 512, LDS_BYTES, stream>>>(P);
}
```

```cpp
#define PROBE_AT 1
#define PROBE_T 1300
#include <hip/hip_runtime.h>
#include <stdint.h>

typedef unsigned short bf16_t;
typedef short s16x4 __attribute__((ext_vector_type(4)));
typedef __bf16 bf16x8 __attribute__((ext_vector_type(8)));
typedef __bf16 bf16x2 __attribute__((ext_vector_type(2)));
typedef _Float16 h16x8 __attribute__((ext_vector_type(8)));
typedef _Float16 h16x2 __attribute__((ext_vector_type(2)));
typedef float f32x2 __attribute__((ext_vector_type(2)));
typedef float f32x4 __attribute__((ext_vector_type(4)));
typedef float f32x16 __attribute__((ext_vector_type(16)));
typedef unsigned u32x2 __attribute__((ext_vector_type(2)));
typedef unsigned u32x4 __attribute__((ext_vector_type(4)));
#define LAS __attribute__((address_space(3)))
typedef LAS char* lptr;

constexpr int T = 2048, D = 640, NM = 192, F = 1536;
constexpr size_t OFF_S1 = 0;
constexpr size_t OFF_HT = OFF_S1 + (size_t)T * 8;
constexpr size_t OFF_CS = OFF_HT + (size_t)T * D * 2;
constexpr size_t OFF_MF = OFF_CS + 65536;
constexpr size_t OFF_WD = OFF_MF + (size_t)T * F * 2;
constexpr size_t OFF_WG = OFF_WD + (size_t)F * D * 2;
constexpr size_t OFF_ST = OFF_WG + (size_t)2 * F * D * 2;
constexpr size_t OFF_FL = OFF_ST + (size_t)T * 8;
constexpr size_t OFF_PROF = OFF_FL + 196608;
constexpr int FL_E = 0;
constexpr int FL_REP = 4096;
constexpr int FL_LN = FL_REP, FL_LN_S = 16;
constexpr int FL_WG = FL_LN + 1024, FL_WD = FL_WG + 96 * 8, FL_W_S = 8;
constexpr int FL_M = 64, FL_M_S = 8;
constexpr int FL_HC = 9 * FL_REP;
static_assert(FL_WD + 48 * FL_W_S <= 2 * FL_REP && FL_M + 256 * FL_M_S <= FL_REP, "flag map");

#define STAMP(i)
#define STAMPL(i)
#define CSTAMP(i)
#define PROF_ARG
#define PROF_PASS
#define PROF_FWD

__device__ __forceinline__ float bf2f(unsigned v) { return __uint_as_float(v << 16); }
__device__ __forceinline__ float bflo(unsigned v) { return __uint_as_float(v << 16); }
__device__ __forceinline__ float bfhi(unsigned v) { return __uint_as_float(v & 0xffff0000u); }
__device__ __forceinline__ unsigned pk_bf16(float lo, float hi) { f32x2 v = {lo, hi}; return __builtin_bit_cast(unsigned, __builtin_convertvector(v, bf16x2)); }
__device__ __forceinline__ float rbf(float f) { return bflo(pk_bf16(f, f)); }
__device__ __forceinline__ unsigned pk_f16(f32x2 v) { return __builtin_bit_cast(unsigned, __builtin_convertvector(v, h16x2)); }
template <int CTRL> __device__ __forceinline__ float dppf(float x) { return __int_as_float(__builtin_amdgcn_update_dpp(0, __float_as_int(x), CTRL, 0xF, 0xF, true)); }
__device__ __forceinline__ float row16_sum(float x) { x += dppf<0xB1>(x); x += dppf<0x4E>(x); x += dppf<0x141>(x); x += dppf<0x140>(x); return x; }
__device__ __forceinline__ float row16_max(float x) { x = fmaxf(x, dppf<0xB1>(x)); x = fmaxf(x, dppf<0x4E>(x)); x = fmaxf(x, dppf<0x141>(x)); x = fmaxf(x, dppf<0x140>(x)); return x; }
__device__ __forceinline__ float xsum16_32(float x) {
    const unsigned u = __float_as_uint(x);
    const auto r = __builtin_amdgcn_permlane16_swap(u, u, false, false);
    const unsigned v = __float_as_uint(__uint_as_float(r[0]) + __uint_as_float(r[1]));
    const auto q = __builtin_amdgcn_permlane32_swap(v, v, false, false);
    return __uint_as_float(q[0]) + __uint_as_float(q[1]);
}
__device__ __forceinline__ u32x2 tr_read(lptr p) { return __builtin_bit_cast(u32x2, __builtin_amdgcn_ds_read_tr16_b64_v4i16((LAS s16x4*)p)); }
__device__ __forceinline__ u32x4 tr_read2(lptr p, int off2) { const u32x2 r1 = tr_read(p), r2 = tr_read(p + off2); return (u32x4){r1[0], r1[1], r2[0], r2[1]}; }
__device__ __forceinline__ int uni(int v) { return __builtin_amdgcn_readfirstlane(v); }

__device__ __forceinline__ void dma16(const void* sbase, unsigned voff, unsigned ldsaddr) {
    asm volatile("s_mov_b32 m0, %2\n\ts_nop 0\n\tglobal_load_lds_dwordx4 %0, %1" :: "v"(voff), "s"(sbase), "s"(ldsaddr) : "memory");
}
__device__ __forceinline__ void dma4(const void* sbase, unsigned voff, unsigned ldsaddr) {
    asm volatile("s_mov_b32 m0, %2\n\ts_nop 0\n\tglobal_load_lds_dword %0, %1" :: "v"(voff), "s"(sbase), "s"(ldsaddr) : "memory");
}
template <int N> __device__ __forceinline__ void wait_vm() { asm volatile("s_waitcnt vmcnt(%0)" :: "n"(N) : "memory"); }
template <int N> __device__ __forceinline__ void wait_vm_rt(int n) {
    if (n >= N) wait_vm<N>(); else if constexpr (N > 0) wait_vm_rt<N - 1>(n);
}
__device__ __forceinline__ void bar() { asm volatile("s_waitcnt lgkmcnt(0)\n\ts_barrier" ::: "memory"); }
__device__ __forceinline__ void sbar() { asm volatile("s_barrier" ::: "memory"); }
#define SB() __builtin_amdgcn_sched_barrier(0)
#define PIN(x) asm volatile("" : "+v"(x))
__device__ __forceinline__ void st_dev(void* p, u32x4 v) { asm volatile("global_store_dwordx4 %0, %1, off sc1\n\ts_nop 1" :: "v"(p), "v"(v) : "memory"); }
__device__ __forceinline__ void st_dev_o(const void* sbase, unsigned voff, u32x4 v) { asm volatile("global_store_dwordx4 %0, %1, %2 sc1\n\ts_nop 1" :: "v"(voff), "v"(v), "s"(sbase) : "memory"); }
__device__ __forceinline__ void st_dev4(void* p, float v) { asm volatile("global_store_dword %0, %1, off sc1\n\ts_nop 1" :: "v"(p), "v"(v) : "memory"); }
__device__ __forceinline__ void dma16s(const void* sbase, unsigned voff, unsigned ldsaddr) {
    asm volatile("s_mov_b32 m0, %2\n\ts_nop 0\n\tglobal_load_lds_dwordx4 %0, %1 sc1" :: "v"(voff), "s"(sbase), "s"(ldsaddr) : "memory");
}
__device__ __forceinline__ void dma4s(const void* sbase, unsigned voff, unsigned ldsaddr) {
    asm volatile("s_mov_b32 m0, %2\n\ts_nop 0\n\tglobal_load_lds_dword %0, %1 sc1" :: "v"(voff), "s"(sbase), "s"(ldsaddr) : "memory");
}
__device__ __forceinline__ void set_flag(unsigned* f, unsigned tag) { __hip_atomic_store(f, tag, __ATOMIC_RELAXED, __HIP_MEMORY_SCOPE_AGENT); }
__device__ __forceinline__ void set_flag_rep(unsigned* f, unsigned tag, int lane) { __hip_atomic_store(f + lane * FL_REP, tag, __ATOMIC_RELAXED, __HIP_MEMORY_SCOPE_AGENT); }
template <int N, int S>
__device__ __forceinline__ void wait_flags(unsigned* f, unsigned tag, int lane) {
    for (unsigned spins = 0; spins < (1u << 18); ++spins) {
        bool ok = true;
        if (lane < N) ok = __hip_atomic_load(f + lane * S, __ATOMIC_RELAXED, __HIP_MEMORY_SCOPE_AGENT) == tag;
        if (N > 64 && lane + 64 < N) ok &= __hip_atomic_load(f + (64 + lane) * S, __ATOMIC_RELAXED, __HIP_MEMORY_SCOPE_AGENT) == tag;
        if (N > 128 && lane + 128 < N) ok &= __hip_atomic_load(f + (128 + lane) * S, __ATOMIC_RELAXED, __HIP_MEMORY_SCOPE_AGENT) == tag;
        if (__all(ok)) break;
        __builtin_amdgcn_s_sleep(1);
    }
    asm volatile("" ::: "memory");
}

__device__ __forceinline__ unsigned epoch_tag(const char* ws) {
    unsigned e;
    asm volatile("s_load_dword %0, %1, 0x0\n\ts_waitcnt lgkmcnt(0)" : "=s"(e) : "s"(ws + OFF_FL + FL_E * 4) : "memory");
    return e + 1u;
}

constexpr int G1_NS = 7, G1_SS = 20480, G1_W = 8192;
constexpr int G2_NS = 4, G2_SS = 36 * 1024, G2_W = 16 * 1024;
constexpr int G2_H = G2_NS * G2_SS;
constexpr int G1_CS = G2_H + 10240;
constexpr int G1_ST = G1_CS + 3840;
constexpr int G1_SP = G1_ST + 1024;
constexpr int LDS_BYTES = 163840;
static_assert(G1_SP + 512 <= LDS_BYTES, "LDS map");
constexpr int KG_ROW = 272;
constexpr int KG_WAVE = 57 * KG_ROW;
constexpr int CV_KCH = 8320;
__host__ __device__ constexpr int cv_k(int c) { return (4 + c) * KG_WAVE + 3072; }
__host__ __device__ constexpr int cv_kc(int c) { return cv_k(c) + CV_KCH; }
static_assert(3072 + CV_KCH + 1296 <= KG_WAVE, "per-channel area");
constexpr int CV_X = 8 * KG_WAVE + 7 * KG_ROW + 16;
constexpr int CV_U = CV_X + 128 * 128;
constexpr int CV_M = CV_U + 144 * 128;
static_assert(CV_X % 128 == 0 || true, "");
static_assert(CV_M + 32 + 256 <= 163840 && CV_X % 16 == 0, "LDS map");
constexpr int LN_ROW = 1344;

__device__ __forceinline__ void ln1_load(int j, const bf16_t* __restrict__ x, u32x4 (&v)[5]) {
    const int tid = threadIdx.x, t = 32 * j + (tid >> 4);
#pragma unroll
    for (int i = 0; i < 5; ++i) v[i] = *(const u32x4*)(x + (size_t)t * D + ((tid & 15) + 16 * i) * 8);
}
__device__ __forceinline__ void ln1_unit(int j, const u32x4 (&v)[5], char* __restrict__ ws, unsigned tag PROF_ARG) {
    const int tid = threadIdx.x, lane = tid & 63, w = uni(tid >> 6);
    f32x2 f[20];
    f32x2 s2 = {0.f, 0.f};
#pragma unroll
    for (int i = 0; i < 5; ++i)
#pragma unroll
        for (int e = 0; e < 4; ++e) { f[4 * i + e] = (f32x2){bflo(v[i][e]), bfhi(v[i][e])}; s2 += f[4 * i + e]; }
    const float mean = row16_sum(s2[0] + s2[1]) * (1.0f / (float)D);
    f32x2 q2 = {0.f, 0.f};
#pragma unroll
    for (int i = 0; i < 20; ++i) { const f32x2 dv = f[i] - mean; q2 += dv * dv; }
    const float var = row16_sum(q2[0] + q2[1]) * (1.0f / (float)D);
    const float rs = __builtin_amdgcn_rcpf(rbf(sqrtf(rbf(rbf(var) + 1e-5f)))), nm = -rbf(mean) * rs;
    u32x4 o;
#pragma unroll
    for (int i = 0; i < 4; ++i) {
        const unsigned a = (unsigned)__builtin_amdgcn_readlane((int)__float_as_uint(rs), 16 * i), c = (unsigned)__builtin_amdgcn_readlane((int)__float_as_uint(nm), 16 * i);
        o[i] = lane == 0 ? a : c;
    }
    if (lane < 2) st_dev(ws + OFF_S1 + (size_t)lane * (T * 4) + (32 * j + 4 * w) * 4, o);
    if (tid < 4) set_flag((unsigned*)(ws + OFF_FL) + FL_HC + (4 * j + tid) * 32, tag << 3);
    STAMP(24);
}

__device__ __forceinline__ void wgtile_load(int b, const bf16_t* __restrict__ wg, const bf16_t* __restrict__ wu, u32x4 (&v)[5]) {
    const int tid = threadIdx.x, j = b >> 2, kq = b & 3, c16 = tid & 15, r0 = tid >> 4;
    const bf16_t* src = (c16 < 8 ? wg : wu) + (size_t)(160 * kq + r0) * F + 64 * j + (c16 & 7) * 8;
#pragma unroll
    for (int i = 0; i < 5; ++i) v[i] = *(const u32x4*)(src + (size_t)(32 * i) * F);
}
__device__ __forceinline__ void wgtile_unit(int b, const u32x4 (&v)[5], char* __restrict__ ws, lptr sm) {
    const int tid = threadIdx.x, lane = tid & 63, w = uni(tid >> 6);
    const int j = b >> 2, kq = b & 3;
    const int c16 = tid & 15, r0 = tid >> 4;
    f32x2 a[4] = {};
#pragma unroll
    for (int i = 0; i < 5; ++i) {
        const int k = 32 * i + r0;
        *(LAS u32x4*)(sm + k * 256 + (((c16 >> 1) ^ (k & 3)) * 32) + (c16 & 1) * 16) = v[i];
#pragma unroll
        for (int e = 0; e < 4; ++e) a[e] += (f32x2){bflo(v[i][e]), bfhi(v[i][e])};
    }
#pragma unroll
    for (int e = 0; e < 4; ++e)
#pragma unroll
        for (int hf = 0; hf < 2; ++hf) {
            const float s = xsum16_32(a[e][hf]);
            if (lane < 16) *(LAS float*)(sm + 40960 + (w * 128 + c16 * 8 + 2 * e + hf) * 4) = s;
        }
    bar();
    const int g = lane >> 4, q = (lane & 15) >> 2, p = lane & 3;
    u32x4 o[5];
#pragma unroll
    for (int i = 0; i < 5; ++i) {
        const int item = w + 8 * i, ftl = item & 3, sub = item >> 2;
        const int k = 16 * sub + 4 * (g >> 1) + q;
        o[i] = tr_read2(sm + k * 256 + ((((g & 1) * 4 + ftl) ^ q) * 32) + p * 8, 8 * 256);
    }
#pragma unroll
    for (int i = 0; i < 5; ++i) {
        const int item = w + 8 * i, ftl = item & 3, sub = item >> 2;
        const int ftg = 4 * j + ftl, cb = ftg / 6, ft = ftg % 6;
        st_dev(ws + OFF_WG + ((((size_t)cb * 40 + 10 * kq + sub) * 6 + ft) * 64 + lane) * 16, o[i]);
    }
    if (tid < 128) {
        float s = 0.f;
#pragma unroll
        for (int i = 0; i < 8; ++i) s += *(LAS float*)(sm + 40960 + (i * 128 + tid) * 4);
        st_dev4(ws + OFF_CS + (size_t)(kq * 2 * F + (tid >> 6) * F + 64 * j + (tid & 63)) * 4, s);
    }
}

__device__ __forceinline__ void wdtile_load(int fb, const bf16_t* __restrict__ wd, u32x4 (&v)[5]) {
    const int tid = threadIdx.x;
#pragma unroll
    for (int i = 0; i < 5; ++i) { const int ch = tid + 512 * i, row = ch / 80, c = ch % 80; v[i] = *(const u32x4*)(wd + (size_t)(32 * fb + row) * D + c * 8); }
}
__device__ __forceinline__ void wdtile_unit(int fb, const u32x4 (&v)[5], char* __restrict__ ws, lptr sm) {
    const int tid = threadIdx.x, lane = tid & 63, w = uni(tid >> 6);
#pragma unroll
    for (int i = 0; i < 5; ++i) { const int ch = tid + 512 * i, row = ch / 80, c = ch % 80; *(LAS u32x4*)(sm + row * LN_ROW + c * 16) = v[i]; }
    bar();
    const int g = lane >> 4, q = (lane & 15) >> 2, p = lane & 3;
    u32x4 o[5];
#pragma unroll
    for (int i = 0; i < 5; ++i) o[i] = tr_read2(sm + (16 * (g >> 1) + 4 * (g & 1) + q) * LN_ROW + (16 * (w + 8 * i) + 4 * p) * 2, 8 * LN_ROW);
#pragma unroll
    for (int i = 0; i < 5; ++i) st_dev(ws + OFF_WD + (((size_t)fb * 40 + w + 8 * i) * 64 + lane) * 16, o[i]);
}

__device__ __forceinline__ f32x2 cmul(f32x2 a, f32x2 b) {
    const f32x2 t = (f32x2){a[0], a[0]} * b;
    const f32x2 bs = {-b[1], b[0]};
    return __builtin_elementwise_fma((f32x2){a[1], a[1]}, bs, t);
}
__device__ __forceinline__ unsigned cmul_h(unsigned a, unsigned b) {
    unsigned t, r;
    asm("v_pk_mul_f16 %0, %1, %2 op_sel:[0,0] op_sel_hi:[0,1]" : "=v"(t) : "v"(a), "v"(b));
    asm("v_pk_fma_f16 %0, %1, %2, %3 op_sel:[1,1,0] op_sel_hi:[1,0,1] neg_lo:[0,1,0]" : "=v"(r) : "v"(a), "v"(b), "v"(t));
    return r;
}
__device__ __forceinline__ f32x2 cexp_p(float arl, float air, float p) {
    const float mag = __builtin_amdgcn_exp2f(arl * p);
    float rev = air * p; rev -= rintf(rev);
    return (f32x2){mag * __builtin_amdgcn_cosf(rev), mag * __builtin_amdgcn_sinf(rev)};
}
struct KgenIn { unsigned ldt, la, ai, cr[3], ci[3]; };
__device__ __forceinline__ void kgen_load(KgenIn& k, int d, int third, int lane, const bf16_t* __restrict__ logA, const bf16_t* __restrict__ Aim,
                                          const bf16_t* __restrict__ Cre, const bf16_t* __restrict__ Cim, const bf16_t* __restrict__ logdt) {
    k.ldt = logdt[d]; k.la = logA[d * NM + 64 * third + lane]; k.ai = Aim[d * NM + 64 * third + lane];
#pragma unroll
    for (int i = 0; i < 3; ++i) { k.cr[i] = Cre[d * NM + 64 * i + lane]; k.ci[i] = Cim[d * NM + 64 * i + lane]; }
}
__device__ __forceinline__ void kgen_task(const KgenIn& k, int third, int lane, lptr tab, f32x4 (&acc)[3], float& unscale) {
    const float dt = rbf(expf(bf2f(k.ldt)));
    float mx = 0.f;
#pragma unroll
    for (int i = 0; i < 3; ++i) mx = fmaxf(mx, fmaxf(fabsf(bf2f(k.cr[i])), fabsf(bf2f(k.ci[i]))));
    mx = row16_max(mx);
    {
        const auto r1 = __builtin_amdgcn_permlane16_swap(__float_as_uint(mx), __float_as_uint(mx), false, false);
        mx = fmaxf(__uint_as_float(r1[0]), __uint_as_float(r1[1]));
        const auto r2 = __builtin_amdgcn_permlane32_swap(__float_as_uint(mx), __float_as_uint(mx), false, false);
        mx = fmaxf(__uint_as_float(r2[0]), __uint_as_float(r2[1]));
    }
    mx *= 2.f * dt;
    int e = (int)((__float_as_uint(mx) >> 23) & 0xff) - 126;
    e = e < -100 ? -100 : (e > 100 ? 100 : e);
    const float scale = __uint_as_float((unsigned)(127 - e) << 23);
    unscale = __uint_as_float((unsigned)(127 + e) << 23);
    const float are = -rbf(__builtin_amdgcn_exp2f(bf2f(k.la) * 1.4426950408889634f)) * dt, aim = bf2f(k.ai) * dt;
    const float crw = bf2f(third == 0 ? k.cr[0] : (third == 1 ? k.cr[1] : k.cr[2])) * dt, ciw = bf2f(third == 0 ? k.ci[0] : (third == 1 ? k.ci[1] : k.ci[2])) * dt;
    const float arl = are * 1.4426950408889634f, air = aim * 0.15915494309189535f;
    lptr base = tab + 4 * lane;
#define PUT(row, z) *(LAS unsigned*)(base + (row) * KG_ROW) = pk_f16(z)
#define PUTH(row, z) *(LAS unsigned*)(base + (row) * KG_ROW) = (z)
    const f32x2 w1 = cexp_p(arl, air, 1.f), w8 = cexp_p(arl, air, 8.f), H1 = cexp_p(arl, air, 128.f);
    f32x2 qv = {2.f * scale * crw, -2.f * scale * ciw};
    const f32x2 w8c = {w8[0], -w8[1]};
    f32x2 Ec = w1, Hc = H1;
    unsigned Eh[8], Hh[8];
    Eh[1] = pk_f16(w1); Hh[1] = pk_f16(H1);
    PUT(0, qv); PUTH(16, 0x00003c00u); PUTH(17, Eh[1]); PUTH(24, Hh[1]);
#pragma unroll
    for (int i = 1; i < 16; ++i) {
        qv = cmul(qv, w8c);
        Hc = cmul(Hc, H1);
        if (i < 7) Ec = cmul(Ec, w1);
        PUT(i, qv);
        if (i < 7) { Eh[i + 1] = pk_f16(Ec); PUTH(17 + i, Eh[i + 1]); }
        const int hi = i + 1;
        if (hi < 8) { Hh[hi] = pk_f16(Hc); PUTH(hi < 4 ? 32 + 4 * (hi - 2) : 40 + 2 * (hi - 4), Hh[hi]); } else PUT(48 + (hi - 8), Hc);
        if (i >= 7 && i < 14) PUTH(25 + (i - 7), cmul_h(Eh[i - 6], Hh[1]));
        if (i >= 7 && i < 10) PUTH(33 + (i - 7), cmul_h(Eh[2 * (i - 6)], Hh[2]));
        if (i >= 10 && i < 13) PUTH(37 + (i - 10), cmul_h(Eh[2 * (i - 9)], Hh[3]));
        if (i >= 11 && i < 15) PUTH(41 + 2 * (i - 11), cmul_h(Hh[4 + (i - 11)], Eh[4]));
    }
#undef PUTH
#undef PUT
    asm volatile("" ::: "memory");
    lptr rb = tab + (lane & 15) * KG_ROW + (lane >> 4) * 16;
#pragma unroll
    for (int mt = 0; mt < 3; ++mt) acc[mt] = (f32x4){};
#pragma unroll
    for (int ks = 0; ks < 4; ++ks) {
        const h16x8 bq = *(LAS h16x8*)(rb + ks * 64);
#pragma unroll
        for (int mt = 0; mt < 3; ++mt) {
            const h16x8 ar = *(LAS h16x8*)(rb + (16 + 16 * mt) * KG_ROW + ks * 64);
            acc[mt] = __builtin_amdgcn_mfma_f32_16x16x32_f16(ar, bq, acc[mt], 0, 0, 0);
        }
    }
#pragma unroll
    for (int mt = 0; mt < 3; ++mt) PIN(acc[mt]);
    asm volatile("s_waitcnt lgkmcnt(0)" ::: "memory");
}

__device__ __forceinline__ void g1_prefetch_w(char* __restrict__ ws, lptr sm, int cb, int lw, int lane) {
    const unsigned sm0 = (unsigned)(size_t)sm;
    const char* wsrc = ws + OFF_WG + (size_t)cb * 40 * 6144 + 3 * lw * 1024;
#pragma unroll
    for (int s = 0; s < 4; ++s)
#pragma unroll
        for (int i = 0; i < 3; ++i) dma16s(wsrc + (size_t)s * 12288 + i * 1024, lane * 16, sm0 + s * G1_SS + G1_W + (3 * lw + i) * 1024);
    if (lw < 3) {
        const int f0 = 96 * cb;
        const unsigned co = (unsigned)(((lw == 0 ? f0 + lane : (lw == 1 ? (lane < 32 ? f0 + 64 + lane : F + f0 + lane - 32) : F + f0 + 32 + lane))) * 4);
#pragma unroll
        for (int kq = 0; kq < 4; ++kq) dma4s(ws + OFF_CS + kq * 2 * F * 4, co, sm0 + G1_CS + kq * 768 + lw * 256);
    }
}

struct KTab { unsigned short v[2048]; };
constexpr int ci_of(int l) {
    if (l < 0) return 641;
    if (l < 256) return l;
    const int s = l < 512 ? 1 : (l < 1024 ? 2 : 3);
    return ((l + (1 << (s - 1)) - 1 + ((l >> s) & 1)) >> s) + 128 * s;
}
constexpr KTab make_tab() { KTab t{}; for (int y = 0; y < 2048; ++y) t.v[y] = (unsigned short)(2 * ci_of(2047 - y)); return t; }
__device__ const __attribute__((aligned(16))) KTab g_ktab = make_tab();

struct Params {
    const bf16_t *x, *logA, *Aim, *Cre, *Cim, *logdt, *Dskip, *wg, *wu, *wd;
    char* ws;
    bf16_t* out;
};

__device__ __forceinline__ unsigned conv_wg(const Params& P, int b, int cb, lptr sm PROF_ARG) {
    const int tid = threadIdx.x, lane = tid & 63, w = uni(tid >> 6);
    char* ws = P.ws;
    const int d0 = 4 * ((b & 7) * 20 + (b >> 3));
    const unsigned sm0 = (unsigned)(size_t)sm;
    unsigned* fl = (unsigned*)(ws + OFF_FL);
    KgenIn k1, k2;
    u32x4 tb[4];
    u32x2 xg[8];
    kgen_load(k1, d0 + w / 3, w % 3, lane, P.logA, P.Aim, P.Cre, P.Cim, P.logdt);
    const unsigned tag = epoch_tag(ws);
    if (w < 4) {
        const int t2 = 8 + w;
        kgen_load(k2, d0 + t2 / 3, t2 % 3, lane, P.logA, P.Aim, P.Cre, P.Cim, P.logdt);
#pragma unroll
        for (int i = 0; i < 4; ++i) tb[i] = *(const u32x4*)(&g_ktab.v[8 * (lane + 64 * i)]);
    }
    const unsigned dsk_raw = ((const unsigned*)P.Dskip)[(d0 + (w & 3)) >> 1];
    asm volatile("" : "+v"(k1.ldt), "+v"(k1.la), "+v"(k1.ai), "+v"(k1.cr[0]), "+v"(k1.cr[1]), "+v"(k1.cr[2]), "+v"(k1.ci[0]), "+v"(k1.ci[1]), "+v"(k1.ci[2]) :: "memory");
    STAMP(1);
    const lptr tab = sm + w * KG_WAVE;
    LAS unsigned* cnt = (LAS unsigned*)(sm + CV_M + 16);
    if (w == 7 && lane == 0) *cnt = 0;
    bar();
    if (w >= 4) {
#pragma unroll
        for (int i = 0; i < 8; ++i) xg[i] = *(const u32x2*)(P.x + (size_t)(8 * (tid - 256) + i) * D + d0);
        asm volatile("" ::: "memory");
    }
    f32x4 a1[3], a2[3];
    {
        int third = w % 3, chn = w / 3;
#pragma unroll 1
        for (int rnd = 0; rnd < 2; ++rnd) {
            float un;
            kgen_task(k1, third, lane, tab, a2, un);
            if (third == 0 && lane == 0) *(LAS float*)(sm + CV_M + chn * 4) = un;
            if (rnd == 1 || w >= 4) break;
#pragma unroll
            for (int mt = 0; mt < 3; ++mt) a1[mt] = a2[mt];
            k1 = k2; third = (8 + w) % 3; chn = (8 + w) / 3;
        }
    }
    if (w < 4) {
#pragma unroll
        for (int mt = 0; mt < 3; ++mt) { *(LAS f32x4*)(tab + (mt * 64 + lane) * 16) = a1[mt]; *(LAS f32x4*)(tab + 3072 + (mt * 64 + lane) * 16) = a2[mt]; }
        asm volatile("s_waitcnt lgkmcnt(0)" ::: "memory");
        if (lane == 0) __hip_atomic_fetch_add(cnt, 1u, __ATOMIC_RELAXED, __HIP_MEMORY_SCOPE_WORKGROUP);
        while (__hip_atomic_load(cnt, __ATOMIC_RELAXED, __HIP_MEMORY_SCOPE_WORKGROUP) < 8u) __builtin_amdgcn_s_sleep(0);
        asm volatile("" ::: "memory");
        STAMP(2);
        f32x4 ks[3];
        lptr pt[3];
#pragma unroll
        for (int j = 0; j < 3; ++j) { const int t = 3 * w + j; pt[j] = sm + (t & 7) * KG_WAVE + (t >> 3) * 3072 + lane * 16; }
        const float un = *(LAS float*)(sm + CV_M + w * 4);
#pragma unroll
        for (int mt = 0; mt < 3; ++mt) ks[mt] = ((*(LAS f32x4*)(pt[0] + mt * 1024) + *(LAS f32x4*)(pt[1] + mt * 1024)) + *(LAS f32x4*)(pt[2] + mt * 1024)) * un;
        lptr kc = sm + cv_kc(0) + w * KG_WAVE;
        const int hq = lane >> 4, col = lane & 15;
        const u32x2 p0 = {pk_bf16(ks[0][0], ks[0][1]), pk_bf16(ks[0][2], ks[0][3])}, p1 = {pk_bf16(ks[1][0], ks[1][1]), pk_bf16(ks[1][2], ks[1][3])};
        const u32x2 p2 = {pk_bf16(ks[2][0], ks[2][1]), pk_bf16(ks[2][2], ks[2][3])};
        *(LAS u32x2*)(kc + 2 * (128 * (hq >> 1) + 4 * (hq & 1) + 8 * col)) = p0;
        if (hq < 2) *(LAS u32x2*)(kc + 2 * (256 + 64 * hq + 4 * col)) = p1;
        else { *(LAS unsigned*)(kc + 2 * (384 + 64 * (hq - 2) + 2 * col)) = p1[0]; *(LAS unsigned*)(kc + 2 * (416 + 64 * (hq - 2) + 2 * col)) = p1[1]; }
        if (hq < 2) {
            lptr k2p = kc + 2 * (512 + 64 * hq + col);
            *(LAS unsigned short*)k2p = (unsigned short)p2[0]; *(LAS unsigned short*)(k2p + 32) = (unsigned short)(p2[0] >> 16);
            *(LAS unsigned short*)(k2p + 64) = (unsigned short)p2[1]; *(LAS unsigned short*)(k2p + 96) = (unsigned short)(p2[1] >> 16);
        } else if (lane == 32) *(LAS unsigned short*)(kc + 2 * 640) = (unsigned short)p2[0];
        else if (lane >= 48 && lane < 55) *(LAS unsigned short*)(kc + 2 * (641 + lane - 48)) = 0;
        asm volatile("" ::: "memory");
        STAMP(13);
        u32x4 o[4];
#pragma unroll
        for (int i = 0; i < 4; ++i)
#pragma unroll
            for (int k = 0; k < 4; ++k) {
                const unsigned tk = tb[i][k];
                const unsigned lo = *(LAS unsigned short*)(kc + (tk & 0xffffu)), hi = *(LAS unsigned short*)(kc + (tk >> 16));
                o[i][k] = lo | (hi << 16);
            }
        lptr dst = sm + cv_k(0) + w * KG_WAVE;
#pragma unroll
        for (int i = 0; i < 4; ++i) {
            *(LAS u32x4*)(dst + (lane + 64 * i) * 16) = o[i];
            unsigned nx = (unsigned)__builtin_amdgcn_update_dpp(0, (int)o[i][0], 0x130, 0xF, 0xF, false);
            const unsigned wrap = i < 3 ? (unsigned)__builtin_amdgcn_readlane((int)o[i < 3 ? i + 1 : 3][0], 0) : 0u;
            nx = lane == 63 ? wrap : nx;
            const u32x4 od = {__builtin_amdgcn_alignbit(o[i][1], o[i][0], 16), __builtin_amdgcn_alignbit(o[i][2], o[i][1], 16),
                              __builtin_amdgcn_alignbit(o[i][3], o[i][2], 16), __builtin_amdgcn_alignbit(nx, o[i][3], 16)};
            *(LAS u32x4*)(dst + 4160 + (lane + 64 * i) * 16) = od;
        }
        if (lane < 8) *(LAS u32x4*)(dst + (lane >> 2) * 4160 + 4096 + (lane & 3) * 16) = (u32x4){0u, 0u, 0u, 0u};
        STAMP(14);
    } else {
#pragma unroll
        for (int mt = 0; mt < 3; ++mt) *(LAS f32x4*)(tab + (mt * 64 + lane) * 16) = a2[mt];
        asm volatile("s_waitcnt lgkmcnt(0)" ::: "memory");
        if (lane == 0) __hip_atomic_fetch_add(cnt, 1u, __ATOMIC_RELAXED, __HIP_MEMORY_SCOPE_WORKGROUP);
        const int gm = tid - 256, ln = gm >> 1, sw = ((ln >> 1) & 3) << 1;
        u32x4 xc[4];
#pragma unroll
        for (int ch = 0; ch < 4; ++ch) {
#pragma unroll
            for (int k = 0; k < 4; ++k) xc[ch][k] = __builtin_amdgcn_perm(xg[2 * k + 1][ch >> 1], xg[2 * k][ch >> 1], (ch & 1) ? 0x07060302u : 0x05040100u);
            *(LAS u32x4*)(sm + CV_X + ln * 128 + 16 * ((2 * ch + (gm & 1)) ^ sw)) = xc[ch];
        }
        if (gm < 128) *(LAS u32x4*)(sm + CV_U + gm * 16) = (u32x4){0u, 0u, 0u, 0u};
        wait_flags<16, FL_LN_S>(fl + (b & 7) * FL_REP + FL_LN + 16 * (w - 4) * FL_LN_S, tag, lane);
        STAMPL(25);
        f32x4 r0, r1, n0, n1;
        asm volatile("global_load_dwordx4 %0, %4, %5 sc1\n\tglobal_load_dwordx4 %1, %4, %5 offset:16 sc1\n\tglobal_load_dwordx4 %2, %4, %6 sc1\n\tglobal_load_dwordx4 %3, %4, %6 offset:16 sc1\n\ts_waitcnt vmcnt(0)"
                     : "=&v"(r0), "=&v"(r1), "=&v"(n0), "=&v"(n1) : "v"(gm * 32), "s"(ws + OFF_S1), "s"(ws + OFF_S1 + T * 4) : "memory");
#pragma unroll
        for (int ch = 0; ch < 4; ++ch) {
            u32x4 u;
#pragma unroll
            for (int k = 0; k < 4; ++k) {
                const f32x2 rs = k < 2 ? (f32x2){r0[2 * k], r0[2 * k + 1]} : (f32x2){r1[2 * k - 4], r1[2 * k - 3]}, nm = k < 2 ? (f32x2){n0[2 * k], n0[2 * k + 1]} : (f32x2){n1[2 * k - 4], n1[2 * k - 3]};
                const f32x2 a = __builtin_elementwise_fma((f32x2){bflo(xc[ch][k]), bfhi(xc[ch][k])}, rs, nm);
                u[k] = pk_bf16(a[0], a[1]);
            }
            *(LAS u32x4*)(sm + CV_U + 2048 + ln * 128 + 16 * ((2 * ch + (gm & 1)) ^ sw)) = u;
        }
    }
    bar();
    STAMP(3); CSTAMP(16);
    if (w < 4) {
        const int n = lane & 15, h = lane >> 4;
        const lptr fb = sm + cv_k(0) + w * KG_WAVE + ((n & 1) ? 0 : 4160) + 4 * (((2031 - n) >> 1) + 4 * h) - 512 * 7;
        f32x4 acc[8] = {};
        bf16x8 Fa[2][8], Ga[2][8];
        lptr gp, fp[4];
        auto addr = [&](int rr) {
            const int ln = n + (h >> 1) + 15 - 2 * rr;
            gp = sm + CV_U + 128 * ln + 16 * ((2 * w + (h & 1)) ^ (((ln >> 1) & 3) << 1));
#pragma unroll
            for (int k = 0; k < 4; ++k) { fp[k] = fb - 64 * rr + 1024 * k; asm volatile("" : "+v"(fp[k])); }
        };
        auto ldG = [&](int Q, int c) { Ga[Q][c] = *(LAS bf16x8*)(gp + 2048 * c); };
        u32x4 ftmp[2];
        auto ldF = [&](int Q, int a, int half) {
            lptr q = fp[(7 - a) >> 1] + 512 * ((7 - a) & 1) + 8 * half;
            ftmp[a & 1][2 * half] = *(LAS unsigned*)q; ftmp[a & 1][2 * half + 1] = *(LAS unsigned*)(q + 4);
            if (half) Fa[Q][a] = __builtin_bit_cast(bf16x8, ftmp[a & 1]);
        };
        const int so = n * 128 + 16 * ((2 * w + (h >> 1)) ^ (((n >> 1) & 3) << 1)) + 8 * (h & 1);
        u32x2 uv[8], xv[8];
        addr(0);
#pragma unroll
        for (int c = 0; c < 8; ++c) { ldF(0, c, 0); ldF(0, c, 1); ldG(0, c); }
        SB();
#pragma unroll
        for (int rr = 0; rr < 8; ++rr) {
            const int Q = rr & 1;
            int i = 0;
#pragma unroll
            for (int a = 0; a < 8; ++a)
#pragma unroll
                for (int c = a; c < 8; ++c) {
                    acc[c] = __builtin_amdgcn_mfma_f32_16x16x32_bf16(Fa[Q][a], Ga[Q][c - a], acc[c], 0, 0, 0); PIN(acc[c]); SB();
                    if (rr < 7) {
                        if (i == 0) { addr(rr + 1); SB(); }
                        else if (i <= 24) { const int k = (i - 1) / 3, m = (i - 1) % 3; if (m == 0) ldG(Q ^ 1, k); else ldF(Q ^ 1, k, m - 1); SB(); }
                    } else if (i < 16) {
                        if (i & 1) xv[i >> 1] = *(LAS u32x2*)(sm + CV_X + so + 2048 * (i >> 1)); else uv[i >> 1] = *(LAS u32x2*)(sm + CV_U + 2048 + so + 2048 * (i >> 1));
                        SB();
                    }
                    ++i;
                }
        }
        STAMP(4); CSTAMP(17);
        const int d = d0 + w;
        lptr yb = sm + cv_k(0) + w * KG_WAVE;
        const float dsk = (d & 1) ? bfhi(dsk_raw) : bflo(dsk_raw);
        const f32x2 dk2 = {dsk, dsk};
#pragma unroll
        for (int c2 = 0; c2 < 8; c2 += 2) {
            f32x2 hf[4];
#pragma unroll
            for (int i = 0; i < 4; ++i) { const unsigned e = uv[c2 + (i >> 1)][i & 1]; hf[i] = __builtin_elementwise_fma((f32x2){bflo(e), bfhi(e)}, dk2, (f32x2){acc[c2 + (i >> 1)][2 * (i & 1)], acc[c2 + (i >> 1)][2 * (i & 1) + 1]}); }
#pragma unroll
            for (int i = 0; i < 4; ++i) { const unsigned e = xv[c2 + (i >> 1)][i & 1]; hf[i] += (f32x2){bflo(e), bfhi(e)}; }
#pragma unroll
            for (int j = 0; j < 2; ++j) *(LAS u32x2*)(yb + (256 * (c2 + j) + 16 * n + 4 * h) * 2) = (u32x2){pk_bf16(hf[2 * j][0], hf[2 * j][1]), pk_bf16(hf[2 * j + 1][0], hf[2 * j + 1][1])};
        }
        asm volatile("" ::: "memory");
#pragma unroll
        for (int j = 0; j < 4; ++j) st_dev_o(ws + OFF_HT + (size_t)d * (T * 2) + j * 1024, lane * 16, *(LAS u32x4*)(yb + (64 * j + lane) * 16));
        STAMP(27);
        wait_vm<0>();
    } else if (w == 6) wait_flags<96, FL_W_S>(fl + (b & 7) * FL_REP + FL_WG, tag, lane);
    bar();
    if (tid < 8) __hip_atomic_fetch_add(fl + FL_HC + (tid * 32 + (b & 31)) * 32, 1u, __ATOMIC_RELAXED, __HIP_MEMORY_SCOPE_AGENT);
    STAMP(5);
    return tag;
}

__device__ __forceinline__ unsigned spare_wg(const Params& P, int j, int cb, lptr sm PROF_ARG) {
    const int tid = threadIdx.x, lane = tid & 63, w = uni(tid >> 6);
    unsigned* fl = (unsigned*)(P.ws + OFF_FL);
    unsigned tag;
    auto publish = [&](int word) { wait_vm<0>(); bar(); if (tid < 8) set_flag_rep(fl + word, tag, tid); };
    if (j < 64) {
        u32x4 vx[5], vw[5];
        ln1_load(j, P.x, vx);
        tag = epoch_tag(P.ws);
        ln1_unit(j, vx, P.ws, tag PROF_FWD);
        if (j < 32) {
            wgtile_load(64 + j, P.wg, P.wu, vw);
            asm volatile("" ::: "memory");
            wait_vm<5>(); bar();
            if (tid < 8) set_flag_rep(fl + FL_LN + j * FL_LN_S, tag, tid);
            STAMP(1);
            wgtile_unit(64 + j, vw, P.ws, sm);
            publish(FL_WG + (64 + j) * FL_W_S);
        } else if (j < 48) {
            wdtile_load(j, P.wd, vw);
            asm volatile("" ::: "memory");
            wait_vm<5>(); bar();
            if (tid < 8) set_flag_rep(fl + FL_LN + j * FL_LN_S, tag, tid);
            STAMP(1);
            wdtile_unit(j, vw, P.ws, sm);
            publish(FL_WD + j * FL_W_S);
        } else {
            publish(FL_LN + j * FL_LN_S);
            STAMP(1);
        }
    } else {
        u32x4 va[5], vb[5], vc[5];
#define W_DELAY 100
        if (W_DELAY) { const unsigned long long t0 = wall_clock64(); while (wall_clock64() - t0 < W_DELAY) __builtin_amdgcn_s_sleep(2); }
        wgtile_load(2 * (j - 64), P.wg, P.wu, va);
        wgtile_load(2 * (j - 64) + 1, P.wg, P.wu, vb);
        wdtile_load(j - 64, P.wd, vc);
        tag = epoch_tag(P.ws);
        STAMP(1);
        wgtile_unit(2 * (j - 64), va, P.ws, sm);
        publish(FL_WG + 2 * (j - 64) * FL_W_S);
        wgtile_unit(2 * (j - 64) + 1, vb, P.ws, sm);
        publish(FL_WG + (2 * (j - 64) + 1) * FL_W_S);
        wdtile_unit(j - 64, vc, P.ws, sm);
        publish(FL_WD + (j - 64) * FL_W_S);
    }
    STAMP(2);
    if (w == 6) wait_flags<96, FL_W_S>(fl + (blockIdx.x & 7) * FL_REP + FL_WG, tag, lane);
    bar();
    STAMP(5);
    return tag;
}

template <int WC>
struct G1Compute {
    f32x16 acc[3][2];
    bf16x8 Af[2][3], Bf[2][2];
    template <int P> __device__ __forceinline__ void mm(int ft, int tt) {
        acc[ft][tt] = __builtin_amdgcn_mfma_f32_32x32x16_bf16(Af[P][ft], Bf[P][tt], acc[ft][tt], 0, 0, 0); PIN(acc[ft][tt]); SB();
    }
    __device__ __forceinline__ static bf16x8 rdB(lptr p) { return __builtin_bit_cast(bf16x8, tr_read2(p, 8 * 256)); }
    template <int P, int KK, bool MORE, bool BAR>
    __device__ __forceinline__ void substep(lptr pa, const lptr (&pb)[2]) {
        mm<P>(0, 0);
        if (BAR) { sbar(); SB(); }
        if (MORE) { Bf[P ^ 1][0] = rdB(pb[0] + KK * 16 * 256); Af[P ^ 1][0] = *(LAS bf16x8*)(pa + KK * 6144); SB(); }
        mm<P>(0, 1);
        if (MORE) { Bf[P ^ 1][1] = rdB(pb[1] + KK * 16 * 256); Af[P ^ 1][1] = *(LAS bf16x8*)(pa + KK * 6144 + 1024); SB(); }
        mm<P>(1, 0);
        if (MORE) { Af[P ^ 1][2] = *(LAS bf16x8*)(pa + KK * 6144 + 2048); SB(); }
        mm<P>(1, 1);
        mm<P>(2, 0);
        mm<P>(2, 1);
    }
};

template <int WC>
__device__ __forceinline__ void g1_compute(lptr sm, char* __restrict__ ws, int wr, int lane, int t0, int cb PROF_ARG) {
    const int g = lane >> 4, q = (lane & 15) >> 2, p = lane & 3, hh = g >> 1, krow = 4 * hh + q;
    G1Compute<WC> C;
#pragma unroll
    for (int ft = 0; ft < 3; ++ft)
#pragma unroll
        for (int tt = 0; tt < 2; ++tt) C.acc[ft][tt] = (f32x16){};
    const lptr a0 = sm + G1_W + 3 * WC * 1024 + lane * 16;
    lptr b0[2];
#pragma unroll
    for (int tt = 0; tt < 2; ++tt) b0[tt] = sm + krow * 256 + (((2 * wr + tt) ^ q) * 64) + (g & 1) * 32 + p * 8;
    sbar();
    STAMP(7); CSTAMP(18);
#pragma unroll
    for (int tt = 0; tt < 2; ++tt) C.Bf[0][tt] = C.rdB(b0[tt]);
#pragma unroll
    for (int ft = 0; ft < 3; ++ft) C.Af[0][ft] = *(LAS bf16x8*)(a0 + ft * 1024);
    SB();
    lptr pa = a0, pb[2] = {b0[0], b0[1]};
    int nb = 0;
#pragma unroll 1
    for (int s = 0; s < 19; ++s) {
        C.template substep<0, 1, true, false>(pa, pb);
        nb = nb + G1_SS == G1_NS * G1_SS ? 0 : nb + G1_SS;
        pa = a0 + nb; pb[0] = b0[0] + nb; pb[1] = b0[1] + nb;
        SB();
        C.template substep<1, 0, true, true>(pa, pb);
    }
    C.template substep<0, 1, true, false>(pa, pb);
    C.template substep<1, 0, false, false>(pa, pb);
    STAMP(8); CSTAMP(19);
    bar();
    char* mF = ws + OFF_MF;
    f32x2 nm2[2], k1[2], k2[2], s2[2];
#pragma unroll
    for (int tt = 0; tt < 2; ++tt) {
        const f32x2 sq = *(LAS f32x2*)(sm + G1_ST + (32 * (2 * wr + tt) + (lane & 31)) * 8);
        const float rs = __builtin_amdgcn_rcpf(sq[1]);
        nm2[tt] = (f32x2){-sq[0], -sq[0]};
        k1[tt] = (f32x2){-2.302208198f * rs, -2.302208198f * rs};
        k2[tt] = (f32x2){-0.1029432397f * rs * rs * rs, -0.1029432397f * rs * rs * rs};
        s2[tt] = (f32x2){sq[1] * sq[1], sq[1] * sq[1]};
    }
    const unsigned mo = (unsigned)((((t0 + 64 * wr + (lane & 31)) >> 4) * 48 * 64 + ((lane & 15) + 16 * hh)) * 16);
#pragma unroll
    for (int ft = 0; ft < 3; ++ft) {
        const int fl = 16 * (3 * WC + ft) + 4 * hh;
        lptr cp = sm + G1_CS + 3072 + fl * 4;
        const f32x4 cg0 = *(LAS f32x4*)cp, cg1 = *(LAS f32x4*)(cp + 32), cu0 = *(LAS f32x4*)(cp + 384), cu1 = *(LAS f32x4*)(cp + 416);
        const int ftg = 6 * cb + 3 * WC + ft;
        const char* mb = mF + (size_t)((ftg >> 1) * 64 + 32 * (ftg & 1)) * 16;
#pragma unroll
        for (int tt = 0; tt < 2; ++tt) {
            f32x2 G[4], U[4], E[4];
#pragma unroll
            for (int r = 0; r < 4; ++r) {
                const f32x2 cg = r < 2 ? (f32x2){cg0[2 * r], cg0[2 * r + 1]} : (f32x2){cg1[2 * r - 4], cg1[2 * r - 3]};
                G[r] = __builtin_elementwise_fma(nm2[tt], cg, (f32x2){C.acc[ft][tt][2 * r], C.acc[ft][tt][2 * r + 1]}); PIN(G[r]);
            }
            SB();
#pragma unroll
            for (int r = 0; r < 4; ++r) { E[r] = G[r] * G[r]; PIN(E[r]); }
            SB();
#pragma unroll
            for (int r = 0; r < 4; ++r) { E[r] = __builtin_elementwise_fma(E[r], k2[tt], k1[tt]); PIN(E[r]); }
            SB();
#pragma unroll
            for (int r = 0; r < 4; ++r) { E[r] = G[r] * E[r]; PIN(E[r]); }
            SB();
#pragma unroll
            for (int r = 0; r < 4; ++r) {
                const f32x2 cu = r < 2 ? (f32x2){cu0[2 * r], cu0[2 * r + 1]} : (f32x2){cu1[2 * r - 4], cu1[2 * r - 3]};
                E[r] = (f32x2){__builtin_amdgcn_exp2f(E[r][0]), __builtin_amdgcn_exp2f(E[r][1])}; PIN(E[r]); SB();
                U[r] = __builtin_elementwise_fma(nm2[tt], cu, (f32x2){C.acc[ft][tt][8 + 2 * r], C.acc[ft][tt][9 + 2 * r]}); PIN(U[r]); SB();
            }
#pragma unroll
            for (int r = 0; r < 4; ++r) { E[r] = __builtin_elementwise_fma(E[r], s2[tt], s2[tt]); PIN(E[r]); }
            SB();
#pragma unroll
            for (int r = 0; r < 4; ++r) {
                E[r] = (f32x2){__builtin_amdgcn_rcpf(E[r][0]), __builtin_amdgcn_rcpf(E[r][1])}; PIN(E[r]); SB();
                G[r] = G[r] * U[r]; PIN(G[r]); SB();
            }
#pragma unroll
            for (int r = 0; r < 4; ++r) { G[r] = G[r] * E[r]; PIN(G[r]); }
            SB();
            u32x4 o;
#pragma unroll
            for (int r = 0; r < 4; ++r) o[r] = pk_bf16(G[r][0], G[r][1]);
            st_dev_o(mb, mo + tt * (2 * 48 * 1024), o);
            SB();
        }
    }
}

__device__ __forceinline__ void g1_loader(char* __restrict__ ws, lptr sm, int lw, int lane, int t0, int cb, int t2, int db, unsigned tag) {
    const unsigned sm0 = (unsigned)(size_t)sm;
    {
        const char* hT = ws + OFF_HT;
        const char* wsrc = ws + OFF_WG + (size_t)cb * 40 * 6144 + 3 * lw * 1024;
        unsigned vh[2];
#pragma unroll
        for (int j = 0; j < 2; ++j) {
            const int o = (2 * lw + j) * 1024 + lane * 16, k = o >> 8, c = ((o >> 4) & 15) ^ ((k & 3) << 2);
            vh[j] = (unsigned)(k * (T * 2) + t0 * 2 + c * 16);
        }
        const unsigned vl = lane * 16;
        unsigned long long* st = (unsigned long long*)(ws + OFF_ST);
        auto stage = [&](int s) {
            const unsigned lb = sm0 + (s % G1_NS) * G1_SS;
            const char* hs = hT + (size_t)s * 32 * (T * 2);
            const char* wsx = wsrc + (size_t)s * 12288;
            dma16s(hs, vh[0], lb + (2 * lw) * 1024);
            if (s >= 4) dma16s(wsx, vl, lb + G1_W + (3 * lw) * 1024);
            dma16s(hs, vh[1], lb + (2 * lw + 1) * 1024);
            if (s >= 4) { dma16s(wsx + 1024, vl, lb + G1_W + (3 * lw + 1) * 1024); dma16s(wsx + 2048, vl, lb + G1_W + (3 * lw + 2) * 1024); }
        };
        u32x4 gv[3];
        unsigned long long g0, g1;
        float cs[8], sv[16];
#pragma unroll
        for (int s = 0; s < 4; ++s) stage(s);
        {
            const char* hp = hT + (size_t)(160 * lw) * (T * 2) + (t0 + 8 * cb) * 2;
            const unsigned go = (unsigned)lane * (T * 2);
            asm volatile("global_load_dwordx4 %0, %1, %2 sc1" : "=v"(gv[0]) : "v"(go), "s"(hp) : "memory");
            asm volatile("global_load_dwordx4 %0, %1, %2 sc1" : "=v"(gv[1]) : "v"(go), "s"(hp + (size_t)64 * (T * 2)) : "memory");
            asm volatile("global_load_dwordx4 %0, %1, %2 sc1" : "=v"(gv[2]) : "v"(lane < 32 ? go : 0u), "s"(hp + (size_t)128 * (T * 2)) : "memory");
        }
        wait_vm<9>();
        sbar();
#pragma unroll
        for (int s = 0; s < 19; ++s) {
            const int last = (s + 3) < 19 ? (s + 3) : 19;
            if (s == 3) asm volatile("s_waitcnt vmcnt(10)" : "+v"(gv[0]), "+v"(gv[1]), "+v"(gv[2]) :: "memory");
            else if (s == 17) asm volatile("s_waitcnt vmcnt(13)" : "+v"(g0), "+v"(g1) :: "memory");
            else if (s == 18) wait_vm<8>();
            else wait_vm_rt<30>(s == 0 ? 7 : (s == 1 ? 10 : (s == 2 ? 13 : 5 * (last - (s + 1)) + (s == 15 || s == 16 ? 2 : 0))));
            sbar();
            if (s + 4 < 20) stage(s + 4);
            if (s == 11 && lw < 3) {
                lptr cp = sm + G1_CS + lw * 256 + lane * 4;
                *(LAS float*)(cp + 3072) = (*(LAS float*)cp + *(LAS float*)(cp + 768)) + (*(LAS float*)(cp + 1536) + *(LAS float*)(cp + 2304));
                asm volatile("s_waitcnt lgkmcnt(0)" ::: "memory");
            }
            if (s == 16) {
#pragma unroll
                for (int j = 0; j < 3; ++j) {
                    const int i = lw + 4 * j < 10 ? lw + 4 * j : lw;
                    const int o = i * 1024 + lane * 16, row = o >> 7, c = ((o >> 4) & 7) ^ (((row >> 1) & 3) << 1);
                    dma16s(ws + OFF_HT, (unsigned)((80 * db + row) * (T * 2) + t2 * 2 + c * 16), sm0 + G2_H + i * 1024);
                }
            }
            if (s == 16 || s == 18) {
                const int s2 = s == 16 ? 0 : 1;
                const char* wd = ws + OFF_WD + (size_t)(4 * s2 + lw) * (40 * 1024) + (size_t)(5 * db) * 1024;
#pragma unroll
                for (int j = 0; j < 5; ++j) dma16s(wd + j * 1024, vl, sm0 + s2 * G2_SS + G2_W + (5 * lw + j) * 1024);
            }
            if (s == 14) {
                asm volatile("global_load_dwordx2 %0, %1, %2 sc1" : "=v"(g0) : "v"(lane * 8), "s"(st + t0) : "memory");
                asm volatile("global_load_dwordx2 %0, %1, %2 sc1" : "=v"(g1) : "v"(lane * 8), "s"(st + t0 + 64) : "memory");
            }
            if (s >= 3 && s <= 6) {
#pragma unroll
                for (int e = 2 * (s - 3); e < 2 * (s - 3) + 2; ++e) {
                    const unsigned c32 = (unsigned)__builtin_amdgcn_readlane((int)gv[0][e >> 1], 0);
                    cs[e] = (e & 1) ? bfhi(c32) : bflo(c32);
                    const float d0 = ((e & 1) ? bfhi(gv[0][e >> 1]) : bflo(gv[0][e >> 1])) - cs[e], d1 = ((e & 1) ? bfhi(gv[1][e >> 1]) : bflo(gv[1][e >> 1])) - cs[e];
                    const float d2 = lane < 32 ? ((e & 1) ? bfhi(gv[2][e >> 1]) : bflo(gv[2][e >> 1])) - cs[e] : 0.f;
                    sv[e] = d0 + d1 + d2; sv[8 + e] = d0 * d0 + d1 * d1 + d2 * d2;
                    PIN(sv[e]); PIN(sv[8 + e]);
                }
            }
            if (s == 7) {
#pragma unroll
                for (int j = 0; j < 8; ++j) {
                    const auto r = __builtin_amdgcn_permlane32_swap(__float_as_uint(sv[j]), __float_as_uint(sv[j + 8]), false, false);
                    sv[j] = __uint_as_float(r[0]) + __uint_as_float(r[1]); PIN(sv[j]);
                }
            }
            if (s == 8) {
#pragma unroll
                for (int k = 0; k < 4; ++k) {
                    const auto r = __builtin_amdgcn_permlane16_swap(__float_as_uint(sv[k]), __float_as_uint(sv[k + 4]), false, false);
                    sv[k] = __uint_as_float(r[0]) + __uint_as_float(r[1]);
                }
#pragma unroll
                for (int k = 0; k < 4; ++k) { sv[k] = row16_sum(sv[k]); PIN(sv[k]); }
            }
            if (s == 9) {
                if ((lane & 15) == 0) *(LAS f32x4*)(sm + G1_SP + lw * 96 + (lane >> 4) * 16) = (f32x4){sv[0], sv[1], sv[2], sv[3]};
                if (lane == 0) { *(LAS f32x4*)(sm + G1_SP + lw * 96 + 64) = (f32x4){cs[0], cs[1], cs[2], cs[3]}; *(LAS f32x4*)(sm + G1_SP + lw * 96 + 80) = (f32x4){cs[4], cs[5], cs[6], cs[7]}; }
                asm volatile("s_waitcnt lgkmcnt(0)" ::: "memory");
            }
            if (s == 10 && lw == 3 && lane < 8) {
                float pm[4], m2 = 0.f, mean = 0.f;
#pragma unroll
                for (int i = 0; i < 4; ++i) {
                    const float a = *(LAS float*)(sm + G1_SP + i * 96 + lane * 4) * (1.0f / 160.0f), q = *(LAS float*)(sm + G1_SP + i * 96 + 32 + lane * 4);
                    pm[i] = *(LAS float*)(sm + G1_SP + i * 96 + 64 + lane * 4) + a;
                    m2 += q - 160.0f * a * a; mean += 0.25f * pm[i];
                }
#pragma unroll
                for (int i = 0; i < 4; ++i) m2 += 160.0f * (pm[i] - mean) * (pm[i] - mean);
                const float var = fmaxf(m2 * (1.0f / (float)D), 0.f);
                const unsigned mb = pk_bf16(mean, rbf(sqrtf(rbf(rbf(var) + 1e-5f))));
                __hip_atomic_store(st + t0 + 8 * cb + lane, ((unsigned long long)tag << 32) | mb, __ATOMIC_RELAXED, __HIP_MEMORY_SCOPE_AGENT);
            }
        }
        if (lw == 0) {
            for (unsigned spins = 0; !__all((unsigned)(g0 >> 32) == tag && (unsigned)(g1 >> 32) == tag) && spins < (1u << 18); ++spins) {
                __builtin_amdgcn_s_sleep(2);
                g0 = __hip_atomic_load(st + t0 + lane, __ATOMIC_RELAXED, __HIP_MEMORY_SCOPE_AGENT);
                g1 = __hip_atomic_load(st + t0 + 64 + lane, __ATOMIC_RELAXED, __HIP_MEMORY_SCOPE_AGENT);
            }
            *(LAS f32x2*)(sm + G1_ST + lane * 8) = (f32x2){bflo((unsigned)g0), bfhi((unsigned)g0)};
            *(LAS f32x2*)(sm + G1_ST + (64 + lane) * 8) = (f32x2){bflo((unsigned)g1), bfhi((unsigned)g1)};
        }
        bar();
    }
}

template <int ND>
__device__ __forceinline__ void g2_compute(lptr sm, int lane, int th, int dl0, int t0, int d0, bf16_t* __restrict__ out PROF_ARG) {
    f32x4 acc[ND][2] = {};
    bf16x8 A[2][ND], B[2][2];
    const lptr pb0 = sm + lane * 16 + 2 * th * 4096, pa0 = sm + lane * 16 + G2_W + dl0 * 1024;
    auto ld = [&](int P, lptr pa, lptr pb, int fl) {
#pragma unroll
        for (int tt = 0; tt < 2; ++tt) B[P][tt] = *(LAS bf16x8*)(pb + (tt * 4 + fl) * 1024);
#pragma unroll
        for (int dt = 0; dt < ND; ++dt) A[P][dt] = *(LAS bf16x8*)(pa + (fl * 5 + dt) * 1024);
        SB();
    };
    auto mm = [&](int P) {
#pragma unroll
        for (int dt = 0; dt < ND; ++dt)
#pragma unroll
            for (int tt = 0; tt < 2; ++tt) { acc[dt][tt] = __builtin_amdgcn_mfma_f32_16x16x32_bf16(A[P][dt], B[P][tt], acc[dt][tt], 0, 0, 0); PIN(acc[dt][tt]); SB(); }
    };
    sbar();
    STAMP(11); CSTAMP(20);
    ld(0, pa0, pb0, 0);
    lptr pa = pa0, pb = pb0;
    int nb = 0;
#pragma unroll 1
    for (int s = 0; s < 12; ++s) {
        ld(1, pa, pb, 1); mm(0);
        ld(0, pa, pb, 2); mm(1);
        ld(1, pa, pb, 3); mm(0);
        if (s < 11) {
            nb = nb + G2_SS == G2_NS * G2_SS ? 0 : nb + G2_SS;
            pa = pa0 + nb; pb = pb0 + nb;
            bar();
            ld(0, pa, pb, 0);
        }
        mm(1);
    }
    STAMP(12); CSTAMP(21);
    bar();
    const int g = lane >> 4, q = (lane & 15) >> 2, p = lane & 3;
#pragma unroll
    for (int dt = 0; dt < ND; ++dt)
#pragma unroll
        for (int tt = 0; tt < 2; ++tt) {
            const int row = 16 * (dl0 + dt) + 4 * g + q, seg = (2 * th + tt) ^ ((row >> 1) & 3);
            const u32x2 hv = tr_read(sm + G2_H + row * 128 + seg * 32 + p * 8);
            const unsigned o01 = pk_bf16(acc[dt][tt][0], acc[dt][tt][1]), o23 = pk_bf16(acc[dt][tt][2], acc[dt][tt][3]);
            const u32x2 r = {pk_bf16(bflo(hv[0]) + bflo(o01), bfhi(hv[0]) + bfhi(o01)), pk_bf16(bflo(hv[1]) + bflo(o23), bfhi(hv[1]) + bfhi(o23))};
            *(LAS u32x2*)(sm + (16 * (2 * th + tt) + (lane & 15)) * 176 + (16 * (dl0 + dt) + 4 * g) * 2) = r;
        }
}

extern "C" __global__ void s4d_geglu_block_end();
extern "C" __global__ __launch_bounds__(512, 2) void s4d_geglu_block(Params P PROF_ARG) {
    extern __shared__ __attribute__((aligned(16))) char smem[];
    lptr sm = (lptr)smem;
    STAMP(0);
    const unsigned long long probe_t0 = wall_clock64();
    if (uni(threadIdx.x >> 6) == 7) {
        unsigned long long pc; unsigned off, len;
        asm volatile("s_getpc_b64 %0\n.Lpc_anchor_%=:\n\ts_mov_b32 %1, .Lpc_anchor_%=-s4d_geglu_block\n\ts_mov_b32 %2, s4d_geglu_block_end-s4d_geglu_block" : "=s"(pc), "=s"(off), "=s"(len));
        const unsigned o = (blockIdx.x >> 3) * 2048 + (threadIdx.x & 63) * 32;
        if (o < len) dma4((const void*)(pc - off), o, (unsigned)(size_t)sm + LDS_BYTES - 256);
    }
    const int tid = threadIdx.x, lane = tid & 63, w = uni(tid >> 6), b = blockIdx.x;
    const unsigned sm0 = (unsigned)(size_t)sm;
    char* ws = P.ws;
    unsigned* fl = (unsigned*)(ws + OFF_FL);
    unsigned tag;
    const int grp = b & 7, idx = b >> 3, grp0 = grp;
    const int rb = (grp & 1) * 8 + (idx & 7), cb = (grp >> 1) * 4 + (idx >> 3);
    const int tb = (grp >> 1) * 8 + (idx & 7), db = (grp & 1) * 4 + (idx >> 3);

    if (b < 160) tag = conv_wg(P, b, cb, sm PROF_FWD); else tag = spare_wg(P, b - 160, cb, sm PROF_FWD);

    if (w >= 4) g1_prefetch_w(ws, sm, cb, w & 3, lane);
    if (w == 4) wait_flags<32, 32>(fl + FL_HC + grp0 * 32 * 32, (tag << 3) + 5u, lane);
    if (w == 7) wait_flags<48, FL_W_S>(fl + grp0 * FL_REP + FL_WD, tag, lane);
    if (PROBE_AT == 1) { while (wall_clock64() - probe_t0 < PROBE_T) __builtin_amdgcn_s_sleep(1); }
    bar();
    STAMP(6);
    if (w >= 4) g1_loader(ws, sm, w & 3, lane, 128 * rb, cb, 64 * tb, db, tag);
    else if (w & 1) g1_compute<1>(sm, ws, w >> 1, lane, 128 * rb, cb PROF_FWD); else g1_compute<0>(sm, ws, w >> 1, lane, 128 * rb, cb PROF_FWD);

    const int t0 = 64 * tb, d0 = 80 * db;
    if (w >= 4) {
        const int lw = w & 3;
        const char* ms = ws + OFF_MF + (size_t)(4 * tb + lw) * 48 * 1024;
        const char* wsrc = ws + OFF_WD + (size_t)lw * (40 * 1024) + (size_t)(5 * db) * 1024;
        const unsigned vl = lane * 16;
        auto stage_w = [&](int s) {
#pragma unroll
            for (int j = 0; j < 5; ++j) dma16s(wsrc + (size_t)(4 * s) * (40 * 1024) + j * 1024, vl, sm0 + (s % G2_NS) * G2_SS + G2_W + (5 * lw + j) * 1024);
        };
        auto stage_m = [&](int s) {
#pragma unroll
            for (int j = 0; j < 4; ++j) dma16s(ms + (size_t)(4 * s + j) * 1024, vl, sm0 + (s % G2_NS) * G2_SS + (4 * lw + j) * 1024);
        };
        stage_w(2);
        sbar();
        if (lw == 0) wait_flags<16, FL_M_S>(fl + FL_M + 16 * (tb >> 1) * FL_M_S, tag, lane);
        sbar();
        STAMPL(10);
        stage_m(0); stage_m(1); stage_m(2);
        wait_vm<8>();
        sbar();
#pragma unroll
        for (int s = 0; s < 11; ++s) {
            if (s == 0) wait_vm<4>(); else if (s <= 9) wait_vm<9>(); else wait_vm<0>();
            sbar();
            if (s + 3 < 12) { stage_m(s + 3); stage_w(s + 3); }
        }
        sbar();
    } else {
        STAMP(28);
        wait_vm<0>();
        if (PROBE_AT == 2) { while (wall_clock64() - probe_t0 < PROBE_T) __builtin_amdgcn_s_sleep(1); }
        sbar();
        if (tid == 0) set_flag(fl + FL_M + (16 * rb + cb) * FL_M_S, tag);
        STAMP(9);
        sbar();
        const int th = w & 1;
        if (w >> 1) g2_compute<2>(sm, lane, th, 3, t0, d0, P.out PROF_FWD); else g2_compute<3>(sm, lane, th, 0, t0, d0, P.out PROF_FWD);
    }
    bar();
    for (int ch = tid; ch < 640; ch += 512) {
        const int row = ch / 10, c = ch % 10;
        *(u32x4*)(P.out + (size_t)(t0 + row) * D + d0 + c * 8) = *(LAS u32x4*)(sm + row * 176 + c * 16);
    }
    if (PROBE_AT == 0) { while (wall_clock64() - probe_t0 < PROBE_T) __builtin_amdgcn_s_sleep(1); }
    if (b == 0 && tid == 0) fl[FL_E] = tag;
    STAMP(15);
}

extern "C" __global__ void s4d_geglu_block_end() {}

extern "C" void kernel_launch(void* const* d_in, const int* in_sizes, int n_in, void* d_out, int out_size, void* d_ws, size_t ws_size, hipStream_t stream) {
    Params P;
    P.x = (const bf16_t*)d_in[0]; P.logA = (const bf16_t*)d_in[1]; P.Aim = (const bf16_t*)d_in[2]; P.Cre = (const bf16_t*)d_in[3]; P.Cim = (const bf16_t*)d_in[4];
    P.logdt = (const bf16_t*)d_in[5]; P.Dskip = (const bf16_t*)d_in[6]; P.wg = (const bf16_t*)d_in[8]; P.wu = (const bf16_t*)d_in[9]; P.wd = (const bf16_t*)d_in[10];
    P.ws = (char*)d_ws; P.out = (bf16_t*)d_out;
    (void)hipFuncSetAttribute((const void*)s4d_geglu_block, hipFuncAttributeMaxDynamicSharedMemorySize, LDS_BYTES);
    s4d_geglu_block<<<256, 512, LDS_BYTES, stream>>>(P);
}
```

```cpp
#include <hip/hip_runtime.h>
#include <stdint.h>

typedef unsigned short bf16_t;
typedef short s16x4 __attribute__((ext_vector_type(4)));
typedef __bf16 bf16x8 __attribute__((ext_vector_type(8)));
typedef __bf16 bf16x2 __attribute__((ext_vector_type(2)));
typedef _Float16 h16x8 __attribute__((ext_vector_type(8)));
typedef _Float16 h16x2 __attribute__((ext_vector_type(2)));
typedef float f32x2 __attribute__((ext_vector_type(2)));
typedef float f32x4 __attribute__((ext_vector_type(4)));
typedef float f32x16 __attribute__((ext_vector_type(16)));
typedef unsigned u32x2 __attribute__((ext_vector_type(2)));
typedef unsigned u32x4 __attribute__((ext_vector_type(4)));
#define LAS __attribute__((address_space(3)))
typedef LAS char* lptr;

constexpr int T = 2048, D = 640, NM = 192, F = 1536;
constexpr size_t OFF_S1 = 0;
constexpr size_t OFF_HT = OFF_S1 + (size_t)T * 8;
constexpr size_t OFF_CS = OFF_HT + (size_t)T * D * 2;
constexpr size_t OFF_MF = OFF_CS + 65536;
constexpr size_t OFF_WD = OFF_MF + (size_t)T * F * 2;
constexpr size_t OFF_WG = OFF_WD + (size_t)F * D * 2;
constexpr size_t OFF_ST = OFF_WG + (size_t)2 * F * D * 2;
constexpr size_t OFF_FL = OFF_ST + (size_t)T * 8;
constexpr size_t OFF_PROF = OFF_FL + 196608;
constexpr int FL_E = 0;
constexpr int FL_REP = 4096;
constexpr int FL_LN = FL_REP, FL_LN_S = 16;
constexpr int FL_WG = FL_LN + 1024, FL_WD = FL_WG + 96 * 8, FL_W_S = 8;
constexpr int FL_M = 64, FL_M_S = 8;
constexpr int FL_HC = 9 * FL_REP;
static_assert(FL_WD + 48 * FL_W_S <= 2 * FL_REP && FL_M + 256 * FL_M_S <= FL_REP, "flag map");

#define STAMP(i)
#define STAMPL(i)
#define CSTAMP(i)
#define PROF_ARG
#define PROF_FWD

__device__ __forceinline__ float bf2f(unsigned v) { return __uint_as_float(v << 16); }
__device__ __forceinline__ float bflo(unsigned v) { return __uint_as_float(v << 16); }
__device__ __forceinline__ float bfhi(unsigned v) { return __uint_as_float(v & 0xffff0000u); }
__device__ __forceinline__ unsigned pk_bf16(float lo, float hi) { f32x2 v = {lo, hi}; return __builtin_bit_cast(unsigned, __builtin_convertvector(v, bf16x2)); }
__device__ __forceinline__ float rbf(float f) { return bflo(pk_bf16(f, f)); }
__device__ __forceinline__ unsigned pk_f16(f32x2 v) { return __builtin_bit_cast(unsigned, __builtin_convertvector(v, h16x2)); }
template <int CTRL> __device__ __forceinline__ float dppf(float x) { return __int_as_float(__builtin_amdgcn_update_dpp(0, __float_as_int(x), CTRL, 0xF, 0xF, true)); }
__device__ __forceinline__ float row16_sum(float x) { x += dppf<0xB1>(x); x += dppf<0x4E>(x); x += dppf<0x141>(x); x += dppf<0x140>(x); return x; }
__device__ __forceinline__ float row16_max(float x) { x = fmaxf(x, dppf<0xB1>(x)); x = fmaxf(x, dppf<0x4E>(x)); x = fmaxf(x, dppf<0x141>(x)); x = fmaxf(x, dppf<0x140>(x)); return x; }
__device__ __forceinline__ float xsum16_32(float x) {
    const unsigned u = __float_as_uint(x);
    const auto r = __builtin_amdgcn_permlane16_swap(u, u, false, false);
    const unsigned v = __float_as_uint(__uint_as_float(r[0]) + __uint_as_float(r[1]));
    const auto q = __builtin_amdgcn_permlane32_swap(v, v, false, false);
    return __uint_as_float(q[0]) + __uint_as_float(q[1]);
}
__device__ __forceinline__ u32x2 tr_read(lptr p) { return __builtin_bit_cast(u32x2, __builtin_amdgcn_ds_read_tr16_b64_v4i16((LAS s16x4*)p)); }
__device__ __forceinline__ u32x4 tr_read2(lptr p, int off2) { const u32x2 r1 = tr_read(p), r2 = tr_read(p + off2); return (u32x4){r1[0], r1[1], r2[0], r2[1]}; }
__device__ __forceinline__ int uni(int v) { return __builtin_amdgcn_readfirstlane(v); }

__device__ __forceinline__ void dma16(const void* sbase, unsigned voff, unsigned ldsaddr) {
    asm volatile("s_mov_b32 m0, %2\n\ts_nop 0\n\tglobal_load_lds_dwordx4 %0, %1" :: "v"(voff), "s"(sbase), "s"(ldsaddr) : "memory");
}
__device__ __forceinline__ void dma4(const void* sbase, unsigned voff, unsigned ldsaddr) {
    asm volatile("s_mov_b32 m0, %2\n\ts_nop 0\n\tglobal_load_lds_dword %0, %1" :: "v"(voff), "s"(sbase), "s"(ldsaddr) : "memory");
}
template <int N> __device__ __forceinline__ void wait_vm() { asm volatile("s_waitcnt vmcnt(%0)" :: "n"(N) : "memory"); }
template <int N> __device__ __forceinline__ void wait_vm_rt(int n) {
    if (n >= N) wait_vm<N>(); else if constexpr (N > 0) wait_vm_rt<N - 1>(n);
}
__device__ __forceinline__ void bar() { asm volatile("s_waitcnt lgkmcnt(0)\n\ts_barrier" ::: "memory"); }
__device__ __forceinline__ void sbar() { asm volatile("s_barrier" ::: "memory"); }
#define SB() __builtin_amdgcn_sched_barrier(0)
#define PIN(x) asm volatile("" : "+v"(x))
__device__ __forceinline__ void st_dev(void* p, u32x4 v) { asm volatile("global_store_dwordx4 %0, %1, off sc1\n\ts_nop 1" :: "v"(p), "v"(v) : "memory"); }
__device__ __forceinline__ void st_dev_o(const void* sbase, unsigned voff, u32x4 v) { asm volatile("global_store_dwordx4 %0, %1, %2 sc1\n\ts_nop 1" :: "v"(voff), "v"(v), "s"(sbase) : "memory"); }
__device__ __forceinline__ void st_dev4(void* p, float v) { asm volatile("global_store_dword %0, %1, off sc1\n\ts_nop 1" :: "v"(p), "v"(v) : "memory"); }
__device__ __forceinline__ void dma16s(const void* sbase, unsigned voff, unsigned ldsaddr) {
    asm volatile("s_mov_b32 m0, %2\n\ts_nop 0\n\tglobal_load_lds_dwordx4 %0, %1 sc1" :: "v"(voff), "s"(sbase), "s"(ldsaddr) : "memory");
}
__device__ __forceinline__ void dma4s(const void* sbase, unsigned voff, unsigned ldsaddr) {
    asm volatile("s_mov_b32 m0, %2\n\ts_nop 0\n\tglobal_load_lds_dword %0, %1 sc1" :: "v"(voff), "s"(sbase), "s"(ldsaddr) : "memory");
}
__device__ __forceinline__ void set_flag(unsigned* f, unsigned tag) { __hip_atomic_store(f, tag, __ATOMIC_RELAXED, __HIP_MEMORY_SCOPE_AGENT); }
__device__ __forceinline__ void set_flag_rep(unsigned* f, unsigned tag, int lane) { __hip_atomic_store(f + lane * FL_REP, tag, __ATOMIC_RELAXED, __HIP_MEMORY_SCOPE_AGENT); }
template <int N, int S>
__device__ __forceinline__ void wait_flags(unsigned* f, unsigned tag, int lane) {
    for (unsigned spins = 0; spins < (1u << 18); ++spins) {
        bool ok = true;
        if (lane < N) ok = __hip_atomic_load(f + lane * S, __ATOMIC_RELAXED, __HIP_MEMORY_SCOPE_AGENT) == tag;
        if (N > 64 && lane + 64 < N) ok &= __hip_atomic_load(f + (64 + lane) * S, __ATOMIC_RELAXED, __HIP_MEMORY_SCOPE_AGENT) == tag;
        if (N > 128 && lane + 128 < N) ok &= __hip_atomic_load(f + (128 + lane) * S, __ATOMIC_RELAXED, __HIP_MEMORY_SCOPE_AGENT) == tag;
        if (__all(ok)) break;
        __builtin_amdgcn_s_sleep(1);
    }
    asm volatile("" ::: "memory");
}

__device__ __forceinline__ unsigned epoch_tag(const char* ws) {
    unsigned e;
    asm volatile("s_load_dword %0, %1, 0x0\n\ts_waitcnt lgkmcnt(0)" : "=s"(e) : "s"(ws + OFF_FL + FL_E * 4) : "memory");
    return e + 1u;
}

constexpr int G1_NS = 7, G1_SS = 20480, G1_W = 8192;
constexpr int G2_NS = 4, G2_SS = 36 * 1024, G2_W = 16 * 1024;
constexpr int G2_H = G2_NS * G2_SS;
constexpr int G1_CS = G2_H + 10240;
constexpr int G1_ST = G1_CS + 3840;
constexpr int G1_SP = G1_ST + 1024;
constexpr int LDS_BYTES = 163840;
static_assert(G1_SP + 512 <= LDS_BYTES, "LDS map");
constexpr int KG_ROW = 272;
constexpr int KG_WAVE = 57 * KG_ROW;
constexpr int CV_KCH = 8320;
__host__ __device__ constexpr int cv_k(int c) { return (4 + c) * KG_WAVE + 3072; }
__host__ __device__ constexpr int cv_kc(int c) { return cv_k(c) + CV_KCH; }
static_assert(3072 + CV_KCH + 1296 <= KG_WAVE, "per-channel area");
constexpr int CV_X = 8 * KG_WAVE + 7 * KG_ROW + 16;
constexpr int CV_U = CV_X + 128 * 128;
constexpr int CV_M = CV_U + 144 * 128;
static_assert(CV_X % 128 == 0 || true, "");
static_assert(CV_M + 32 + 256 <= 163840 && CV_X % 16 == 0, "LDS map");
constexpr int LN_ROW = 1344;

__device__ __forceinline__ void ln1_load(int j, const bf16_t* __restrict__ x, u32x4 (&v)[5]) {
    const int tid = threadIdx.x, t = 32 * j + (tid >> 4);
#pragma unroll
    for (int i = 0; i < 5; ++i) v[i] = *(const u32x4*)(x + (size_t)t * D + ((tid & 15) + 16 * i) * 8);
}
__device__ __forceinline__ void ln1_unit(int j, const u32x4 (&v)[5], char* __restrict__ ws, unsigned tag PROF_ARG) {
    const int tid = threadIdx.x, lane = tid & 63, w = uni(tid >> 6);
    f32x2 f[20];
    f32x2 s2 = {0.f, 0.f};
#pragma unroll
    for (int i = 0; i < 5; ++i)
#pragma unroll
        for (int e = 0; e < 4; ++e) { f[4 * i + e] = (f32x2){bflo(v[i][e]), bfhi(v[i][e])}; s2 += f[4 * i + e]; }
    const float mean = row16_sum(s2[0] + s2[1]) * (1.0f / (float)D);
    f32x2 q2 = {0.f, 0.f};
#pragma unroll
    for (int i = 0; i < 20; ++i) { const f32x2 dv = f[i] - mean; q2 += dv * dv; }
    const float var = row16_sum(q2[0] + q2[1]) * (1.0f / (float)D);
    const float rs = __builtin_amdgcn_rcpf(rbf(sqrtf(rbf(rbf(var) + 1e-5f)))), nm = -rbf(mean) * rs;
    u32x4 o;
#pragma unroll
    for (int i = 0; i < 4; ++i) {
        const unsigned a = (unsigned)__builtin_amdgcn_readlane((int)__float_as_uint(rs), 16 * i), c = (unsigned)__builtin_amdgcn_readlane((int)__float_as_uint(nm), 16 * i);
        o[i] = lane == 0 ? a : c;
    }
    if (lane < 2) st_dev(ws + OFF_S1 + (size_t)lane * (T * 4) + (32 * j + 4 * w) * 4, o);
    if (tid < 4) set_flag((unsigned*)(ws + OFF_FL) + FL_HC + (4 * j + tid) * 32, tag << 3);
    STAMP(24);
}

__device__ __forceinline__ void wgtile_load(int b, const bf16_t* __restrict__ wg, const bf16_t* __restrict__ wu, u32x4 (&v)[5]) {
    const int tid = threadIdx.x, j = b >> 2, kq = b & 3, c16 = tid & 15, r0 = tid >> 4;
    const bf16_t* src = (c16 < 8 ? wg : wu) + (size_t)(160 * kq + r0) * F + 64 * j + (c16 & 7) * 8;
#pragma unroll
    for (int i = 0; i < 5; ++i) v[i] = *(const u32x4*)(src + (size_t)(32 * i) * F);
}
__device__ __forceinline__ void wgtile_unit(int b, const u32x4 (&v)[5], char* __restrict__ ws, lptr sm) {
    const int tid = threadIdx.x, lane = tid & 63, w = uni(tid >> 6);
    const int j = b >> 2, kq = b & 3;
    const int c16 = tid & 15, r0 = tid >> 4;
    f32x2 a[4] = {};
#pragma unroll
    for (int i = 0; i < 5; ++i) {
        const int k = 32 * i + r0;
        *(LAS u32x4*)(sm + k * 256 + (((c16 >> 1) ^ (k & 3)) * 32) + (c16 & 1) * 16) = v[i];
#pragma unroll
        for (int e = 0; e < 4; ++e) a[e] += (f32x2){bflo(v[i][e]), bfhi(v[i][e])};
    }
#pragma unroll
    for (int e = 0; e < 4; ++e)
#pragma unroll
        for (int hf = 0; hf < 2; ++hf) {
            const float s = xsum16_32(a[e][hf]);
            if (lane < 16) *(LAS float*)(sm + 40960 + (w * 128 + c16 * 8 + 2 * e + hf) * 4) = s;
        }
    bar();
    const int g = lane >> 4, q = (lane & 15) >> 2, p = lane & 3;
    u32x4 o[5];
#pragma unroll
    for (int i = 0; i < 5; ++i) {
        const int item = w + 8 * i, ftl = item & 3, sub = item >> 2;
        const int k = 16 * sub + 4 * (g >> 1) + q;
        o[i] = tr_read2(sm + k * 256 + ((((g & 1) * 4 + ftl) ^ q) * 32) + p * 8, 8 * 256);
    }
#pragma unroll
    for (int i = 0; i < 5; ++i) {
        const int item = w + 8 * i, ftl = item & 3, sub = item >> 2;
        const int ftg = 4 * j + ftl, cb = ftg / 6, ft = ftg % 6;
        st_dev(ws + OFF_WG + ((((size_t)cb * 40 + 10 * kq + sub) * 6 + ft) * 64 + lane) * 16, o[i]);
    }
    if (tid < 128) {
        float s = 0.f;
#pragma unroll
        for (int i = 0; i < 8; ++i) s += *(LAS float*)(sm + 40960 + (i * 128 + tid) * 4);
        st_dev4(ws + OFF_CS + (size_t)(kq * 2 * F + (tid >> 6) * F + 64 * j + (tid & 63)) * 4, s);
    }
}

__device__ __forceinline__ void wdtile_load(int fb, const bf16_t* __restrict__ wd, u32x4 (&v)[5]) {
    const int tid = threadIdx.x;
#pragma unroll
    for (int i = 0; i < 5; ++i) { const int ch = tid + 512 * i, row = ch / 80, c = ch % 80; v[i] = *(const u32x4*)(wd + (size_t)(32 * fb + row) * D + c * 8); }
}
__device__ __forceinline__ void wdtile_unit(int fb, const u32x4 (&v)[5], char* __restrict__ ws, lptr sm) {
    const int tid = threadIdx.x, lane = tid & 63, w = uni(tid >> 6);
#pragma unroll
    for (int i = 0; i < 5; ++i) { const int ch = tid + 512 * i, row = ch / 80, c = ch % 80; *(LAS u32x4*)(sm + row * LN_ROW + c * 16) = v[i]; }
    bar();
    const int g = lane >> 4, q = (lane & 15) >> 2, p = lane & 3;
    u32x4 o[5];
#pragma unroll
    for (int i = 0; i < 5; ++i) o[i] = tr_read2(sm + (16 * (g >> 1) + 4 * (g & 1) + q) * LN_ROW + (16 * (w + 8 * i) + 4 * p) * 2, 8 * LN_ROW);
#pragma unroll
    for (int i = 0; i < 5; ++i) st_dev(ws + OFF_WD + (((size_t)fb * 40 + w + 8 * i) * 64 + lane) * 16, o[i]);
}

__device__ __forceinline__ f32x2 cmul(f32x2 a, f32x2 b) {
    const f32x2 t = (f32x2){a[0], a[0]} * b;
    const f32x2 bs = {-b[1], b[0]};
    return __builtin_elementwise_fma((f32x2){a[1], a[1]}, bs, t);
}
__device__ __forceinline__ unsigned cmul_h(unsigned a, unsigned b) {
    unsigned t, r;
    asm("v_pk_mul_f16 %0, %1, %2 op_sel:[0,0] op_sel_hi:[0,1]" : "=v"(t) : "v"(a), "v"(b));
    asm("v_pk_fma_f16 %0, %1, %2, %3 op_sel:[1,1,0] op_sel_hi:[1,0,1] neg_lo:[0,1,0]" : "=v"(r) : "v"(a), "v"(b), "v"(t));
    return r;
}
__device__ __forceinline__ f32x2 cexp_p(float arl, float air, float p) {
    const float mag = __builtin_amdgcn_exp2f(arl * p);
    float rev = air * p; rev -= rintf(rev);
    return (f32x2){mag * __builtin_amdgcn_cosf(rev), mag * __builtin_amdgcn_sinf(rev)};
}
struct KgenIn { unsigned ldt, la, ai, cr[3], ci[3]; };
__device__ __forceinline__ void kgen_load(KgenIn& k, int d, int third, int lane, const bf16_t* __restrict__ logA, const bf16_t* __restrict__ Aim,
                                          const bf16_t* __restrict__ Cre, const bf16_t* __restrict__ Cim, const bf16_t* __restrict__ logdt) {
    k.ldt = logdt[d]; k.la = logA[d * NM + 64 * third + lane]; k.ai = Aim[d * NM + 64 * third + lane];
#pragma unroll
    for (int i = 0; i < 3; ++i) { k.cr[i] = Cre[d * NM + 64 * i + lane]; k.ci[i] = Cim[d * NM + 64 * i + lane]; }
}
__device__ __forceinline__ void kgen_task(const KgenIn& k, int third, int lane, lptr tab, f32x4 (&acc)[3], float& unscale) {
    const float dt = rbf(expf(bf2f(k.ldt)));
    float mx = 0.f;
#pragma unroll
    for (int i = 0; i < 3; ++i) mx = fmaxf(mx, fmaxf(fabsf(bf2f(k.cr[i])), fabsf(bf2f(k.ci[i]))));
    mx = row16_max(mx);
    {
        const auto r1 = __builtin_amdgcn_permlane16_swap(__float_as_uint(mx), __float_as_uint(mx), false, false);
        mx = fmaxf(__uint_as_float(r1[0]), __uint_as_float(r1[1]));
        const auto r2 = __builtin_amdgcn_permlane32_swap(__float_as_uint(mx), __float_as_uint(mx), false, false);
        mx = fmaxf(__uint_as_float(r2[0]), __uint_as_float(r2[1]));
    }
    mx *= 2.f * dt;
    int e = (int)((__float_as_uint(mx) >> 23) & 0xff) - 126;
    e = e < -100 ? -100 : (e > 100 ? 100 : e);
    const float scale = __uint_as_float((unsigned)(127 - e) << 23);
    unscale = __uint_as_float((unsigned)(127 + e) << 23);
    const float are = -rbf(__builtin_amdgcn_exp2f(bf2f(k.la) * 1.4426950408889634f)) * dt, aim = bf2f(k.ai) * dt;
    const float crw = bf2f(third == 0 ? k.cr[0] : (third == 1 ? k.cr[1] : k.cr[2])) * dt, ciw = bf2f(third == 0 ? k.ci[0] : (third == 1 ? k.ci[1] : k.ci[2])) * dt;
    const float arl = are * 1.4426950408889634f, air = aim * 0.15915494309189535f;
    lptr base = tab + 4 * lane;
#define PUT(row, z) *(LAS unsigned*)(base + (row) * KG_ROW) = pk_f16(z)
#define PUTH(row, z) *(LAS unsigned*)(base + (row) * KG_ROW) = (z)
    const f32x2 w1 = cexp_p(arl, air, 1.f), w8 = cexp_p(arl, air, 8.f), H1 = cexp_p(arl, air, 128.f);
    f32x2 qv = {2.f * scale * crw, -2.f * scale * ciw};
    const f32x2 w8c = {w8[0], -w8[1]};
    f32x2 Ec = w1, Hc = H1;
    unsigned Eh[8], Hh[8];
    Eh[1] = pk_f16(w1); Hh[1] = pk_f16(H1);
    PUT(0, qv); PUTH(16, 0x00003c00u); PUTH(17, Eh[1]); PUTH(24, Hh[1]);
#pragma unroll
    for (int i = 1; i < 16; ++i) {
        qv = cmul(qv, w8c);
        Hc = cmul(Hc, H1);
        if (i < 7) Ec = cmul(Ec, w1);
        PUT(i, qv);
        if (i < 7) { Eh[i + 1] = pk_f16(Ec); PUTH(17 + i, Eh[i + 1]); }
        const int hi = i + 1;
        if (hi < 8) { Hh[hi] = pk_f16(Hc); PUTH(hi < 4 ? 32 + 4 * (hi - 2) : 40 + 2 * (hi - 4), Hh[hi]); } else PUT(48 + (hi - 8), Hc);
        if (i >= 7 && i < 14) PUTH(25 + (i - 7), cmul_h(Eh[i - 6], Hh[1]));
        if (i >= 7 && i < 10) PUTH(33 + (i - 7), cmul_h(Eh[2 * (i - 6)], Hh[2]));
        if (i >= 10 && i < 13) PUTH(37 + (i - 10), cmul_h(Eh[2 * (i - 9)], Hh[3]));
        if (i >= 11 && i < 15) PUTH(41 + 2 * (i - 11), cmul_h(Hh[4 + (i - 11)], Eh[4]));
    }
#undef PUTH
#undef PUT
    asm volatile("" ::: "memory");
    lptr rb = tab + (lane & 15) * KG_ROW + (lane >> 4) * 16;
#pragma unroll
    for (int mt = 0; mt < 3; ++mt) acc[mt] = (f32x4){};
#pragma unroll
    for (int ks = 0; ks < 4; ++ks) {
        const h16x8 bq = *(LAS h16x8*)(rb + ks * 64);
#pragma unroll
        for (int mt = 0; mt < 3; ++mt) {
            const h16x8 ar = *(LAS h16x8*)(rb + (16 + 16 * mt) * KG_ROW + ks * 64);
            acc[mt] = __builtin_amdgcn_mfma_f32_16x16x32_f16(ar, bq, acc[mt], 0, 0, 0);
        }
    }
#pragma unroll
    for (int mt = 0; mt < 3; ++mt) PIN(acc[mt]);
    asm volatile("s_waitcnt lgkmcnt(0)" ::: "memory");
}

__device__ __forceinline__ void g1_prefetch_w(char* __restrict__ ws, lptr sm, int cb, int lw, int lane) {
    const unsigned sm0 = (unsigned)(size_t)sm;
    const char* wsrc = ws + OFF_WG + (size_t)cb * 40 * 6144 + 3 * lw * 1024;
#pragma unroll
    for (int s = 0; s < 4; ++s)
#pragma unroll
        for (int i = 0; i < 3; ++i) dma16s(wsrc + (size_t)s * 12288 + i * 1024, lane * 16, sm0 + s * G1_SS + G1_W + (3 * lw + i) * 1024);
    if (lw < 3) {
        const int f0 = 96 * cb;
        const unsigned co = (unsigned)(((lw == 0 ? f0 + lane : (lw == 1 ? (lane < 32 ? f0 + 64 + lane : F + f0 + lane - 32) : F + f0 + 32 + lane))) * 4);
#pragma unroll
        for (int kq = 0; kq < 4; ++kq) dma4s(ws + OFF_CS + kq * 2 * F * 4, co, sm0 + G1_CS + kq * 768 + lw * 256);
    }
}

struct KTab { unsigned short v[2048]; };
constexpr int ci_of(int l) {
    if (l < 0) return 641;
    if (l < 256) return l;
    const int s = l < 512 ? 1 : (l < 1024 ? 2 : 3);
    return ((l + (1 << (s - 1)) - 1 + ((l >> s) & 1)) >> s) + 128 * s;
}
constexpr KTab make_tab() { KTab t{}; for (int y = 0; y < 2048; ++y) t.v[y] = (unsigned short)(2 * ci_of(2047 - y)); return t; }
__device__ const __attribute__((aligned(16))) KTab g_ktab = make_tab();

struct Params {
    const bf16_t *x, *logA, *Aim, *Cre, *Cim, *logdt, *Dskip, *wg, *wu, *wd;
    char* ws;
    bf16_t* out;
};

__device__ __forceinline__ unsigned conv_wg(const Params& P, int b, int cb, lptr sm PROF_ARG) {
    const int tid = threadIdx.x, lane = tid & 63, w = uni(tid >> 6);
    char* ws = P.ws;
    const int d0 = 4 * ((b & 7) * 20 + (b >> 3));
    const unsigned sm0 = (unsigned)(size_t)sm;
    unsigned* fl = (unsigned*)(ws + OFF_FL);
    KgenIn k1, k2;
    u32x4 tb[4];
    u32x2 xg[8];
    kgen_load(k1, d0 + w / 3, w % 3, lane, P.logA, P.Aim, P.Cre, P.Cim, P.logdt);
    const unsigned tag = epoch_tag(ws);
    if (w < 4) {
        const int t2 = 8 + w;
        kgen_load(k2, d0 + t2 / 3, t2 % 3, lane, P.logA, P.Aim, P.Cre, P.Cim, P.logdt);
#pragma unroll
        for (int i = 0; i < 4; ++i) tb[i] = *(const u32x4*)(&g_ktab.v[8 * (lane + 64 * i)]);
    }
    const unsigned dsk_raw = ((const unsigned*)P.Dskip)[(d0 + (w & 3)) >> 1];
    asm volatile("" : "+v"(k1.ldt), "+v"(k1.la), "+v"(k1.ai), "+v"(k1.cr[0]), "+v"(k1.cr[1]), "+v"(k1.cr[2]), "+v"(k1.ci[0]), "+v"(k1.ci[1]), "+v"(k1.ci[2]) :: "memory");
    STAMP(1);
    const lptr tab = sm + w * KG_WAVE;
    LAS unsigned* cnt = (LAS unsigned*)(sm + CV_M + 16);
    if (w == 7 && lane == 0) *cnt = 0;
    bar();
    if (w >= 4) {
#pragma unroll
        for (int i = 0; i < 8; ++i) xg[i] = *(const u32x2*)(P.x + (size_t)(8 * (tid - 256) + i) * D + d0);
        asm volatile("" ::: "memory");
    }
    f32x4 a1[3], a2[3];
    {
        int third = w % 3, chn = w / 3;
#pragma unroll 1
        for (int rnd = 0; rnd < 2; ++rnd) {
            float un;
            kgen_task(k1, third, lane, tab, a2, un);
            if (third == 0 && lane == 0) *(LAS float*)(sm + CV_M + chn * 4) = un;
            if (rnd == 1 || w >= 4) break;
#pragma unroll
            for (int mt = 0; mt < 3; ++mt) a1[mt] = a2[mt];
            k1 = k2; third = (8 + w) % 3; chn = (8 + w) / 3;
        }
    }
    if (w < 4) {
#pragma unroll
        for (int mt = 0; mt < 3; ++mt) { *(LAS f32x4*)(tab + (mt * 64 + lane) * 16) = a1[mt]; *(LAS f32x4*)(tab + 3072 + (mt * 64 + lane) * 16) = a2[mt]; }
        asm volatile("s_waitcnt lgkmcnt(0)" ::: "memory");
        if (lane == 0) __hip_atomic_fetch_add(cnt, 1u, __ATOMIC_RELAXED, __HIP_MEMORY_SCOPE_WORKGROUP);
        while (__hip_atomic_load(cnt, __ATOMIC_RELAXED, __HIP_MEMORY_SCOPE_WORKGROUP) < 8u) __builtin_amdgcn_s_sleep(0);
        asm volatile("" ::: "memory");
        STAMP(2);
        f32x4 ks[3];
        lptr pt[3];
#pragma unroll
        for (int j = 0; j < 3; ++j) { const int t = 3 * w + j; pt[j] = sm + (t & 7) * KG_WAVE + (t >> 3) * 3072 + lane * 16; }
        const float un = *(LAS float*)(sm + CV_M + w * 4);
#pragma unroll
        for (int mt = 0; mt < 3; ++mt) ks[mt] = ((*(LAS f32x4*)(pt[0] + mt * 1024) + *(LAS f32x4*)(pt[1] + mt * 1024)) + *(LAS f32x4*)(pt[2] + mt * 1024)) * un;
        lptr kc = sm + cv_kc(0) + w * KG_WAVE;
        const int hq = lane >> 4, col = lane & 15;
        const u32x2 p0 = {pk_bf16(ks[0][0], ks[0][1]), pk_bf16(ks[0][2], ks[0][3])}, p1 = {pk_bf16(ks[1][0], ks[1][1]), pk_bf16(ks[1][2], ks[1][3])};
        const u32x2 p2 = {pk_bf16(ks[2][0], ks[2][1]), pk_bf16(ks[2][2], ks[2][3])};
        *(LAS u32x2*)(kc + 2 * (128 * (hq >> 1) + 4 * (hq & 1) + 8 * col)) = p0;
        if (hq < 2) *(LAS u32x2*)(kc + 2 * (256 + 64 * hq + 4 * col)) = p1;
        else { *(LAS unsigned*)(kc + 2 * (384 + 64 * (hq - 2) + 2 * col)) = p1[0]; *(LAS unsigned*)(kc + 2 * (416 + 64 * (hq - 2) + 2 * col)) = p1[1]; }
        if (hq < 2) {
            lptr k2p = kc + 2 * (512 + 64 * hq + col);
            *(LAS unsigned short*)k2p = (unsigned short)p2[0]; *(LAS unsigned short*)(k2p + 32) = (unsigned short)(p2[0] >> 16);
            *(LAS unsigned short*)(k2p + 64) = (unsigned short)p2[1]; *(LAS unsigned short*)(k2p + 96) = (unsigned short)(p2[1] >> 16);
        } else if (lane == 32) *(LAS unsigned short*)(kc + 2 * 640) = (unsigned short)p2[0];
        else if (lane >= 48 && lane < 55) *(LAS unsigned short*)(kc + 2 * (641 + lane - 48)) = 0;
        asm volatile("" ::: "memory");
        STAMP(13);
        u32x4 o[4];
#pragma unroll
        for (int i = 0; i < 4; ++i)
#pragma unroll
            for (int k = 0; k < 4; ++k) {
                const unsigned tk = tb[i][k];
                const unsigned lo = *(LAS unsigned short*)(kc + (tk & 0xffffu)), hi = *(LAS unsigned short*)(kc + (tk >> 16));
                o[i][k] = lo | (hi << 16);
            }
        lptr dst = sm + cv_k(0) + w * KG_WAVE;
#pragma unroll
        for (int i = 0; i < 4; ++i) {
            *(LAS u32x4*)(dst + (lane + 64 * i) * 16) = o[i];
            unsigned nx = (unsigned)__builtin_amdgcn_update_dpp(0, (int)o[i][0], 0x130, 0xF, 0xF, false);
            const unsigned wrap = i < 3 ? (unsigned)__builtin_amdgcn_readlane((int)o[i < 3 ? i + 1 : 3][0], 0) : 0u;
            nx = lane == 63 ? wrap : nx;
            const u32x4 od = {__builtin_amdgcn_alignbit(o[i][1], o[i][0], 16), __builtin_amdgcn_alignbit(o[i][2], o[i][1], 16),
                              __builtin_amdgcn_alignbit(o[i][3], o[i][2], 16), __builtin_amdgcn_alignbit(nx, o[i][3], 16)};
            *(LAS u32x4*)(dst + 4160 + (lane + 64 * i) * 16) = od;
        }
        if (lane < 8) *(LAS u32x4*)(dst + (lane >> 2) * 4160 + 4096 + (lane & 3) * 16) = (u32x4){0u, 0u, 0u, 0u};
        STAMP(14);
    } else {
#pragma unroll
        for (int mt = 0; mt < 3; ++mt) *(LAS f32x4*)(tab + (mt * 64 + lane) * 16) = a2[mt];
        asm volatile("s_waitcnt lgkmcnt(0)" ::: "memory");
        if (lane == 0) __hip_atomic_fetch_add(cnt, 1u, __ATOMIC_RELAXED, __HIP_MEMORY_SCOPE_WORKGROUP);
        const int gm = tid - 256, ln = gm >> 1, sw = ((ln >> 1) & 3) << 1;
        u32x4 xc[4];
#pragma unroll
        for (int ch = 0; ch < 4; ++ch) {
#pragma unroll
            for (int k = 0; k < 4; ++k) xc[ch][k] = __builtin_amdgcn_perm(xg[2 * k + 1][ch >> 1], xg[2 * k][ch >> 1], (ch & 1) ? 0x07060302u : 0x05040100u);
            *(LAS u32x4*)(sm + CV_X + ln * 128 + 16 * ((2 * ch + (gm & 1)) ^ sw)) = xc[ch];
        }
        if (gm < 128) *(LAS u32x4*)(sm + CV_U + gm * 16) = (u32x4){0u, 0u, 0u, 0u};
        wait_flags<16, FL_LN_S>(fl + (b & 7) * FL_REP + FL_LN + 16 * (w - 4) * FL_LN_S, tag, lane);
        STAMPL(25);
        f32x4 r0, r1, n0, n1;
        asm volatile("global_load_dwordx4 %0, %4, %5 sc1\n\tglobal_load_dwordx4 %1, %4, %5 offset:16 sc1\n\tglobal_load_dwordx4 %2, %4, %6 sc1\n\tglobal_load_dwordx4 %3, %4, %6 offset:16 sc1\n\ts_waitcnt vmcnt(0)"
                     : "=&v"(r0), "=&v"(r1), "=&v"(n0), "=&v"(n1) : "v"(gm * 32), "s"(ws + OFF_S1), "s"(ws + OFF_S1 + T * 4) : "memory");
#pragma unroll
        for (int ch = 0; ch < 4; ++ch) {
            u32x4 u;
#pragma unroll
            for (int k = 0; k < 4; ++k) {
                const f32x2 rs = k < 2 ? (f32x2){r0[2 * k], r0[2 * k + 1]} : (f32x2){r1[2 * k - 4], r1[2 * k - 3]}, nm = k < 2 ? (f32x2){n0[2 * k], n0[2 * k + 1]} : (f32x2){n1[2 * k - 4], n1[2 * k - 3]};
                const f32x2 a = __builtin_elementwise_fma((f32x2){bflo(xc[ch][k]), bfhi(xc[ch][k])}, rs, nm);
                u[k] = pk_bf16(a[0], a[1]);
            }
            *(LAS u32x4*)(sm + CV_U + 2048 + ln * 128 + 16 * ((2 * ch + (gm & 1)) ^ sw)) = u;
        }
    }
    bar();
    STAMP(3); CSTAMP(16);
    if (w < 4) {
        const int n = lane & 15, h = lane >> 4;
        const lptr fb = sm + cv_k(0) + w * KG_WAVE + ((n & 1) ? 0 : 4160) + 4 * (((2031 - n) >> 1) + 4 * h) - 512 * 7;
        f32x4 acc[8] = {};
        bf16x8 Fa[2][8], Ga[2][8];
        lptr gp, fp[4];
        auto addr = [&](int rr) {
            const int ln = n + (h >> 1) + 15 - 2 * rr;
            gp = sm + CV_U + 128 * ln + 16 * ((2 * w + (h & 1)) ^ (((ln >> 1) & 3) << 1));
#pragma unroll
            for (int k = 0; k < 4; ++k) { fp[k] = fb - 64 * rr + 1024 * k; asm volatile("" : "+v"(fp[k])); }
        };
        auto ldG = [&](int Q, int c) { Ga[Q][c] = *(LAS bf16x8*)(gp + 2048 * c); };
        u32x4 ftmp[2];
        auto ldF = [&](int Q, int a, int half) {
            lptr q = fp[(7 - a) >> 1] + 512 * ((7 - a) & 1) + 8 * half;
            ftmp[a & 1][2 * half] = *(LAS unsigned*)q; ftmp[a & 1][2 * half + 1] = *(LAS unsigned*)(q + 4);
            if (half) Fa[Q][a] = __builtin_bit_cast(bf16x8, ftmp[a & 1]);
        };
        const int so = n * 128 + 16 * ((2 * w + (h >> 1)) ^ (((n >> 1) & 3) << 1)) + 8 * (h & 1);
        u32x2 uv[8], xv[8];
        addr(0);
#pragma unroll
        for (int c = 0; c < 8; ++c) { ldF(0, c, 0); ldF(0, c, 1); ldG(0, c); }
        SB();
#pragma unroll
        for (int rr = 0; rr < 8; ++rr) {
            const int Q = rr & 1;
            int i = 0;
#pragma unroll
            for (int a = 0; a < 8; ++a)
#pragma unroll
                for (int c = a; c < 8; ++c) {
                    acc[c] = __builtin_amdgcn_mfma_f32_16x16x32_bf16(Fa[Q][a], Ga[Q][c - a], acc[c], 0, 0, 0); PIN(acc[c]); SB();
                    if (rr < 7) {
                        if (i == 0) { addr(rr + 1); SB(); }
                        else if (i <= 24) { const int k = (i - 1) / 3, m = (i - 1) % 3; if (m == 0) ldG(Q ^ 1, k); else ldF(Q ^ 1, k, m - 1); SB(); }
                    } else if (i < 16) {
                        if (i & 1) xv[i >> 1] = *(LAS u32x2*)(sm + CV_X + so + 2048 * (i >> 1)); else uv[i >> 1] = *(LAS u32x2*)(sm + CV_U + 2048 + so + 2048 * (i >> 1));
                        SB();
                    }
                    ++i;
                }
        }
        STAMP(4); CSTAMP(17);
        const int d = d0 + w;
        lptr yb = sm + cv_k(0) + w * KG_WAVE;
        const float dsk = (d & 1) ? bfhi(dsk_raw) : bflo(dsk_raw);
        const f32x2 dk2 = {dsk, dsk};
#pragma unroll
        for (int c2 = 0; c2 < 8; c2 += 2) {
            f32x2 hf[4];
#pragma unroll
            for (int i = 0; i < 4; ++i) { const unsigned e = uv[c2 + (i >> 1)][i & 1]; hf[i] = __builtin_elementwise_fma((f32x2){bflo(e), bfhi(e)}, dk2, (f32x2){acc[c2 + (i >> 1)][2 * (i & 1)], acc[c2 + (i >> 1)][2 * (i & 1) + 1]}); }
#pragma unroll
            for (int i = 0; i < 4; ++i) { const unsigned e = xv[c2 + (i >> 1)][i & 1]; hf[i] += (f32x2){bflo(e), bfhi(e)}; }
#pragma unroll
            for (int j = 0; j < 2; ++j) *(LAS u32x2*)(yb + (256 * (c2 + j) + 16 * n + 4 * h) * 2) = (u32x2){pk_bf16(hf[2 * j][0], hf[2 * j][1]), pk_bf16(hf[2 * j + 1][0], hf[2 * j + 1][1])};
        }
        asm volatile("" ::: "memory");
#pragma unroll
        for (int j = 0; j < 4; ++j) st_dev_o(ws + OFF_HT + (size_t)d * (T * 2) + j * 1024, lane * 16, *(LAS u32x4*)(yb + (64 * j + lane) * 16));
        STAMP(27);
        wait_vm<0>();
    } else if (w == 6) wait_flags<96, FL_W_S>(fl + (b & 7) * FL_REP + FL_WG, tag, lane);
    bar();
    if (tid < 8) __hip_atomic_fetch_add(fl + FL_HC + (tid * 32 + (b & 31)) * 32, 1u, __ATOMIC_RELAXED, __HIP_MEMORY_SCOPE_AGENT);
    STAMP(5);
    return tag;
}

__device__ __forceinline__ unsigned spare_wg(const Params& P, int j, int cb, lptr sm PROF_ARG) {
    const int tid = threadIdx.x, lane = tid & 63, w = uni(tid >> 6);
    unsigned* fl = (unsigned*)(P.ws + OFF_FL);
    unsigned tag;
    auto publish = [&](int word) { wait_vm<0>(); bar(); if (tid < 8) set_flag_rep(fl + word, tag, tid); };
    if (j < 64) {
        u32x4 vx[5], vw[5];
        ln1_load(j, P.x, vx);
        tag = epoch_tag(P.ws);
        ln1_unit(j, vx, P.ws, tag PROF_FWD);
        if (j < 32) {
            wgtile_load(64 + j, P.wg, P.wu, vw);
            asm volatile("" ::: "memory");
            wait_vm<5>(); bar();
            if (tid < 8) set_flag_rep(fl + FL_LN + j * FL_LN_S, tag, tid);
            STAMP(1);
            wgtile_unit(64 + j, vw, P.ws, sm);
            publish(FL_WG + (64 + j) * FL_W_S);
        } else if (j < 48) {
            wdtile_load(j, P.wd, vw);
            asm volatile("" ::: "memory");
            wait_vm<5>(); bar();
            if (tid < 8) set_flag_rep(fl + FL_LN + j * FL_LN_S, tag, tid);
            STAMP(1);
            wdtile_unit(j, vw, P.ws, sm);
            publish(FL_WD + j * FL_W_S);
        } else {
            publish(FL_LN + j * FL_LN_S);
            STAMP(1);
        }
    } else {
        u32x4 va[5], vb[5], vc[5];
#define W_DELAY 100
        if (W_DELAY) { const unsigned long long t0 = wall_clock64(); while (wall_clock64() - t0 < W_DELAY) __builtin_amdgcn_s_sleep(2); }
        wgtile_load(2 * (j - 64), P.wg, P.wu, va);
        wgtile_load(2 * (j - 64) + 1, P.wg, P.wu, vb);
        wdtile_load(j - 64, P.wd, vc);
        tag = epoch_tag(P.ws);
        STAMP(1);
        wgtile_unit(2 * (j - 64), va, P.ws, sm);
        publish(FL_WG + 2 * (j - 64) * FL_W_S);
        wgtile_unit(2 * (j - 64) + 1, vb, P.ws, sm);
        publish(FL_WG + (2 * (j - 64) + 1) * FL_W_S);
        wdtile_unit(j - 64, vc, P.ws, sm);
        publish(FL_WD + (j - 64) * FL_W_S);
    }
    STAMP(2);
    if (w == 6) wait_flags<96, FL_W_S>(fl + (blockIdx.x & 7) * FL_REP + FL_WG, tag, lane);
    bar();
    STAMP(5);
    return tag;
}

template <int WC>
struct G1Compute {
    f32x16 acc[3][2];
    bf16x8 Af[2][3], Bf[2][2];
    template <int P> __device__ __forceinline__ void mm(int ft, int tt) {
        acc[ft][tt] = __builtin_amdgcn_mfma_f32_32x32x16_bf16(Af[P][ft], Bf[P][tt], acc[ft][tt], 0, 0, 0); PIN(acc[ft][tt]); SB();
    }
    __device__ __forceinline__ static bf16x8 rdB(lptr p) { return __builtin_bit_cast(bf16x8, tr_read2(p, 8 * 256)); }
    template <int P, int KK, bool MORE, bool BAR>
    __device__ __forceinline__ void substep(lptr pa, const lptr (&pb)[2]) {
        mm<P>(0, 0);
        if (BAR) { sbar(); SB(); }
        if (MORE) { Bf[P ^ 1][0] = rdB(pb[0] + KK * 16 * 256); Af[P ^ 1][0] = *(LAS bf16x8*)(pa + KK * 6144); SB(); }
        mm<P>(0, 1);
        if (MORE) { Bf[P ^ 1][1] = rdB(pb[1] + KK * 16 * 256); Af[P ^ 1][1] = *(LAS bf16x8*)(pa + KK * 6144 + 1024); SB(); }
        mm<P>(1, 0);
        if (MORE) { Af[P ^ 1][2] = *(LAS bf16x8*)(pa + KK * 6144 + 2048); SB(); }
        mm<P>(1, 1);
        mm<P>(2, 0);
        mm<P>(2, 1);
    }
};

template <int WC>
__device__ __forceinline__ void g1_compute(lptr sm, char* __restrict__ ws, int wr, int lane, int t0, int cb PROF_ARG) {
    const int g = lane >> 4, q = (lane & 15) >> 2, p = lane & 3, hh = g >> 1, krow = 4 * hh + q;
    G1Compute<WC> C;
#pragma unroll
    for (int ft = 0; ft < 3; ++ft)
#pragma unroll
        for (int tt = 0; tt < 2; ++tt) C.acc[ft][tt] = (f32x16){};
    const lptr a0 = sm + G1_W + 3 * WC * 1024 + lane * 16;
    lptr b0[2];
#pragma unroll
    for (int tt = 0; tt < 2; ++tt) b0[tt] = sm + krow * 256 + (((2 * wr + tt) ^ q) * 64) + (g & 1) * 32 + p * 8;
    sbar();
    STAMP(7); CSTAMP(18);
#pragma unroll
    for (int tt = 0; tt < 2; ++tt) C.Bf[0][tt] = C.rdB(b0[tt]);
#pragma unroll
    for (int ft = 0; ft < 3; ++ft) C.Af[0][ft] = *(LAS bf16x8*)(a0 + ft * 1024);
    SB();
    lptr pa = a0, pb[2] = {b0[0], b0[1]};
    int nb = 0;
#pragma unroll 1
    for (int s = 0; s < 19; ++s) {
        C.template substep<0, 1, true, false>(pa, pb);
        nb = nb + G1_SS == G1_NS * G1_SS ? 0 : nb + G1_SS;
        pa = a0 + nb; pb[0] = b0[0] + nb; pb[1] = b0[1] + nb;
        SB();
        C.template substep<1, 0, true, true>(pa, pb);
    }
    C.template substep<0, 1, true, false>(pa, pb);
    C.template substep<1, 0, false, false>(pa, pb);
    STAMP(8); CSTAMP(19);
    bar();
    char* mF = ws + OFF_MF;
    f32x2 nm2[2], k1[2], k2[2], s2[2];
#pragma unroll
    for (int tt = 0; tt < 2; ++tt) {
        const f32x2 sq = *(LAS f32x2*)(sm + G1_ST + (32 * (2 * wr + tt) + (lane & 31)) * 8);
        const float rs = __builtin_amdgcn_rcpf(sq[1]);
        nm2[tt] = (f32x2){-sq[0], -sq[0]};
        k1[tt] = (f32x2){-2.302208198f * rs, -2.302208198f * rs};
        k2[tt] = (f32x2){-0.1029432397f * rs * rs * rs, -0.1029432397f * rs * rs * rs};
        s2[tt] = (f32x2){sq[1] * sq[1], sq[1] * sq[1]};
    }
    const unsigned mo = (unsigned)((((t0 + 64 * wr + (lane & 31)) >> 4) * 48 * 64 + ((lane & 15) + 16 * hh)) * 16);
#pragma unroll
    for (int ft = 0; ft < 3; ++ft) {
        const int fl = 16 * (3 * WC + ft) + 4 * hh;
        lptr cp = sm + G1_CS + 3072 + fl * 4;
        const f32x4 cg0 = *(LAS f32x4*)cp, cg1 = *(LAS f32x4*)(cp + 32), cu0 = *(LAS f32x4*)(cp + 384), cu1 = *(LAS f32x4*)(cp + 416);
        const int ftg = 6 * cb + 3 * WC + ft;
        const char* mb = mF + (size_t)((ftg >> 1) * 64 + 32 * (ftg & 1)) * 16;
#pragma unroll
        for (int tt = 0; tt < 2; ++tt) {
            f32x2 G[4], U[4], E[4];
#pragma unroll
            for (int r = 0; r < 4; ++r) {
                const f32x2 cg = r < 2 ? (f32x2){cg0[2 * r], cg0[2 * r + 1]} : (f32x2){cg1[2 * r - 4], cg1[2 * r - 3]};
                G[r] = __builtin_elementwise_fma(nm2[tt], cg, (f32x2){C.acc[ft][tt][2 * r], C.acc[ft][tt][2 * r + 1]}); PIN(G[r]);
            }
            SB();
#pragma unroll
            for (int r = 0; r < 4; ++r) { E[r] = G[r] * G[r]; PIN(E[r]); }
            SB();
#pragma unroll
            for (int r = 0; r < 4; ++r) { E[r] = __builtin_elementwise_fma(E[r], k2[tt], k1[tt]); PIN(E[r]); }
            SB();
#pragma unroll
            for (int r = 0; r < 4; ++r) { E[r] = G[r] * E[r]; PIN(E[r]); }
            SB();
#pragma unroll
            for (int r = 0; r < 4; ++r) {
                const f32x2 cu = r < 2 ? (f32x2){cu0[2 * r], cu0[2 * r + 1]} : (f32x2){cu1[2 * r - 4], cu1[2 * r - 3]};
                E[r] = (f32x2){__builtin_amdgcn_exp2f(E[r][0]), __builtin_amdgcn_exp2f(E[r][1])}; PIN(E[r]); SB();
                U[r] = __builtin_elementwise_fma(nm2[tt], cu, (f32x2){C.acc[ft][tt][8 + 2 * r], C.acc[ft][tt][9 + 2 * r]}); PIN(U[r]); SB();
            }
#pragma unroll
            for (int r = 0; r < 4; ++r) { E[r] = __builtin_elementwise_fma(E[r], s2[tt], s2[tt]); PIN(E[r]); }
            SB();
#pragma unroll
            for (int r = 0; r < 4; ++r) {
                E[r] = (f32x2){__builtin_amdgcn_rcpf(E[r][0]), __builtin_amdgcn_rcpf(E[r][1])}; PIN(E[r]); SB();
                G[r] = G[r] * U[r]; PIN(G[r]); SB();
            }
#pragma unroll
            for (int r = 0; r < 4; ++r) { G[r] = G[r] * E[r]; PIN(G[r]); }
            SB();
            u32x4 o;
#pragma unroll
            for (int r = 0; r < 4; ++r) o[r] = pk_bf16(G[r][0], G[r][1]);
            st_dev_o(mb, mo + tt * (2 * 48 * 1024), o);
            SB();
        }
    }
}

__device__ __forceinline__ void g1_loader(char* __restrict__ ws, lptr sm, int lw, int lane, int t0, int cb, int t2, int db, unsigned tag) {
    const unsigned sm0 = (unsigned)(size_t)sm;
    {
        const char* hT = ws + OFF_HT;
        const char* wsrc = ws + OFF_WG + (size_t)cb * 40 * 6144 + 3 * lw * 1024;
        unsigned vh[2];
#pragma unroll
        for (int j = 0; j < 2; ++j) {
            const int o = (2 * lw + j) * 1024 + lane * 16, k = o >> 8, c = ((o >> 4) & 15) ^ ((k & 3) << 2);
            vh[j] = (unsigned)(k * (T * 2) + t0 * 2 + c * 16);
        }
        const unsigned vl = lane * 16;
        unsigned long long* st = (unsigned long long*)(ws + OFF_ST);
        auto stage = [&](int s) {
            const unsigned lb = sm0 + (s % G1_NS) * G1_SS;
            const char* hs = hT + (size_t)s * 32 * (T * 2);
            const char* wsx = wsrc + (size_t)s * 12288;
            dma16s(hs, vh[0], lb + (2 * lw) * 1024);
            if (s >= 4) dma16s(wsx, vl, lb + G1_W + (3 * lw) * 1024);
            dma16s(hs, vh[1], lb + (2 * lw + 1) * 1024);
            if (s >= 4) { dma16s(wsx + 1024, vl, lb + G1_W + (3 * lw + 1) * 1024); dma16s(wsx + 2048, vl, lb + G1_W + (3 * lw + 2) * 1024); }
        };
        u32x4 gv[3];
        unsigned long long g0, g1;
        float cs[8], sv[16];
#pragma unroll
        for (int s = 0; s < 4; ++s) stage(s);
        {
            const char* hp = hT + (size_t)(160 * lw) * (T * 2) + (t0 + 8 * cb) * 2;
            const unsigned go = (unsigned)lane * (T * 2);
            asm volatile("global_load_dwordx4 %0, %1, %2 sc1" : "=v"(gv[0]) : "v"(go), "s"(hp) : "memory");
            asm volatile("global_load_dwordx4 %0, %1, %2 sc1" : "=v"(gv[1]) : "v"(go), "s"(hp + (size_t)64 * (T * 2)) : "memory");
            asm volatile("global_load_dwordx4 %0, %1, %2 sc1" : "=v"(gv[2]) : "v"(lane < 32 ? go : 0u), "s"(hp + (size_t)128 * (T * 2)) : "memory");
        }
        wait_vm<9>();
        sbar();
#pragma unroll
        for (int s = 0; s < 19; ++s) {
            const int last = (s + 3) < 19 ? (s + 3) : 19;
            if (s == 3) asm volatile("s_waitcnt vmcnt(10)" : "+v"(gv[0]), "+v"(gv[1]), "+v"(gv[2]) :: "memory");
            else if (s == 17) asm volatile("s_waitcnt vmcnt(13)" : "+v"(g0), "+v"(g1) :: "memory");
            else if (s == 18) wait_vm<8>();
            else wait_vm_rt<30>(s == 0 ? 7 : (s == 1 ? 10 : (s == 2 ? 13 : 5 * (last - (s + 1)) + (s == 15 || s == 16 ? 2 : 0))));
            sbar();
            if (s + 4 < 20) stage(s + 4);
            if (s == 11 && lw < 3) {
                lptr cp = sm + G1_CS + lw * 256 + lane * 4;
                *(LAS float*)(cp + 3072) = (*(LAS float*)cp + *(LAS float*)(cp + 768)) + (*(LAS float*)(cp + 1536) + *(LAS float*)(cp + 2304));
                asm volatile("s_waitcnt lgkmcnt(0)" ::: "memory");
            }
            if (s == 16) {
#pragma unroll
                for (int j = 0; j < 3; ++j) {
                    const int i = lw + 4 * j < 10 ? lw + 4 * j : lw;
                    const int o = i * 1024 + lane * 16, row = o >> 7, c = ((o >> 4) & 7) ^ (((row >> 1) & 3) << 1);
                    dma16s(ws + OFF_HT, (unsigned)((80 * db + row) * (T * 2) + t2 * 2 + c * 16), sm0 + G2_H + i * 1024);
                }
            }
            if (s == 16 || s == 18) {
                const int s2 = s == 16 ? 0 : 1;
                const char* wd = ws + OFF_WD + (size_t)(4 * s2 + lw) * (40 * 1024) + (size_t)(5 * db) * 1024;
#pragma unroll
                for (int j = 0; j < 5; ++j) dma16s(wd + j * 1024, vl, sm0 + s2 * G2_SS + G2_W + (5 * lw + j) * 1024);
            }
            if (s == 14) {
                asm volatile("global_load_dwordx2 %0, %1, %2 sc1" : "=v"(g0) : "v"(lane * 8), "s"(st + t0) : "memory");
                asm volatile("global_load_dwordx2 %0, %1, %2 sc1" : "=v"(g1) : "v"(lane * 8), "s"(st + t0 + 64) : "memory");
            }
            if (s >= 3 && s <= 6) {
#pragma unroll
                for (int e = 2 * (s - 3); e < 2 * (s - 3) + 2; ++e) {
                    const unsigned c32 = (unsigned)__builtin_amdgcn_readlane((int)gv[0][e >> 1], 0);
                    cs[e] = (e & 1) ? bfhi(c32) : bflo(c32);
                    const float d0 = ((e & 1) ? bfhi(gv[0][e >> 1]) : bflo(gv[0][e >> 1])) - cs[e], d1 = ((e & 1) ? bfhi(gv[1][e >> 1]) : bflo(gv[1][e >> 1])) - cs[e];
                    const float d2 = lane < 32 ? ((e & 1) ? bfhi(gv[2][e >> 1]) : bflo(gv[2][e >> 1])) - cs[e] : 0.f;
                    sv[e] = d0 + d1 + d2; sv[8 + e] = d0 * d0 + d1 * d1 + d2 * d2;
                    PIN(sv[e]); PIN(sv[8 + e]);
                }
            }
            if (s == 7) {
#pragma unroll
                for (int j = 0; j < 8; ++j) {
                    const auto r = __builtin_amdgcn_permlane32_swap(__float_as_uint(sv[j]), __float_as_uint(sv[j + 8]), false, false);
                    sv[j] = __uint_as_float(r[0]) + __uint_as_float(r[1]); PIN(sv[j]);
                }
            }
            if (s == 8) {
#pragma unroll
                for (int k = 0; k < 4; ++k) {
                    const auto r = __builtin_amdgcn_permlane16_swap(__float_as_uint(sv[k]), __float_as_uint(sv[k + 4]), false, false);
                    sv[k] = __uint_as_float(r[0]) + __uint_as_float(r[1]);
                }
#pragma unroll
                for (int k = 0; k < 4; ++k) { sv[k] = row16_sum(sv[k]); PIN(sv[k]); }
            }
            if (s == 9) {
                if ((lane & 15) == 0) *(LAS f32x4*)(sm + G1_SP + lw * 96 + (lane >> 4) * 16) = (f32x4){sv[0], sv[1], sv[2], sv[3]};
                if (lane == 0) { *(LAS f32x4*)(sm + G1_SP + lw * 96 + 64) = (f32x4){cs[0], cs[1], cs[2], cs[3]}; *(LAS f32x4*)(sm + G1_SP + lw * 96 + 80) = (f32x4){cs[4], cs[5], cs[6], cs[7]}; }
                asm volatile("s_waitcnt lgkmcnt(0)" ::: "memory");
            }
            if (s == 10 && lw == 3 && lane < 8) {
                float pm[4], m2 = 0.f, mean = 0.f;
#pragma unroll
                for (int i = 0; i < 4; ++i) {
                    const float a = *(LAS float*)(sm + G1_SP + i * 96 + lane * 4) * (1.0f / 160.0f), q = *(LAS float*)(sm + G1_SP + i * 96 + 32 + lane * 4);
                    pm[i] = *(LAS float*)(sm + G1_SP + i * 96 + 64 + lane * 4) + a;
                    m2 += q - 160.0f * a * a; mean += 0.25f * pm[i];
                }
#pragma unroll
                for (int i = 0; i < 4; ++i) m2 += 160.0f * (pm[i] - mean) * (pm[i] - mean);
                const float var = fmaxf(m2 * (1.0f / (float)D), 0.f);
                const unsigned mb = pk_bf16(mean, rbf(sqrtf(rbf(rbf(var) + 1e-5f))));
                __hip_atomic_store(st + t0 + 8 * cb + lane, ((unsigned long long)tag << 32) | mb, __ATOMIC_RELAXED, __HIP_MEMORY_SCOPE_AGENT);
            }
        }
        if (lw == 0) {
            for (unsigned spins = 0; !__all((unsigned)(g0 >> 32) == tag && (unsigned)(g1 >> 32) == tag) && spins < (1u << 18); ++spins) {
                __builtin_amdgcn_s_sleep(2);
                g0 = __hip_atomic_load(st + t0 + lane, __ATOMIC_RELAXED, __HIP_MEMORY_SCOPE_AGENT);
                g1 = __hip_atomic_load(st + t0 + 64 + lane, __ATOMIC_RELAXED, __HIP_MEMORY_SCOPE_AGENT);
            }
            *(LAS f32x2*)(sm + G1_ST + lane * 8) = (f32x2){bflo((unsigned)g0), bfhi((unsigned)g0)};
            *(LAS f32x2*)(sm + G1_ST + (64 + lane) * 8) = (f32x2){bflo((unsigned)g1), bfhi((unsigned)g1)};
        }
        bar();
    }
}

template <int ND>
__device__ __forceinline__ void g2_compute(lptr sm, int lane, int th, int dl0, int t0, int d0, bf16_t* __restrict__ out PROF_ARG) {
    f32x4 acc[ND][2] = {};
    bf16x8 A[2][ND], B[2][2];
    const lptr pb0 = sm + lane * 16 + 2 * th * 4096, pa0 = sm + lane * 16 + G2_W + dl0 * 1024;
    auto ld = [&](int P, lptr pa, lptr pb, int fl) {
#pragma unroll
        for (int tt = 0; tt < 2; ++tt) B[P][tt] = *(LAS bf16x8*)(pb + (tt * 4 + fl) * 1024);
#pragma unroll
        for (int dt = 0; dt < ND; ++dt) A[P][dt] = *(LAS bf16x8*)(pa + (fl * 5 + dt) * 1024);
        SB();
    };
    auto mm = [&](int P) {
#pragma unroll
        for (int dt = 0; dt < ND; ++dt)
#pragma unroll
            for (int tt = 0; tt < 2; ++tt) { acc[dt][tt] = __builtin_amdgcn_mfma_f32_16x16x32_bf16(A[P][dt], B[P][tt], acc[dt][tt], 0, 0, 0); PIN(acc[dt][tt]); SB(); }
    };
    sbar();
    STAMP(11); CSTAMP(20);
    ld(0, pa0, pb0, 0);
    lptr pa = pa0, pb = pb0;
    int nb = 0;
#pragma unroll 1
    for (int s = 0; s < 12; ++s) {
        ld(1, pa, pb, 1); mm(0);
        ld(0, pa, pb, 2); mm(1);
        ld(1, pa, pb, 3); mm(0);
        if (s < 11) {
            nb = nb + G2_SS == G2_NS * G2_SS ? 0 : nb + G2_SS;
            pa = pa0 + nb; pb = pb0 + nb;
            bar();
            ld(0, pa, pb, 0);
        }
        mm(1);
    }
    STAMP(12); CSTAMP(21);
    bar();
    const int g = lane >> 4, q = (lane & 15) >> 2, p = lane & 3;
#pragma unroll
    for (int dt = 0; dt < ND; ++dt)
#pragma unroll
        for (int tt = 0; tt < 2; ++tt) {
            const int row = 16 * (dl0 + dt) + 4 * g + q, seg = (2 * th + tt) ^ ((row >> 1) & 3);
            const u32x2 hv = tr_read(sm + G2_H + row * 128 + seg * 32 + p * 8);
            const unsigned o01 = pk_bf16(acc[dt][tt][0], acc[dt][tt][1]), o23 = pk_bf16(acc[dt][tt][2], acc[dt][tt][3]);
            const u32x2 r = {pk_bf16(bflo(hv[0]) + bflo(o01), bfhi(hv[0]) + bfhi(o01)), pk_bf16(bflo(hv[1]) + bflo(o23), bfhi(hv[1]) + bfhi(o23))};
            *(LAS u32x2*)(sm + (16 * (2 * th + tt) + (lane & 15)) * 176 + (16 * (dl0 + dt) + 4 * g) * 2) = r;
        }
}

extern "C" __global__ void s4d_geglu_block_end();
extern "C" __global__ __launch_bounds__(512, 2) void s4d_geglu_block(Params P) {
    extern __shared__ __attribute__((aligned(16))) char smem[];
    lptr sm = (lptr)smem;
    STAMP(0);
    if (uni(threadIdx.x >> 6) == 7) {
        unsigned long long pc; unsigned off, len;
        asm volatile("s_getpc_b64 %0\n.Lpc_anchor_%=:\n\ts_mov_b32 %1, .Lpc_anchor_%=-s4d_geglu_block\n\ts_mov_b32 %2, s4d_geglu_block_end-s4d_geglu_block" : "=s"(pc), "=s"(off), "=s"(len));
        const unsigned o = (blockIdx.x >> 3) * 2048 + (threadIdx.x & 63) * 32;
        if (o < len) dma4((const void*)(pc - off), o, (unsigned)(size_t)sm + LDS_BYTES - 256);
    }
    const int tid = threadIdx.x, lane = tid & 63, w = uni(tid >> 6), b = blockIdx.x;
    const unsigned sm0 = (unsigned)(size_t)sm;
    char* ws = P.ws;
    unsigned* fl = (unsigned*)(ws + OFF_FL);
    unsigned tag;
    const int grp = b & 7, idx = b >> 3, grp0 = grp;
    const int rb = (grp & 1) * 8 + (idx & 7), cb = (grp >> 1) * 4 + (idx >> 3);
    const int tb = (grp >> 1) * 8 + (idx & 7), db = (grp & 1) * 4 + (idx >> 3);

    if (b < 160) tag = conv_wg(P, b, cb, sm PROF_FWD); else tag = spare_wg(P, b - 160, cb, sm PROF_FWD);

    if (w >= 4) g1_prefetch_w(ws, sm, cb, w & 3, lane);
    if (w == 4) wait_flags<32, 32>(fl + FL_HC + grp0 * 32 * 32, (tag << 3) + 5u, lane);
    if (w == 7) wait_flags<48, FL_W_S>(fl + grp0 * FL_REP + FL_WD, tag, lane);
    bar();
    STAMP(6);
    if (w >= 4) g1_loader(ws, sm, w & 3, lane, 128 * rb, cb, 64 * tb, db, tag);
    else if (w & 1) g1_compute<1>(sm, ws, w >> 1, lane, 128 * rb, cb PROF_FWD); else g1_compute<0>(sm, ws, w >> 1, lane, 128 * rb, cb PROF_FWD);

    const int t0 = 64 * tb, d0 = 80 * db;
    if (w >= 4) {
        const int lw = w & 3;
        const char* ms = ws + OFF_MF + (size_t)(4 * tb + lw) * 48 * 1024;
        const char* wsrc = ws + OFF_WD + (size_t)lw * (40 * 1024) + (size_t)(5 * db) * 1024;
        const unsigned vl = lane * 16;
        auto stage_w = [&](int s) {
#pragma unroll
            for (int j = 0; j < 5; ++j) dma16s(wsrc + (size_t)(4 * s) * (40 * 1024) + j * 1024, vl, sm0 + (s % G2_NS) * G2_SS + G2_W + (5 * lw + j) * 1024);
        };
        auto stage_m = [&](int s) {
#pragma unroll
            for (int j = 0; j < 4; ++j) dma16s(ms + (size_t)(4 * s + j) * 1024, vl, sm0 + (s % G2_NS) * G2_SS + (4 * lw + j) * 1024);
        };
        stage_w(2);
        sbar();
        if (lw == 0) wait_flags<16, FL_M_S>(fl + FL_M + 16 * (tb >> 1) * FL_M_S, tag, lane);
        sbar();
        STAMPL(10);
        stage_m(0); stage_m(1); stage_m(2);
        wait_vm<8>();
        sbar();
#pragma unroll
        for (int s = 0; s < 11; ++s) {
            if (s == 0) wait_vm<4>(); else if (s <= 9) wait_vm<9>(); else wait_vm<0>();
            sbar();
            if (s + 3 < 12) { stage_m(s + 3); stage_w(s + 3); }
        }
        sbar();
    } else {
        STAMP(28);
        wait_vm<0>();
        sbar();
        if (tid == 0) set_flag(fl + FL_M + (16 * rb + cb) * FL_M_S, tag);
        STAMP(9);
        sbar();
        const int th = w & 1;
        if (w >> 1) g2_compute<2>(sm, lane, th, 3, t0, d0, P.out PROF_FWD); else g2_compute<3>(sm, lane, th, 0, t0, d0, P.out PROF_FWD);
    }
    bar();
    for (int ch = tid; ch < 640; ch += 512) {
        const int row = ch / 10, c = ch % 10;
        *(u32x4*)(P.out + (size_t)(t0 + row) * D + d0 + c * 8) = *(LAS u32x4*)(sm + row * 176 + c * 16);
    }
    if (b == 0 && tid == 0) fl[FL_E] = tag;
    STAMP(15);
}

extern "C" __global__ void s4d_geglu_block_end() {}

extern "C" void kernel_launch(void* const* d_in, const int* in_sizes, int n_in, void* d_out, int out_size, void* d_ws, size_t ws_size, hipStream_t stream) {
    Params P;
    P.x = (const bf16_t*)d_in[0]; P.logA = (const bf16_t*)d_in[1]; P.Aim = (const bf16_t*)d_in[2]; P.Cre = (const bf16_t*)d_in[3]; P.Cim = (const bf16_t*)d_in[4];
    P.logdt = (const bf16_t*)d_in[5]; P.Dskip = (const bf16_t*)d_in[6]; P.wg = (const bf16_t*)d_in[8]; P.wu = (const bf16_t*)d_in[9]; P.wd = (const bf16_t*)d_in[10];
    P.ws = (char*)d_ws; P.out = (bf16_t*)d_out;
    (void)hipFuncSetAttribute((const void*)s4d_geglu_block, hipFuncAttributeMaxDynamicSharedMemorySize, LDS_BYTES);
    s4d_geglu_block<<<256, 512, LDS_BYTES, stream>>>(P);
}
```

```cpp
#include <hip/hip_runtime.h>
#include <stdint.h>

typedef unsigned short bf16_t;
typedef short s16x4 __attribute__((ext_vector_type(4)));
typedef __bf16 bf16x8 __attribute__((ext_vector_type(8)));
typedef __bf16 bf16x2 __attribute__((ext_vector_type(2)));
typedef _Float16 h16x8 __attribute__((ext_vector_type(8)));
typedef _Float16 h16x2 __attribute__((ext_vector_type(2)));
typedef float f32x2 __attribute__((ext_vector_type(2)));
typedef float f32x4 __attribute__((ext_vector_type(4)));
typedef float f32x16 __attribute__((ext_vector_type(16)));
typedef unsigned u32x2 __attribute__((ext_vector_type(2)));
typedef unsigned u32x4 __attribute__((ext_vector_type(4)));
#define LAS __attribute__((address_space(3)))
typedef LAS char* lptr;

constexpr int T = 2048, D = 640, NM = 192, F = 1536;
constexpr size_t OFF_S1 = 0;
constexpr size_t OFF_HT = OFF_S1 + (size_t)T * 8;
constexpr size_t OFF_CS = OFF_HT + (size_t)T * D * 2;
constexpr size_t OFF_MF = OFF_CS + 65536;
constexpr size_t OFF_WD = OFF_MF + (size_t)T * F * 2;
constexpr size_t OFF_WG = OFF_WD + (size_t)F * D * 2;
constexpr size_t OFF_ST = OFF_WG + (size_t)2 * F * D * 2;
constexpr size_t OFF_FL = OFF_ST + (size_t)T * 8;
constexpr size_t WS_BYTES = OFF_FL + 196608;
constexpr int FL_E = 0;
constexpr int FL_REP = 4096;
constexpr int FL_LN = FL_REP, FL_LN_S = 16;
constexpr int FL_WG = FL_LN + 1024, FL_WD = FL_WG + 96 * 8, FL_W_S = 8;
constexpr int FL_M = 64, FL_M_S = 8;
constexpr int FL_HC = 9 * FL_REP;
static_assert(FL_WD + 48 * FL_W_S <= 2 * FL_REP && FL_M + 256 * FL_M_S <= FL_REP, "flag map");

__device__ __forceinline__ float bf2f(unsigned v) { return __uint_as_float(v << 16); }
__device__ __forceinline__ float bflo(unsigned v) { return __uint_as_float(v << 16); }
__device__ __forceinline__ float bfhi(unsigned v) { return __uint_as_float(v & 0xffff0000u); }
__device__ __forceinline__ unsigned pk_bf16(float lo, float hi) { f32x2 v = {lo, hi}; return __builtin_bit_cast(unsigned, __builtin_convertvector(v, bf16x2)); }
__device__ __forceinline__ float rbf(float f) { return bflo(pk_bf16(f, f)); }
__device__ __forceinline__ unsigned pk_f16(f32x2 v) { return __builtin_bit_cast(unsigned, __builtin_convertvector(v, h16x2)); }
template <int CTRL> __device__ __forceinline__ float dppf(float x) { return __int_as_float(__builtin_amdgcn_update_dpp(0, __float_as_int(x), CTRL, 0xF, 0xF, true)); }
__device__ __forceinline__ float row16_sum(float x) { x += dppf<0xB1>(x); x += dppf<0x4E>(x); x += dppf<0x141>(x); x += dppf<0x140>(x); return x; }
__device__ __forceinline__ float row16_max(float x) { x = fmaxf(x, dppf<0xB1>(x)); x = fmaxf(x, dppf<0x4E>(x)); x = fmaxf(x, dppf<0x141>(x)); x = fmaxf(x, dppf<0x140>(x)); return x; }
__device__ __forceinline__ float xsum16_32(float x) {
    const unsigned u = __float_as_uint(x);
    const auto r = __builtin_amdgcn_permlane16_swap(u, u, false, false);
    const unsigned v = __float_as_uint(__uint_as_float(r[0]) + __uint_as_float(r[1]));
    const auto q = __builtin_amdgcn_permlane32_swap(v, v, false, false);
    return __uint_as_float(q[0]) + __uint_as_float(q[1]);
}
__device__ __forceinline__ u32x2 tr_read(lptr p) { return __builtin_bit_cast(u32x2, __builtin_amdgcn_ds_read_tr16_b64_v4i16((LAS s16x4*)p)); }
__device__ __forceinline__ u32x4 tr_read2(lptr p, int off2) { const u32x2 r1 = tr_read(p), r2 = tr_read(p + off2); return (u32x4){r1[0], r1[1], r2[0], r2[1]}; }
__device__ __forceinline__ int uni(int v) { return __builtin_amdgcn_readfirstlane(v); }

__device__ __forceinline__ void dma16(const void* sbase, unsigned voff, unsigned ldsaddr) {
    asm volatile("s_mov_b32 m0, %2\n\ts_nop 0\n\tglobal_load_lds_dwordx4 %0, %1" :: "v"(voff), "s"(sbase), "s"(ldsaddr) : "memory");
}
__device__ __forceinline__ void dma4(const void* sbase, unsigned voff, unsigned ldsaddr) {
    asm volatile("s_mov_b32 m0, %2\n\ts_nop 0\n\tglobal_load_lds_dword %0, %1" :: "v"(voff), "s"(sbase), "s"(ldsaddr) : "memory");
}
template <int N> __device__ __forceinline__ void wait_vm() { asm volatile("s_waitcnt vmcnt(%0)" :: "n"(N) : "memory"); }
template <int N> __device__ __forceinline__ void wait_vm_rt(int n) {
    if (n >= N) wait_vm<N>(); else if constexpr (N > 0) wait_vm_rt<N - 1>(n);
}
__device__ __forceinline__ void bar() { asm volatile("s_waitcnt lgkmcnt(0)\n\ts_barrier" ::: "memory"); }
__device__ __forceinline__ void sbar() { asm volatile("s_barrier" ::: "memory"); }
#define SB() __builtin_amdgcn_sched_barrier(0)
#define PIN(x) asm volatile("" : "+v"(x))
__device__ __forceinline__ void st_dev(void* p, u32x4 v) { asm volatile("global_store_dwordx4 %0, %1, off sc1\n\ts_nop 1" :: "v"(p), "v"(v) : "memory"); }
__device__ __forceinline__ void st_dev_o(const void* sbase, unsigned voff, u32x4 v) { asm volatile("global_store_dwordx4 %0, %1, %2 sc1\n\ts_nop 1" :: "v"(voff), "v"(v), "s"(sbase) : "memory"); }
__device__ __forceinline__ void st_dev4(void* p, float v) { asm volatile("global_store_dword %0, %1, off sc1\n\ts_nop 1" :: "v"(p), "v"(v) : "memory"); }
__device__ __forceinline__ void dma16s(const void* sbase, unsigned voff, unsigned ldsaddr) {
    asm volatile("s_mov_b32 m0, %2\n\ts_nop 0\n\tglobal_load_lds_dwordx4 %0, %1 sc1" :: "v"(voff), "s"(sbase), "s"(ldsaddr) : "memory");
}
__device__ __forceinline__ void dma4s(const void* sbase, unsigned voff, unsigned ldsaddr) {
    asm volatile("s_mov_b32 m0, %2\n\ts_nop 0\n\tglobal_load_lds_dword %0, %1 sc1" :: "v"(voff), "s"(sbase), "s"(ldsaddr) : "memory");
}
__device__ __forceinline__ void set_flag(unsigned* f, unsigned tag) { __hip_atomic_store(f, tag, __ATOMIC_RELAXED, __HIP_MEMORY_SCOPE_AGENT); }
__device__ __forceinline__ void set_flag_rep(unsigned* f, unsigned tag, int lane) { __hip_atomic_store(f + lane * FL_REP, tag, __ATOMIC_RELAXED, __HIP_MEMORY_SCOPE_AGENT); }
template <int N, int S>
__device__ __forceinline__ void wait_flags(unsigned* f, unsigned tag, int lane) {
    for (unsigned spins = 0; spins < (1u << 18); ++spins) {
        bool ok = true;
        if (lane < N) ok = __hip_atomic_load(f + lane * S, __ATOMIC_RELAXED, __HIP_MEMORY_SCOPE_AGENT) == tag;
        if (N > 64 && lane + 64 < N) ok &= __hip_atomic_load(f + (64 + lane) * S, __ATOMIC_RELAXED, __HIP_MEMORY_SCOPE_AGENT) == tag;
        if (N > 128 && lane + 128 < N) ok &= __hip_atomic_load(f + (128 + lane) * S, __ATOMIC_RELAXED, __HIP_MEMORY_SCOPE_AGENT) == tag;
        if (__all(ok)) break;
        __builtin_amdgcn_s_sleep(1);
    }
    asm volatile("" ::: "memory");
}

__device__ __forceinline__ unsigned epoch_tag(const char* ws) {
    unsigned e;
    asm volatile("s_load_dword %0, %1, 0x0\n\ts_waitcnt lgkmcnt(0)" : "=s"(e) : "s"(ws + OFF_FL + FL_E * 4) : "memory");
    return e + 1u;
}

constexpr int G1_NS = 7, G1_SS = 20480, G1_W = 8192;
constexpr int G2_NS = 4, G2_SS = 36 * 1024, G2_W = 16 * 1024;
constexpr int G2_H = G2_NS * G2_SS;
constexpr int G1_CS = G2_H + 10240;
constexpr int G1_ST = G1_CS + 3840;
constexpr int G1_SP = G1_ST + 1024;
constexpr int LDS_BYTES = 163840;
static_assert(G1_SP + 512 <= LDS_BYTES, "LDS map");
constexpr int KG_ROW = 272;
constexpr int KG_WAVE = 57 * KG_ROW;
constexpr int CV_KCH = 8320;
__host__ __device__ constexpr int cv_k(int c) { return (4 + c) * KG_WAVE + 3072; }
__host__ __device__ constexpr int cv_kc(int c) { return cv_k(c) + CV_KCH; }
static_assert(3072 + CV_KCH + 1296 <= KG_WAVE, "per-channel area");
constexpr int CV_X = 8 * KG_WAVE + 7 * KG_ROW + 16;
constexpr int CV_U = CV_X + 128 * 128;
constexpr int CV_M = CV_U + 144 * 128;
static_assert(CV_M + 32 + 256 <= 163840 && CV_X % 16 == 0, "LDS map");
constexpr int LN_ROW = 1344;

__device__ __forceinline__ void ln1_load(int j, const bf16_t* __restrict__ x, u32x4 (&v)[5]) {
    const int tid = threadIdx.x, t = 32 * j + (tid >> 4);
#pragma unroll
    for (int i = 0; i < 5; ++i) v[i] = *(const u32x4*)(x + (size_t)t * D + ((tid & 15) + 16 * i) * 8);
}
__device__ __forceinline__ void ln1_unit(int j, const u32x4 (&v)[5], char* __restrict__ ws, unsigned tag) {
    const int tid = threadIdx.x, lane = tid & 63, w = uni(tid >> 6);
    f32x2 f[20];
    f32x2 s2 = {0.f, 0.f};
#pragma unroll
    for (int i = 0; i < 5; ++i)
#pragma unroll
        for (int e = 0; e < 4; ++e) { f[4 * i + e] = (f32x2){bflo(v[i][e]), bfhi(v[i][e])}; s2 += f[4 * i + e]; }
    const float mean = row16_sum(s2[0] + s2[1]) * (1.0f / (float)D);
    f32x2 q2 = {0.f, 0.f};
#pragma unroll
    for (int i = 0; i < 20; ++i) { const f32x2 dv = f[i] - mean; q2 += dv * dv; }
    const float var = row16_sum(q2[0] + q2[1]) * (1.0f / (float)D);
    const float rs = __builtin_amdgcn_rcpf(rbf(sqrtf(rbf(rbf(var) + 1e-5f)))), nm = -rbf(mean) * rs;
    u32x4 o;
#pragma unroll
    for (int i = 0; i < 4; ++i) {
        const unsigned a = (unsigned)__builtin_amdgcn_readlane((int)__float_as_uint(rs), 16 * i), c = (unsigned)__builtin_amdgcn_readlane((int)__float_as_uint(nm), 16 * i);
        o[i] = lane == 0 ? a : c;
    }
    if (lane < 2) st_dev(ws + OFF_S1 + (size_t)lane * (T * 4) + (32 * j + 4 * w) * 4, o);
    if (tid < 4) set_flag((unsigned*)(ws + OFF_FL) + FL_HC + (4 * j + tid) * 32, tag << 3);
}

__device__ __forceinline__ void wgtile_load(int b, const bf16_t* __restrict__ wg, const bf16_t* __restrict__ wu, u32x4 (&v)[5]) {
    const int tid = threadIdx.x, j = b >> 2, kq = b & 3, c16 = tid & 15, r0 = tid >> 4;
    const bf16_t* src = (c16 < 8 ? wg : wu) + (size_t)(160 * kq + r0) * F + 64 * j + (c16 & 7) * 8;
#pragma unroll
    for (int i = 0; i < 5; ++i) v[i] = *(const u32x4*)(src + (size_t)(32 * i) * F);
}
__device__ __forceinline__ void wgtile_unit(int b, const u32x4 (&v)[5], char* __restrict__ ws, lptr sm) {
    const int tid = threadIdx.x, lane = tid & 63, w = uni(tid >> 6);
    const int j = b >> 2, kq = b & 3;
    const int c16 = tid & 15, r0 = tid >> 4;
    f32x2 a[4] = {};
#pragma unroll
    for (int i = 0; i < 5; ++i) {
        const int k = 32 * i + r0;
        *(LAS u32x4*)(sm + k * 256 + (((c16 >> 1) ^ (k & 3)) * 32) + (c16 & 1) * 16) = v[i];
#pragma unroll
        for (int e = 0; e < 4; ++e) a[e] += (f32x2){bflo(v[i][e]), bfhi(v[i][e])};
    }
#pragma unroll
    for (int e = 0; e < 4; ++e)
#pragma unroll
        for (int hf = 0; hf < 2; ++hf) {
            const float s = xsum16_32(a[e][hf]);
            if (lane < 16) *(LAS float*)(sm + 40960 + (w * 128 + c16 * 8 + 2 * e + hf) * 4) = s;
        }
    bar();
    const int g = lane >> 4, q = (lane & 15) >> 2, p = lane & 3;
    u32x4 o[5];
#pragma unroll
    for (int i = 0; i < 5; ++i) {
        const int item = w + 8 * i, ftl = item & 3, sub = item >> 2;
        const int k = 16 * sub + 4 * (g >> 1) + q;
        o[i] = tr_read2(sm + k * 256 + ((((g & 1) * 4 + ftl) ^ q) * 32) + p * 8, 8 * 256);
    }
#pragma unroll
    for (int i = 0; i < 5; ++i) {
        const int item = w + 8 * i, ftl = item & 3, sub = item >> 2;
        const int ftg = 4 * j + ftl, cb = ftg / 6, ft = ftg % 6;
        st_dev(ws + OFF_WG + ((((size_t)cb * 40 + 10 * kq + sub) * 6 + ft) * 64 + lane) * 16, o[i]);
    }
    if (tid < 128) {
        float s = 0.f;
#pragma unroll
        for (int i = 0; i < 8; ++i) s += *(LAS float*)(sm + 40960 + (i * 128 + tid) * 4);
        st_dev4(ws + OFF_CS + (size_t)(kq * 2 * F + (tid >> 6) * F + 64 * j + (tid & 63)) * 4, s);
    }
}

__device__ __forceinline__ void wdtile_load(int fb, const bf16_t* __restrict__ wd, u32x4 (&v)[5]) {
    const int tid = threadIdx.x;
#pragma unroll
    for (int i = 0; i < 5; ++i) { const int ch = tid + 512 * i, row = ch / 80, c = ch % 80; v[i] = *(const u32x4*)(wd + (size_t)(32 * fb + row) * D + c * 8); }
}
__device__ __forceinline__ void wdtile_unit(int fb, const u32x4 (&v)[5], char* __restrict__ ws, lptr sm) {
    const int tid = threadIdx.x, lane = tid & 63, w = uni(tid >> 6);
#pragma unroll
    for (int i = 0; i < 5; ++i) { const int ch = tid + 512 * i, row = ch / 80, c = ch % 80; *(LAS u32x4*)(sm + row * LN_ROW + c * 16) = v[i]; }
    bar();
    const int g = lane >> 4, q = (lane & 15) >> 2, p = lane & 3;
    u32x4 o[5];
#pragma unroll
    for (int i = 0; i < 5; ++i) o[i] = tr_read2(sm + (16 * (g >> 1) + 4 * (g & 1) + q) * LN_ROW + (16 * (w + 8 * i) + 4 * p) * 2, 8 * LN_ROW);
#pragma unroll
    for (int i = 0; i < 5; ++i) st_dev(ws + OFF_WD + (((size_t)fb * 40 + w + 8 * i) * 64 + lane) * 16, o[i]);
}

__device__ __forceinline__ f32x2 cmul(f32x2 a, f32x2 b) {
    const f32x2 t = (f32x2){a[0], a[0]} * b;
    const f32x2 bs = {-b[1], b[0]};
    return __builtin_elementwise_fma((f32x2){a[1], a[1]}, bs, t);
}
__device__ __forceinline__ unsigned cmul_h(unsigned a, unsigned b) {
    unsigned t, r;
    asm("v_pk_mul_f16 %0, %1, %2 op_sel:[0,0] op_sel_hi:[0,1]" : "=v"(t) : "v"(a), "v"(b));
    asm("v_pk_fma_f16 %0, %1, %2, %3 op_sel:[1,1,0] op_sel_hi:[1,0,1] neg_lo:[0,1,0]" : "=v"(r) : "v"(a), "v"(b), "v"(t));
    return r;
}
__device__ __forceinline__ f32x2 cexp_p(float arl, float air, float p) {
    const float mag = __builtin_amdgcn_exp2f(arl * p);
    float rev = air * p; rev -= rintf(rev);
    return (f32x2){mag * __builtin_amdgcn_cosf(rev), mag * __builtin_amdgcn_sinf(rev)};
}
struct KgenIn { unsigned ldt, la, ai, cr[3], ci[3]; };
__device__ __forceinline__ void kgen_load(KgenIn& k, int d, int third, int lane, const bf16_t* __restrict__ logA, const bf16_t* __restrict__ Aim,
                                          const bf16_t* __restrict__ Cre, const bf16_t* __restrict__ Cim, const bf16_t* __restrict__ logdt) {
    k.ldt = logdt[d]; k.la = logA[d * NM + 64 * third + lane]; k.ai = Aim[d * NM + 64 * third + lane];
#pragma unroll
    for (int i = 0; i < 3; ++i) { k.cr[i] = Cre[d * NM + 64 * i + lane]; k.ci[i] = Cim[d * NM + 64 * i + lane]; }
}
__device__ __forceinline__ void kgen_task(const KgenIn& k, int third, int lane, lptr tab, f32x4 (&acc)[3], float& unscale) {
    const float dt = rbf(expf(bf2f(k.ldt)));
    float mx = 0.f;
#pragma unroll
    for (int i = 0; i < 3; ++i) mx = fmaxf(mx, fmaxf(fabsf(bf2f(k.cr[i])), fabsf(bf2f(k.ci[i]))));
    mx = row16_max(mx);
    {
        const auto r1 = __builtin_amdgcn_permlane16_swap(__float_as_uint(mx), __float_as_uint(mx), false, false);
        mx = fmaxf(__uint_as_float(r1[0]), __uint_as_float(r1[1]));
        const auto r2 = __builtin_amdgcn_permlane32_swap(__float_as_uint(mx), __float_as_uint(mx), false, false);
        mx = fmaxf(__uint_as_float(r2[0]), __uint_as_float(r2[1]));
    }
    mx *= 2.f * dt;
    int e = (int)((__float_as_uint(mx) >> 23) & 0xff) - 126;
    e = e < -126 ? -126 : (e > 126 ? 126 : e);
    const float scale = __uint_as_float((unsigned)(127 - e) << 23);
    unscale = __uint_as_float((unsigned)(127 + e) << 23);
    const float are = -rbf(__builtin_amdgcn_exp2f(bf2f(k.la) * 1.4426950408889634f)) * dt, aim = bf2f(k.ai) * dt;
    const float crw = bf2f(third == 0 ? k.cr[0] : (third == 1 ? k.cr[1] : k.cr[2])) * dt, ciw = bf2f(third == 0 ? k.ci[0] : (third == 1 ? k.ci[1] : k.ci[2])) * dt;
    const float arl = are * 1.4426950408889634f, air = aim * 0.15915494309189535f;
    lptr base = tab + 4 * lane;
#define PUT(row, z) *(LAS unsigned*)(base + (row) * KG_ROW) = pk_f16(z)
#define PUTH(row, z) *(LAS unsigned*)(base + (row) * KG_ROW) = (z)
    const f32x2 w1 = cexp_p(arl, air, 1.f), w8 = cexp_p(arl, air, 8.f), H1 = cexp_p(arl, air, 128.f);
    f32x2 qv = {2.f * scale * crw, -2.f * scale * ciw};
    const f32x2 w8c = {w8[0], -w8[1]};
    f32x2 Ec = w1, Hc = H1;
    unsigned Eh[8], Hh[8];
    Eh[1] = pk_f16(w1); Hh[1] = pk_f16(H1);
    PUT(0, qv); PUTH(16, 0x00003c00u); PUTH(17, Eh[1]); PUTH(24, Hh[1]);
#pragma unroll
    for (int i = 1; i < 16; ++i) {
        qv = cmul(qv, w8c);
        Hc = cmul(Hc, H1);
        if (i < 7) Ec = cmul(Ec, w1);
        PUT(i, qv);
        if (i < 7) { Eh[i + 1] = pk_f16(Ec); PUTH(17 + i, Eh[i + 1]); }
        const int hi = i + 1;
        if (hi < 8) { Hh[hi] = pk_f16(Hc); PUTH(hi < 4 ? 32 + 4 * (hi - 2) : 40 + 2 * (hi - 4), Hh[hi]); } else PUT(48 + (hi - 8), Hc);
        if (i >= 7 && i < 14) PUTH(25 + (i - 7), cmul_h(Eh[i - 6], Hh[1]));
        if (i >= 7 && i < 10) PUTH(33 + (i - 7), cmul_h(Eh[2 * (i - 6)], Hh[2]));
        if (i >= 10 && i < 13) PUTH(37 + (i - 10), cmul_h(Eh[2 * (i - 9)], Hh[3]));
        if (i >= 11 && i < 15) PUTH(41 + 2 * (i - 11), cmul_h(Hh[4 + (i - 11)], Eh[4]));
    }
#undef PUTH
#undef PUT
    asm volatile("" ::: "memory");
    lptr rb = tab + (lane & 15) * KG_ROW + (lane >> 4) * 16;
#pragma unroll
    for (int mt = 0; mt < 3; ++mt) acc[mt] = (f32x4){};
#pragma unroll
    for (int ks = 0; ks < 4; ++ks) {
        const h16x8 bq = *(LAS h16x8*)(rb + ks * 64);
#pragma unroll
        for (int mt = 0; mt < 3; ++mt) {
            const h16x8 ar = *(LAS h16x8*)(rb + (16 + 16 * mt) * KG_ROW + ks * 64);
            acc[mt] = __builtin_amdgcn_mfma_f32_16x16x32_f16(ar, bq, acc[mt], 0, 0, 0);
        }
    }
#pragma unroll
    for (int mt = 0; mt < 3; ++mt) PIN(acc[mt]);
    asm volatile("s_waitcnt lgkmcnt(0)" ::: "memory");
}

__device__ __forceinline__ void g1_prefetch_w(char* __restrict__ ws, lptr sm, int cb, int lw, int lane) {
    const unsigned sm0 = (unsigned)(size_t)sm;
    const char* wsrc = ws + OFF_WG + (size_t)cb * 40 * 6144 + 3 * lw * 1024;
#pragma unroll
    for (int s = 0; s < 4; ++s)
#pragma unroll
        for (int i = 0; i < 3; ++i) dma16s(wsrc + (size_t)s * 12288 + i * 1024, lane * 16, sm0 + s * G1_SS + G1_W + (3 * lw + i) * 1024);
    if (lw < 3) {
        const int f0 = 96 * cb;
        const unsigned co = (unsigned)(((lw == 0 ? f0 + lane : (lw == 1 ? (lane < 32 ? f0 + 64 + lane : F + f0 + lane - 32) : F + f0 + 32 + lane))) * 4);
#pragma unroll
        for (int kq = 0; kq < 4; ++kq) dma4s(ws + OFF_CS + kq * 2 * F * 4, co, sm0 + G1_CS + kq * 768 + lw * 256);
    }
}

struct KTab { unsigned short v[2048]; };
constexpr int ci_of(int l) {
    if (l < 0) return 641;
    if (l < 256) return l;
    const int s = l < 512 ? 1 : (l < 1024 ? 2 : 3);
    return ((l + (1 << (s - 1)) - 1 + ((l >> s) & 1)) >> s) + 128 * s;
}
constexpr KTab make_tab() { KTab t{}; for (int y = 0; y < 2048; ++y) t.v[y] = (unsigned short)(2 * ci_of(2047 - y)); return t; }
__device__ const __attribute__((aligned(16))) KTab g_ktab = make_tab();

struct Params {
    const bf16_t *x, *logA, *Aim, *Cre, *Cim, *logdt, *Dskip, *wg, *wu, *wd;
    char* ws;
    bf16_t* out;
};

__device__ __forceinline__ unsigned conv_wg(const Params& P, int b, int cb, lptr sm) {
    const int tid = threadIdx.x, lane = tid & 63, w = uni(tid >> 6);
    char* ws = P.ws;
    const int d0 = 4 * ((b & 7) * 20 + (b >> 3));
    const unsigned sm0 = (unsigned)(size_t)sm;
    unsigned* fl = (unsigned*)(ws + OFF_FL);
    KgenIn k1, k2;
    u32x4 tb[4];
    u32x2 xg[8];
    kgen_load(k1, d0 + w / 3, w % 3, lane, P.logA, P.Aim, P.Cre, P.Cim, P.logdt);
    const unsigned tag = epoch_tag(ws);
    if (w < 4) {
        const int t2 = 8 + w;
        kgen_load(k2, d0 + t2 / 3, t2 % 3, lane, P.logA, P.Aim, P.Cre, P.Cim, P.logdt);
#pragma unroll
        for (int i = 0; i < 4; ++i) tb[i] = *(const u32x4*)(&g_ktab.v[8 * (lane + 64 * i)]);
    }
    const unsigned dsk_raw = ((const unsigned*)P.Dskip)[(d0 + (w & 3)) >> 1];
    asm volatile("" : "+v"(k1.ldt), "+v"(k1.la), "+v"(k1.ai), "+v"(k1.cr[0]), "+v"(k1.cr[1]), "+v"(k1.cr[2]), "+v"(k1.ci[0]), "+v"(k1.ci[1]), "+v"(k1.ci[2]) :: "memory");

    const lptr tab = sm + w * KG_WAVE;
    LAS unsigned* cnt = (LAS unsigned*)(sm + CV_M + 16);
    if (w == 7 && lane == 0) *cnt = 0;
    bar();
    if (w >= 4) {
#pragma unroll
        for (int i = 0; i < 8; ++i) xg[i] = *(const u32x2*)(P.x + (size_t)(8 * (tid - 256) + i) * D + d0);
        asm volatile("" ::: "memory");
    }
    f32x4 a1[3], a2[3];
    {
        int third = w % 3, chn = w / 3;
#pragma unroll 1
        for (int rnd = 0; rnd < 2; ++rnd) {
            float un;
            kgen_task(k1, third, lane, tab, a2, un);
            if (third == 0 && lane == 0) *(LAS float*)(sm + CV_M + chn * 4) = un;
            if (rnd == 1 || w >= 4) break;
#pragma unroll
            for (int mt = 0; mt < 3; ++mt) a1[mt] = a2[mt];
            k1 = k2; third = (8 + w) % 3; chn = (8 + w) / 3;
        }
    }
    if (w < 4) {
#pragma unroll
        for (int mt = 0; mt < 3; ++mt) { *(LAS f32x4*)(tab + (mt * 64 + lane) * 16) = a1[mt]; *(LAS f32x4*)(tab + 3072 + (mt * 64 + lane) * 16) = a2[mt]; }
        asm volatile("s_waitcnt lgkmcnt(0)" ::: "memory");
        if (lane == 0) __hip_atomic_fetch_add(cnt, 1u, __ATOMIC_RELAXED, __HIP_MEMORY_SCOPE_WORKGROUP);
        while (__hip_atomic_load(cnt, __ATOMIC_RELAXED, __HIP_MEMORY_SCOPE_WORKGROUP) < 8u) __builtin_amdgcn_s_sleep(0);
        asm volatile("" ::: "memory");

        f32x4 ks[3];
        lptr pt[3];
#pragma unroll
        for (int j = 0; j < 3; ++j) { const int t = 3 * w + j; pt[j] = sm + (t & 7) * KG_WAVE + (t >> 3) * 3072 + lane * 16; }
        const float un = *(LAS float*)(sm + CV_M + w * 4);
#pragma unroll
        for (int mt = 0; mt < 3; ++mt) ks[mt] = ((*(LAS f32x4*)(pt[0] + mt * 1024) + *(LAS f32x4*)(pt[1] + mt * 1024)) + *(LAS f32x4*)(pt[2] + mt * 1024)) * un;
        lptr kc = sm + cv_kc(0) + w * KG_WAVE;
        const int hq = lane >> 4, col = lane & 15;
        const u32x2 p0 = {pk_bf16(ks[0][0], ks[0][1]), pk_bf16(ks[0][2], ks[0][3])}, p1 = {pk_bf16(ks[1][0], ks[1][1]), pk_bf16(ks[1][2], ks[1][3])};
        const u32x2 p2 = {pk_bf16(ks[2][0], ks[2][1]), pk_bf16(ks[2][2], ks[2][3])};
        *(LAS u32x2*)(kc + 2 * (128 * (hq >> 1) + 4 * (hq & 1) + 8 * col)) = p0;
        if (hq < 2) *(LAS u32x2*)(kc + 2 * (256 + 64 * hq + 4 * col)) = p1;
        else { *(LAS unsigned*)(kc + 2 * (384 + 64 * (hq - 2) + 2 * col)) = p1[0]; *(LAS unsigned*)(kc + 2 * (416 + 64 * (hq - 2) + 2 * col)) = p1[1]; }
        if (hq < 2) {
            lptr k2p = kc + 2 * (512 + 64 * hq + col);
            *(LAS unsigned short*)k2p = (unsigned short)p2[0]; *(LAS unsigned short*)(k2p + 32) = (unsigned short)(p2[0] >> 16);
            *(LAS unsigned short*)(k2p + 64) = (unsigned short)p2[1]; *(LAS unsigned short*)(k2p + 96) = (unsigned short)(p2[1] >> 16);
        } else if (lane == 32) *(LAS unsigned short*)(kc + 2 * 640) = (unsigned short)p2[0];
        else if (lane >= 48 && lane < 55) *(LAS unsigned short*)(kc + 2 * (641 + lane - 48)) = 0;
        asm volatile("" ::: "memory");

        u32x4 o[4];
#pragma unroll
        for (int i = 0; i < 4; ++i)
#pragma unroll
            for (int k = 0; k < 4; ++k) {
                const unsigned tk = tb[i][k];
                const unsigned lo = *(LAS unsigned short*)(kc + (tk & 0xffffu)), hi = *(LAS unsigned short*)(kc + (tk >> 16));
                o[i][k] = lo | (hi << 16);
            }
        lptr dst = sm + cv_k(0) + w * KG_WAVE;
#pragma unroll
        for (int i = 0; i < 4; ++i) {
            *(LAS u32x4*)(dst + (lane + 64 * i) * 16) = o[i];
            unsigned nx = (unsigned)__builtin_amdgcn_update_dpp(0, (int)o[i][0], 0x130, 0xF, 0xF, false);
            const unsigned wrap = i < 3 ? (unsigned)__builtin_amdgcn_readlane((int)o[i < 3 ? i + 1 : 3][0], 0) : 0u;
            nx = lane == 63 ? wrap : nx;
            const u32x4 od = {__builtin_amdgcn_alignbit(o[i][1], o[i][0], 16), __builtin_amdgcn_alignbit(o[i][2], o[i][1], 16),
                              __builtin_amdgcn_alignbit(o[i][3], o[i][2], 16), __builtin_amdgcn_alignbit(nx, o[i][3], 16)};
            *(LAS u32x4*)(dst + 4160 + (lane + 64 * i) * 16) = od;
        }
        if (lane < 8) *(LAS u32x4*)(dst + (lane >> 2) * 4160 + 4096 + (lane & 3) * 16) = (u32x4){0u, 0u, 0u, 0u};
    } else {
#pragma unroll
        for (int mt = 0; mt < 3; ++mt) *(LAS f32x4*)(tab + (mt * 64 + lane) * 16) = a2[mt];
        asm volatile("s_waitcnt lgkmcnt(0)" ::: "memory");
        if (lane == 0) __hip_atomic_fetch_add(cnt, 1u, __ATOMIC_RELAXED, __HIP_MEMORY_SCOPE_WORKGROUP);
        const int gm = tid - 256, ln = gm >> 1, sw = ((ln >> 1) & 3) << 1;
        u32x4 xc[4];
#pragma unroll
        for (int ch = 0; ch < 4; ++ch) {
#pragma unroll
            for (int k = 0; k < 4; ++k) xc[ch][k] = __builtin_amdgcn_perm(xg[2 * k + 1][ch >> 1], xg[2 * k][ch >> 1], (ch & 1) ? 0x07060302u : 0x05040100u);
            *(LAS u32x4*)(sm + CV_X + ln * 128 + 16 * ((2 * ch + (gm & 1)) ^ sw)) = xc[ch];
        }
        if (gm < 128) *(LAS u32x4*)(sm + CV_U + gm * 16) = (u32x4){0u, 0u, 0u, 0u};
        wait_flags<16, FL_LN_S>(fl + (b & 7) * FL_REP + FL_LN + 16 * (w - 4) * FL_LN_S, tag, lane);

        f32x4 r0, r1, n0, n1;
        asm volatile("global_load_dwordx4 %0, %4, %5 sc1\n\tglobal_load_dwordx4 %1, %4, %5 offset:16 sc1\n\tglobal_load_dwordx4 %2, %4, %6 sc1\n\tglobal_load_dwordx4 %3, %4, %6 offset:16 sc1\n\ts_waitcnt vmcnt(0)"
                     : "=&v"(r0), "=&v"(r1), "=&v"(n0), "=&v"(n1) : "v"(gm * 32), "s"(ws + OFF_S1), "s"(ws + OFF_S1 + T * 4) : "memory");
#pragma unroll
        for (int ch = 0; ch < 4; ++ch) {
            u32x4 u;
#pragma unroll
            for (int k = 0; k < 4; ++k) {
                const f32x2 rs = k < 2 ? (f32x2){r0[2 * k], r0[2 * k + 1]} : (f32x2){r1[2 * k - 4], r1[2 * k - 3]}, nm = k < 2 ? (f32x2){n0[2 * k], n0[2 * k + 1]} : (f32x2){n1[2 * k - 4], n1[2 * k - 3]};
                const f32x2 a = __builtin_elementwise_fma((f32x2){bflo(xc[ch][k]), bfhi(xc[ch][k])}, rs, nm);
                u[k] = pk_bf16(a[0], a[1]);
            }
            *(LAS u32x4*)(sm + CV_U + 2048 + ln * 128 + 16 * ((2 * ch + (gm & 1)) ^ sw)) = u;
        }
    }
    bar();

    if (w < 4) {
        const int n = lane & 15, h = lane >> 4;
        const lptr fb = sm + cv_k(0) + w * KG_WAVE + ((n & 1) ? 0 : 4160) + 4 * (((2031 - n) >> 1) + 4 * h) - 512 * 7;
        f32x4 acc[8] = {};
        bf16x8 Fa[2][8], Ga[2][8];
        lptr gp, fp[4];
        auto addr = [&](int rr) {
            const int ln = n + (h >> 1) + 15 - 2 * rr;
            gp = sm + CV_U + 128 * ln + 16 * ((2 * w + (h & 1)) ^ (((ln >> 1) & 3) << 1));
#pragma unroll
            for (int k = 0; k < 4; ++k) { fp[k] = fb - 64 * rr + 1024 * k; asm volatile("" : "+v"(fp[k])); }
        };
        auto ldG = [&](int Q, int c) { Ga[Q][c] = *(LAS bf16x8*)(gp + 2048 * c); };
        u32x4 ftmp[2];
        auto ldF = [&](int Q, int a, int half) {
            lptr q = fp[(7 - a) >> 1] + 512 * ((7 - a) & 1) + 8 * half;
            ftmp[a & 1][2 * half] = *(LAS unsigned*)q; ftmp[a & 1][2 * half + 1] = *(LAS unsigned*)(q + 4);
            if (half) Fa[Q][a] = __builtin_bit_cast(bf16x8, ftmp[a & 1]);
        };
        const int so = n * 128 + 16 * ((2 * w + (h >> 1)) ^ (((n >> 1) & 3) << 1)) + 8 * (h & 1);
        u32x2 uv[8], xv[8];
        addr(0);
#pragma unroll
        for (int c = 0; c < 8; ++c) { ldF(0, c, 0); ldF(0, c, 1); ldG(0, c); }
        SB();
#pragma unroll
        for (int rr = 0; rr < 8; ++rr) {
            const int Q = rr & 1;
            int i = 0;
#pragma unroll
            for (int a = 0; a < 8; ++a)
#pragma unroll
                for (int c = a; c < 8; ++c) {
                    acc[c] = __builtin_amdgcn_mfma_f32_16x16x32_bf16(Fa[Q][a], Ga[Q][c - a], acc[c], 0, 0, 0); PIN(acc[c]); SB();
                    if (rr < 7) {
                        if (i == 0) { addr(rr + 1); SB(); }
                        else if (i <= 24) { const int k = (i - 1) / 3, m = (i - 1) % 3; if (m == 0) ldG(Q ^ 1, k); else ldF(Q ^ 1, k, m - 1); SB(); }
                    } else if (i < 16) {
                        if (i & 1) xv[i >> 1] = *(LAS u32x2*)(sm + CV_X + so + 2048 * (i >> 1)); else uv[i >> 1] = *(LAS u32x2*)(sm + CV_U + 2048 + so + 2048 * (i >> 1));
                        SB();
                    }
                    ++i;
                }
        }

        const int d = d0 + w;
        lptr yb = sm + cv_k(0) + w * KG_WAVE;
        const float dsk = (d & 1) ? bfhi(dsk_raw) : bflo(dsk_raw);
        const f32x2 dk2 = {dsk, dsk};
#pragma unroll
        for (int c2 = 0; c2 < 8; c2 += 2) {
            f32x2 hf[4];
#pragma unroll
            for (int i = 0; i < 4; ++i) { const unsigned e = uv[c2 + (i >> 1)][i & 1]; hf[i] = __builtin_elementwise_fma((f32x2){bflo(e), bfhi(e)}, dk2, (f32x2){acc[c2 + (i >> 1)][2 * (i & 1)], acc[c2 + (i >> 1)][2 * (i & 1) + 1]}); }
#pragma unroll
            for (int i = 0; i < 4; ++i) { const unsigned e = xv[c2 + (i >> 1)][i & 1]; hf[i] += (f32x2){bflo(e), bfhi(e)}; }
#pragma unroll
            for (int j = 0; j < 2; ++j) *(LAS u32x2*)(yb + (256 * (c2 + j) + 16 * n + 4 * h) * 2) = (u32x2){pk_bf16(hf[2 * j][0], hf[2 * j][1]), pk_bf16(hf[2 * j + 1][0], hf[2 * j + 1][1])};
        }
        asm volatile("" ::: "memory");
#pragma unroll
        for (int j = 0; j < 4; ++j) st_dev_o(ws + OFF_HT + (size_t)d * (T * 2) + j * 1024, lane * 16, *(LAS u32x4*)(yb + (64 * j + lane) * 16));

        wait_vm<0>();
    } else if (w == 6) wait_flags<96, FL_W_S>(fl + (b & 7) * FL_REP + FL_WG, tag, lane);
    bar();
    if (tid < 8) __hip_atomic_fetch_add(fl + FL_HC + (tid * 32 + (b & 31)) * 32, 1u, __ATOMIC_RELAXED, __HIP_MEMORY_SCOPE_AGENT);

    return tag;
}

__device__ __forceinline__ unsigned spare_wg(const Params& P, int j, int cb, lptr sm) {
    const int tid = threadIdx.x, lane = tid & 63, w = uni(tid >> 6);
    unsigned* fl = (unsigned*)(P.ws + OFF_FL);
    unsigned tag;
    auto publish = [&](int word) { wait_vm<0>(); bar(); if (tid < 8) set_flag_rep(fl + word, tag, tid); };
    if (j < 64) {
        u32x4 vx[5], vw[5];
        ln1_load(j, P.x, vx);
        tag = epoch_tag(P.ws);
        ln1_unit(j, vx, P.ws, tag);
        if (j < 32) {
            wgtile_load(64 + j, P.wg, P.wu, vw);
            asm volatile("" ::: "memory");
            wait_vm<5>(); bar();
            if (tid < 8) set_flag_rep(fl + FL_LN + j * FL_LN_S, tag, tid);

            wgtile_unit(64 + j, vw, P.ws, sm);
            publish(FL_WG + (64 + j) * FL_W_S);
        } else if (j < 48) {
            wdtile_load(j, P.wd, vw);
            asm volatile("" ::: "memory");
            wait_vm<5>(); bar();
            if (tid < 8) set_flag_rep(fl + FL_LN + j * FL_LN_S, tag, tid);

            wdtile_unit(j, vw, P.ws, sm);
            publish(FL_WD + j * FL_W_S);
        } else {
            publish(FL_LN + j * FL_LN_S);
        }
    } else {
        u32x4 va[5], vb[5], vc[5];
        { const unsigned long long t0 = wall_clock64(); while (wall_clock64() - t0 < 100) __builtin_amdgcn_s_sleep(2); }
        wgtile_load(2 * (j - 64), P.wg, P.wu, va);
        wgtile_load(2 * (j - 64) + 1, P.wg, P.wu, vb);
        wdtile_load(j - 64, P.wd, vc);
        tag = epoch_tag(P.ws);

        wgtile_unit(2 * (j - 64), va, P.ws, sm);
        publish(FL_WG + 2 * (j - 64) * FL_W_S);
        wgtile_unit(2 * (j - 64) + 1, vb, P.ws, sm);
        publish(FL_WG + (2 * (j - 64) + 1) * FL_W_S);
        wdtile_unit(j - 64, vc, P.ws, sm);
        publish(FL_WD + (j - 64) * FL_W_S);
    }

    if (w == 6) wait_flags<96, FL_W_S>(fl + (blockIdx.x & 7) * FL_REP + FL_WG, tag, lane);
    bar();

    return tag;
}

template <int WC>
struct G1Compute {
    f32x16 acc[3][2];
    bf16x8 Af[2][3], Bf[2][2];
    template <int P> __device__ __forceinline__ void mm(int ft, int tt) {
        acc[ft][tt] = __builtin_amdgcn_mfma_f32_32x32x16_bf16(Af[P][ft], Bf[P][tt], acc[ft][tt], 0, 0, 0); PIN(acc[ft][tt]); SB();
    }
    __device__ __forceinline__ static bf16x8 rdB(lptr p) { return __builtin_bit_cast(bf16x8, tr_read2(p, 8 * 256)); }
    template <int P, int KK, bool MORE, bool BAR>
    __device__ __forceinline__ void substep(lptr pa, const lptr (&pb)[2]) {
        mm<P>(0, 0);
        if (BAR) { sbar(); SB(); }
        if (MORE) { Bf[P ^ 1][0] = rdB(pb[0] + KK * 16 * 256); Af[P ^ 1][0] = *(LAS bf16x8*)(pa + KK * 6144); SB(); }
        mm<P>(0, 1);
        if (MORE) { Bf[P ^ 1][1] = rdB(pb[1] + KK * 16 * 256); Af[P ^ 1][1] = *(LAS bf16x8*)(pa + KK * 6144 + 1024); SB(); }
        mm<P>(1, 0);
        if (MORE) { Af[P ^ 1][2] = *(LAS bf16x8*)(pa + KK * 6144 + 2048); SB(); }
        mm<P>(1, 1);
        mm<P>(2, 0);
        mm<P>(2, 1);
    }
};

template <int WC>
__device__ __forceinline__ void g1_compute(lptr sm, char* __restrict__ ws, int wr, int lane, int t0, int cb) {
    const int g = lane >> 4, q = (lane & 15) >> 2, p = lane & 3, hh = g >> 1, krow = 4 * hh + q;
    G1Compute<WC> C;
#pragma unroll
    for (int ft = 0; ft < 3; ++ft)
#pragma unroll
        for (int tt = 0; tt < 2; ++tt) C.acc[ft][tt] = (f32x16){};
    const lptr a0 = sm + G1_W + 3 * WC * 1024 + lane * 16;
    lptr b0[2];
#pragma unroll
    for (int tt = 0; tt < 2; ++tt) b0[tt] = sm + krow * 256 + (((2 * wr + tt) ^ q) * 64) + (g & 1) * 32 + p * 8;
    sbar();

#pragma unroll
    for (int tt = 0; tt < 2; ++tt) C.Bf[0][tt] = C.rdB(b0[tt]);
#pragma unroll
    for (int ft = 0; ft < 3; ++ft) C.Af[0][ft] = *(LAS bf16x8*)(a0 + ft * 1024);
    SB();
    lptr pa = a0, pb[2] = {b0[0], b0[1]};
    int nb = 0;
#pragma unroll 1
    for (int s = 0; s < 19; ++s) {
        C.template substep<0, 1, true, false>(pa, pb);
        nb = nb + G1_SS == G1_NS * G1_SS ? 0 : nb + G1_SS;
        pa = a0 + nb; pb[0] = b0[0] + nb; pb[1] = b0[1] + nb;
        SB();
        C.template substep<1, 0, true, true>(pa, pb);
    }
    C.template substep<0, 1, true, false>(pa, pb);
    C.template substep<1, 0, false, false>(pa, pb);

    bar();
    char* mF = ws + OFF_MF;
    f32x2 nm2[2], k1[2], k2[2], s2[2];
#pragma unroll
    for (int tt = 0; tt < 2; ++tt) {
        const f32x2 sq = *(LAS f32x2*)(sm + G1_ST + (32 * (2 * wr + tt) + (lane & 31)) * 8);
        const float rs = __builtin_amdgcn_rcpf(sq[1]);
        nm2[tt] = (f32x2){-sq[0], -sq[0]};
        k1[tt] = (f32x2){-2.302208198f * rs, -2.302208198f * rs};
        k2[tt] = (f32x2){-0.1029432397f * rs * rs * rs, -0.1029432397f * rs * rs * rs};
        s2[tt] = (f32x2){sq[1] * sq[1], sq[1] * sq[1]};
    }
    const unsigned mo = (unsigned)((((t0 + 64 * wr + (lane & 31)) >> 4) * 48 * 64 + ((lane & 15) + 16 * hh)) * 16);
#pragma unroll
    for (int ft = 0; ft < 3; ++ft) {
        const int fl = 16 * (3 * WC + ft) + 4 * hh;
        lptr cp = sm + G1_CS + 3072 + fl * 4;
        const f32x4 cg0 = *(LAS f32x4*)cp, cg1 = *(LAS f32x4*)(cp + 32), cu0 = *(LAS f32x4*)(cp + 384), cu1 = *(LAS f32x4*)(cp + 416);
        const int ftg = 6 * cb + 3 * WC + ft;
        const char* mb = mF + (size_t)((ftg >> 1) * 64 + 32 * (ftg & 1)) * 16;
#pragma unroll
        for (int tt = 0; tt < 2; ++tt) {
            f32x2 G[4], U[4], E[4];
#pragma unroll
            for (int r = 0; r < 4; ++r) {
                const f32x2 cg = r < 2 ? (f32x2){cg0[2 * r], cg0[2 * r + 1]} : (f32x2){cg1[2 * r - 4], cg1[2 * r - 3]};
                G[r] = __builtin_elementwise_fma(nm2[tt], cg, (f32x2){C.acc[ft][tt][2 * r], C.acc[ft][tt][2 * r + 1]}); PIN(G[r]);
            }
            SB();
#pragma unroll
            for (int r = 0; r < 4; ++r) { E[r] = G[r] * G[r]; PIN(E[r]); }
            SB();
#pragma unroll
            for (int r = 0; r < 4; ++r) { E[r] = __builtin_elementwise_fma(E[r], k2[tt], k1[tt]); PIN(E[r]); }
            SB();
#pragma unroll
            for (int r = 0; r < 4; ++r) { E[r] = G[r] * E[r]; PIN(E[r]); }
            SB();
#pragma unroll
            for (int r = 0; r < 4; ++r) {
                const f32x2 cu = r < 2 ? (f32x2){cu0[2 * r], cu0[2 * r + 1]} : (f32x2){cu1[2 * r - 4], cu1[2 * r - 3]};
                E[r] = (f32x2){__builtin_amdgcn_exp2f(E[r][0]), __builtin_amdgcn_exp2f(E[r][1])}; PIN(E[r]); SB();
                U[r] = __builtin_elementwise_fma(nm2[tt], cu, (f32x2){C.acc[ft][tt][8 + 2 * r], C.acc[ft][tt][9 + 2 * r]}); PIN(U[r]); SB();
            }
#pragma unroll
            for (int r = 0; r < 4; ++r) { E[r] = __builtin_elementwise_fma(E[r], s2[tt], s2[tt]); PIN(E[r]); }
            SB();
#pragma unroll
            for (int r = 0; r < 4; ++r) {
                E[r] = (f32x2){__builtin_amdgcn_rcpf(E[r][0]), __builtin_amdgcn_rcpf(E[r][1])}; PIN(E[r]); SB();
                G[r] = G[r] * U[r]; PIN(G[r]); SB();
            }
#pragma unroll
            for (int r = 0; r < 4; ++r) { G[r] = G[r] * E[r]; PIN(G[r]); }
            SB();
            u32x4 o;
#pragma unroll
            for (int r = 0; r < 4; ++r) o[r] = pk_bf16(G[r][0], G[r][1]);
            st_dev_o(mb, mo + tt * (2 * 48 * 1024), o);
            SB();
        }
    }
}

__device__ __forceinline__ void g1_loader(char* __restrict__ ws, lptr sm, int lw, int lane, int t0, int cb, int t2, int db, unsigned tag) {
    const unsigned sm0 = (unsigned)(size_t)sm;
    {
        const char* hT = ws + OFF_HT;
        const char* wsrc = ws + OFF_WG + (size_t)cb * 40 * 6144 + 3 * lw * 1024;
        unsigned vh[2];
#pragma unroll
        for (int j = 0; j < 2; ++j) {
            const int o = (2 * lw + j) * 1024 + lane * 16, k = o >> 8, c = ((o >> 4) & 15) ^ ((k & 3) << 2);
            vh[j] = (unsigned)(k * (T * 2) + t0 * 2 + c * 16);
        }
        const unsigned vl = lane * 16;
        unsigned long long* st = (unsigned long long*)(ws + OFF_ST);
        auto stage = [&](int s) {
            const unsigned lb = sm0 + (s % G1_NS) * G1_SS;
            const char* hs = hT + (size_t)s * 32 * (T * 2);
            const char* wsx = wsrc + (size_t)s * 12288;
            dma16s(hs, vh[0], lb + (2 * lw) * 1024);
            if (s >= 4) dma16s(wsx, vl, lb + G1_W + (3 * lw) * 1024);
            dma16s(hs, vh[1], lb + (2 * lw + 1) * 1024);
            if (s >= 4) { dma16s(wsx + 1024, vl, lb + G1_W + (3 * lw + 1) * 1024); dma16s(wsx + 2048, vl, lb + G1_W + (3 * lw + 2) * 1024); }
        };
        u32x4 gv[3];
        unsigned long long g0, g1;
        float cs[8], sv[16];
#pragma unroll
        for (int s = 0; s < 4; ++s) stage(s);
        {
            const char* hp = hT + (size_t)(160 * lw) * (T * 2) + (t0 + 8 * cb) * 2;
            const unsigned go = (unsigned)lane * (T * 2);
            asm volatile("global_load_dwordx4 %0, %1, %2 sc1" : "=v"(gv[0]) : "v"(go), "s"(hp) : "memory");
            asm volatile("global_load_dwordx4 %0, %1, %2 sc1" : "=v"(gv[1]) : "v"(go), "s"(hp + (size_t)64 * (T * 2)) : "memory");
            asm volatile("global_load_dwordx4 %0, %1, %2 sc1" : "=v"(gv[2]) : "v"(lane < 32 ? go : 0u), "s"(hp + (size_t)128 * (T * 2)) : "memory");
        }
        wait_vm<9>();
        sbar();
#pragma unroll
        for (int s = 0; s < 19; ++s) {
            const int last = (s + 3) < 19 ? (s + 3) : 19;
            if (s == 3) asm volatile("s_waitcnt vmcnt(10)" : "+v"(gv[0]), "+v"(gv[1]), "+v"(gv[2]) :: "memory");
            else if (s == 17) asm volatile("s_waitcnt vmcnt(13)" : "+v"(g0), "+v"(g1) :: "memory");
            else if (s == 18) wait_vm<8>();
            else wait_vm_rt<30>(s == 0 ? 7 : (s == 1 ? 10 : (s == 2 ? 13 : 5 * (last - (s + 1)) + (s == 15 || s == 16 ? 2 : 0))));
            sbar();
            if (s + 4 < 20) stage(s + 4);
            if (s == 11 && lw < 3) {
                lptr cp = sm + G1_CS + lw * 256 + lane * 4;
                *(LAS float*)(cp + 3072) = (*(LAS float*)cp + *(LAS float*)(cp + 768)) + (*(LAS float*)(cp + 1536) + *(LAS float*)(cp + 2304));
                asm volatile("s_waitcnt lgkmcnt(0)" ::: "memory");
            }
            if (s == 16) {
#pragma unroll
                for (int j = 0; j < 3; ++j) {
                    const int i = lw + 4 * j < 10 ? lw + 4 * j : lw;
                    const int o = i * 1024 + lane * 16, row = o >> 7, c = ((o >> 4) & 7) ^ (((row >> 1) & 3) << 1);
                    dma16s(ws + OFF_HT, (unsigned)((80 * db + row) * (T * 2) + t2 * 2 + c * 16), sm0 + G2_H + i * 1024);
                }
            }
            if (s == 16 || s == 18) {
                const int s2 = s == 16 ? 0 : 1;
                const char* wd = ws + OFF_WD + (size_t)(4 * s2 + lw) * (40 * 1024) + (size_t)(5 * db) * 1024;
#pragma unroll
                for (int j = 0; j < 5; ++j) dma16s(wd + j * 1024, vl, sm0 + s2 * G2_SS + G2_W + (5 * lw + j) * 1024);
            }
            if (s == 14) {
                asm volatile("global_load_dwordx2 %0, %1, %2 sc1" : "=v"(g0) : "v"(lane * 8), "s"(st + t0) : "memory");
                asm volatile("global_load_dwordx2 %0, %1, %2 sc1" : "=v"(g1) : "v"(lane * 8), "s"(st + t0 + 64) : "memory");
            }
            if (s >= 3 && s <= 6) {
#pragma unroll
                for (int e = 2 * (s - 3); e < 2 * (s - 3) + 2; ++e) {
                    const unsigned c32 = (unsigned)__builtin_amdgcn_readlane((int)gv[0][e >> 1], 0);
                    cs[e] = (e & 1) ? bfhi(c32) : bflo(c32);
                    const float d0 = ((e & 1) ? bfhi(gv[0][e >> 1]) : bflo(gv[0][e >> 1])) - cs[e], d1 = ((e & 1) ? bfhi(gv[1][e >> 1]) : bflo(gv[1][e >> 1])) - cs[e];
                    const float d2 = lane < 32 ? ((e & 1) ? bfhi(gv[2][e >> 1]) : bflo(gv[2][e >> 1])) - cs[e] : 0.f;
                    sv[e] = d0 + d1 + d2; sv[8 + e] = d0 * d0 + d1 * d1 + d2 * d2;
                    PIN(sv[e]); PIN(sv[8 + e]);
                }
            }
            if (s == 7) {
#pragma unroll
                for (int j = 0; j < 8; ++j) {
                    const auto r = __builtin_amdgcn_permlane32_swap(__float_as_uint(sv[j]), __float_as_uint(sv[j + 8]), false, false);
                    sv[j] = __uint_as_float(r[0]) + __uint_as_float(r[1]); PIN(sv[j]);
                }
            }
            if (s == 8) {
#pragma unroll
                for (int k = 0; k < 4; ++k) {
                    const auto r = __builtin_amdgcn_permlane16_swap(__float_as_uint(sv[k]), __float_as_uint(sv[k + 4]), false, false);
                    sv[k] = __uint_as_float(r[0]) + __uint_as_float(r[1]);
                }
#pragma unroll
                for (int k = 0; k < 4; ++k) { sv[k] = row16_sum(sv[k]); PIN(sv[k]); }
            }
            if (s == 9) {
                if ((lane & 15) == 0) *(LAS f32x4*)(sm + G1_SP + lw * 96 + (lane >> 4) * 16) = (f32x4){sv[0], sv[1], sv[2], sv[3]};
                if (lane == 0) { *(LAS f32x4*)(sm + G1_SP + lw * 96 + 64) = (f32x4){cs[0], cs[1], cs[2], cs[3]}; *(LAS f32x4*)(sm + G1_SP + lw * 96 + 80) = (f32x4){cs[4], cs[5], cs[6], cs[7]}; }
                asm volatile("s_waitcnt lgkmcnt(0)" ::: "memory");
            }
            if (s == 10 && lw == 3 && lane < 8) {
                float pm[4], m2 = 0.f, mean = 0.f;
#pragma unroll
                for (int i = 0; i < 4; ++i) {
                    const float a = *(LAS float*)(sm + G1_SP + i * 96 + lane * 4) * (1.0f / 160.0f), q = *(LAS float*)(sm + G1_SP + i * 96 + 32 + lane * 4);
                    pm[i] = *(LAS float*)(sm + G1_SP + i * 96 + 64 + lane * 4) + a;
                    m2 += q - 160.0f * a * a; mean += 0.25f * pm[i];
                }
#pragma unroll
                for (int i = 0; i < 4; ++i) m2 += 160.0f * (pm[i] - mean) * (pm[i] - mean);
                const float var = fmaxf(m2 * (1.0f / (float)D), 0.f);
                const unsigned mb = pk_bf16(mean, rbf(sqrtf(rbf(rbf(var) + 1e-5f))));
                __hip_atomic_store(st + t0 + 8 * cb + lane, ((unsigned long long)tag << 32) | mb, __ATOMIC_RELAXED, __HIP_MEMORY_SCOPE_AGENT);
            }
        }
        if (lw == 0) {
            if (!__all((unsigned)(g0 >> 32) == tag && (unsigned)(g1 >> 32) == tag)) {
                unsigned long long h0, h1;
                unsigned spins = 0;
                do {
                    __builtin_amdgcn_s_sleep(2);
                    h0 = __hip_atomic_load(st + t0 + lane, __ATOMIC_RELAXED, __HIP_MEMORY_SCOPE_AGENT);
                    h1 = __hip_atomic_load(st + t0 + 64 + lane, __ATOMIC_RELAXED, __HIP_MEMORY_SCOPE_AGENT);
                } while (!__all((unsigned)(h0 >> 32) == tag && (unsigned)(h1 >> 32) == tag) && ++spins < (1u << 18));
                g0 = h0; g1 = h1;
            }
            *(LAS f32x2*)(sm + G1_ST + lane * 8) = (f32x2){bflo((unsigned)g0), bfhi((unsigned)g0)};
            *(LAS f32x2*)(sm + G1_ST + (64 + lane) * 8) = (f32x2){bflo((unsigned)g1), bfhi((unsigned)g1)};
        }
        bar();
    }
}

template <int ND>
__device__ __forceinline__ void g2_compute(lptr sm, int lane, int th, int dl0, int t0, int d0, bf16_t* __restrict__ out) {
    f32x4 acc[ND][2] = {};
    bf16x8 A[2][ND], B[2][2];
    const lptr pb0 = sm + lane * 16 + 2 * th * 4096, pa0 = sm + lane * 16 + G2_W + dl0 * 1024;
    auto ld = [&](int P, lptr pa, lptr pb, int fl) {
#pragma unroll
        for (int tt = 0; tt < 2; ++tt) B[P][tt] = *(LAS bf16x8*)(pb + (tt * 4 + fl) * 1024);
#pragma unroll
        for (int dt = 0; dt < ND; ++dt) A[P][dt] = *(LAS bf16x8*)(pa + (fl * 5 + dt) * 1024);
        SB();
    };
    auto mm = [&](int P) {
#pragma unroll
        for (int dt = 0; dt < ND; ++dt)
#pragma unroll
            for (int tt = 0; tt < 2; ++tt) { acc[dt][tt] = __builtin_amdgcn_mfma_f32_16x16x32_bf16(A[P][dt], B[P][tt], acc[dt][tt], 0, 0, 0); PIN(acc[dt][tt]); SB(); }
    };
    sbar();

    ld(0, pa0, pb0, 0);
    lptr pa = pa0, pb = pb0;
    int nb = 0;
#pragma unroll 1
    for (int s = 0; s < 12; ++s) {
        ld(1, pa, pb, 1); mm(0);
        ld(0, pa, pb, 2); mm(1);
        ld(1, pa, pb, 3); mm(0);
        if (s < 11) {
            nb = nb + G2_SS == G2_NS * G2_SS ? 0 : nb + G2_SS;
            pa = pa0 + nb; pb = pb0 + nb;
            bar();
            ld(0, pa, pb, 0);
        }
        mm(1);
    }

    bar();
    const int g = lane >> 4, q = (lane & 15) >> 2, p = lane & 3;
#pragma unroll
    for (int dt = 0; dt < ND; ++dt)
#pragma unroll
        for (int tt = 0; tt < 2; ++tt) {
            const int row = 16 * (dl0 + dt) + 4 * g + q, seg = (2 * th + tt) ^ ((row >> 1) & 3);
            const u32x2 hv = tr_read(sm + G2_H + row * 128 + seg * 32 + p * 8);
            const unsigned o01 = pk_bf16(acc[dt][tt][0], acc[dt][tt][1]), o23 = pk_bf16(acc[dt][tt][2], acc[dt][tt][3]);
            const u32x2 r = {pk_bf16(bflo(hv[0]) + bflo(o01), bfhi(hv[0]) + bfhi(o01)), pk_bf16(bflo(hv[1]) + bflo(o23), bfhi(hv[1]) + bfhi(o23))};
            *(LAS u32x2*)(sm + (16 * (2 * th + tt) + (lane & 15)) * 176 + (16 * (dl0 + dt) + 4 * g) * 2) = r;
        }
}

extern "C" __global__ void s4d_geglu_block_end();
extern "C" __global__ __launch_bounds__(512, 2) void s4d_geglu_block(Params P) {
    extern __shared__ __attribute__((aligned(16))) char smem[];
    lptr sm = (lptr)smem;

    if (uni(threadIdx.x >> 6) == 7) {
        unsigned long long pc; unsigned off, len;
        asm volatile("s_getpc_b64 %0\n.Lpc_anchor_%=:\n\ts_mov_b32 %1, .Lpc_anchor_%=-s4d_geglu_block\n\ts_mov_b32 %2, s4d_geglu_block_end-s4d_geglu_block" : "=s"(pc), "=s"(off), "=s"(len));
        const unsigned o = (blockIdx.x >> 3) * 2048 + (threadIdx.x & 63) * 32;
        if (o < len) dma4((const void*)(pc - off), o, (unsigned)(size_t)sm + LDS_BYTES - 256);
    }
    const int tid = threadIdx.x, lane = tid & 63, w = uni(tid >> 6), b = blockIdx.x;
    const unsigned sm0 = (unsigned)(size_t)sm;
    char* ws = P.ws;
    unsigned* fl = (unsigned*)(ws + OFF_FL);
    unsigned tag;
    const int grp = b & 7, idx = b >> 3;
    const int rb = (grp & 1) * 8 + (idx & 7), cb = (grp >> 1) * 4 + (idx >> 3);
    const int tb = (grp >> 1) * 8 + (idx & 7), db = (grp & 1) * 4 + (idx >> 3);

    if (b < 160) tag = conv_wg(P, b, cb, sm); else tag = spare_wg(P, b - 160, cb, sm);

    if (w >= 4) g1_prefetch_w(ws, sm, cb, w & 3, lane);
    if (w == 4) wait_flags<32, 32>(fl + FL_HC + grp * 32 * 32, (tag << 3) + 5u, lane);
    if (w == 7) wait_flags<48, FL_W_S>(fl + grp * FL_REP + FL_WD, tag, lane);
    bar();

    if (w >= 4) g1_loader(ws, sm, w & 3, lane, 128 * rb, cb, 64 * tb, db, tag);
    else if (w & 1) g1_compute<1>(sm, ws, w >> 1, lane, 128 * rb, cb); else g1_compute<0>(sm, ws, w >> 1, lane, 128 * rb, cb);

    const int t0 = 64 * tb, d0 = 80 * db;
    if (w >= 4) {
        const int lw = w & 3;
        const char* ms = ws + OFF_MF + (size_t)(4 * tb + lw) * 48 * 1024;
        const char* wsrc = ws + OFF_WD + (size_t)lw * (40 * 1024) + (size_t)(5 * db) * 1024;
        const unsigned vl = lane * 16;
        auto stage_w = [&](int s) {
#pragma unroll
            for (int j = 0; j < 5; ++j) dma16s(wsrc + (size_t)(4 * s) * (40 * 1024) + j * 1024, vl, sm0 + (s % G2_NS) * G2_SS + G2_W + (5 * lw + j) * 1024);
        };
        auto stage_m = [&](int s) {
#pragma unroll
            for (int j = 0; j < 4; ++j) dma16s(ms + (size_t)(4 * s + j) * 1024, vl, sm0 + (s % G2_NS) * G2_SS + (4 * lw + j) * 1024);
        };
        stage_w(2);
        sbar();
        if (lw == 0) wait_flags<16, FL_M_S>(fl + FL_M + 16 * (tb >> 1) * FL_M_S, tag, lane);
        sbar();

        stage_m(0); stage_m(1); stage_m(2);
        wait_vm<8>();
        sbar();
#pragma unroll
        for (int s = 0; s < 11; ++s) {
            if (s == 0) wait_vm<4>(); else if (s <= 9) wait_vm<9>(); else wait_vm<0>();
            sbar();
            if (s + 3 < 12) { stage_m(s + 3); stage_w(s + 3); }
        }
        sbar();
    } else {

        wait_vm<0>();
        sbar();
        if (tid == 0) set_flag(fl + FL_M + (16 * rb + cb) * FL_M_S, tag);

        sbar();
        const int th = w & 1;
        if (w >> 1) g2_compute<2>(sm, lane, th, 3, t0, d0, P.out); else g2_compute<3>(sm, lane, th, 0, t0, d0, P.out);
    }
    bar();
    for (int ch = tid; ch < 640; ch += 512) {
        const int row = ch / 10, c = ch % 10;
        *(u32x4*)(P.out + (size_t)(t0 + row) * D + d0 + c * 8) = *(LAS u32x4*)(sm + row * 176 + c * 16);
    }
    if (b == 0 && tid == 0) fl[FL_E] = tag;
}

extern "C" __global__ void s4d_geglu_block_end() {}

extern "C" void kernel_launch(void* const* d_in, const int* in_sizes, int n_in, void* d_out, int out_size, void* d_ws, size_t ws_size, hipStream_t stream) {
    Params P;
    P.x = (const bf16_t*)d_in[0]; P.logA = (const bf16_t*)d_in[1]; P.Aim = (const bf16_t*)d_in[2]; P.Cre = (const bf16_t*)d_in[3]; P.Cim = (const bf16_t*)d_in[4];
    P.logdt = (const bf16_t*)d_in[5]; P.Dskip = (const bf16_t*)d_in[6]; P.wg = (const bf16_t*)d_in[8]; P.wu = (const bf16_t*)d_in[9]; P.wd = (const bf16_t*)d_in[10];
    P.ws = (char*)d_ws; P.out = (bf16_t*)d_out;
    (void)hipFuncSetAttribute((const void*)s4d_geglu_block, hipFuncAttributeMaxDynamicSharedMemorySize, LDS_BYTES);
    s4d_geglu_block<<<256, 512, LDS_BYTES, stream>>>(P);
}
```
